```python
import torch

def _softmax(x, axis=-1):
    return torch.softmax(x, dim=axis)
def _gelu(x):
    return torch.nn.functional.gelu(x, approximate="tanh")
def _silu(x):
    return torch.nn.functional.silu(x)
def _sigmoid(x):
    return torch.sigmoid(x)
def _layernorm(x, axis=-1, eps=1e-5):
    m = x.mean(dim=axis, keepdim=True)
    v = x.var(dim=axis, keepdim=True, unbiased=False)
    return (x - m) / torch.sqrt(v + eps)
def _scatter_add(tgt, idx, src, axis=0):
    return tgt.clone().index_add_(axis, idx, src)
def _segment_sum(src, seg, n):
    out = torch.zeros((n,) + src.shape[1:], dtype=src.dtype)
    return out.index_add_(0, seg, src)
def _pad(x, pw):
    flat = tuple(v for pair in reversed(pw) for v in pair)
    return torch.nn.functional.pad(x, flat)

kernel_name = 'sequential_preLN__grouped_kv_mqa__dense_activation_squared_relu__prompt'

def setup_inputs():

    _w = (lambda a: (lambda t: t.to(torch.bfloat16) if t.is_floating_point() else t)(torch.as_tensor(a)))
    def _A_setup():
        B, S, D, H = 1, 2048, 576, 32
        Dh = D // H
        return {
            "x": _w((lambda *s: torch.randn(*s, dtype=torch.bfloat16) * 0.05)(B, S, D)),
            "wq": _w((lambda *s: torch.randn(*s, dtype=torch.bfloat16) * 0.05)(D, H * Dh)),
            "wk": _w((lambda *s: torch.randn(*s, dtype=torch.bfloat16) * 0.05)(D, Dh)),
            "wv": _w((lambda *s: torch.randn(*s, dtype=torch.bfloat16) * 0.05)(D, Dh)),
            "wo": _w((lambda *s: torch.randn(*s, dtype=torch.bfloat16) * 0.05)(H * Dh, D)),
        }
    _A_in = _A_setup()

    def _B_setup():
        B, S, D, F = 1, 2048, 576, 4608
        return {
            "x": _w((lambda *s: torch.randn(*s, dtype=torch.bfloat16) * 0.05)(B, S, D)),
            "w1": _w((lambda *s: torch.randn(*s, dtype=torch.bfloat16) * 0.05)(D, F)),
            "b1": _w((lambda *s: torch.randn(*s, dtype=torch.bfloat16) * 0.4)(F)),
            "w2": _w((lambda *s: torch.randn(*s, dtype=torch.bfloat16) * 0.05)(F, D)),
            "b2": _w((lambda *s: torch.randn(*s, dtype=torch.bfloat16) * 3.2)(D)),
        }
    _B_in = _B_setup()

    return {
        "A_x": _A_in["x"],
        "A_wq": _A_in["wq"],
        "A_wk": _A_in["wk"],
        "A_wv": _A_in["wv"],
        "A_wo": _A_in["wo"],
        "B_x": _B_in["x"],
        "B_w1": _B_in["w1"],
        "B_b1": _B_in["b1"],
        "B_w2": _B_in["w2"],
        "B_b2": _B_in["b2"],
    }

def reference(A_x, A_wq, A_wk, A_wv, A_wo, B_x, B_w1, B_b1, B_w2, B_b2):
    def _A_ref(x, wq, wk, wv, wo):
        B, S, D = x.shape
        H = 32
        Dh = D // H
        q = (lambda x, *d: x.permute(*d))((x @ wq).reshape(B, S, H, Dh), 0, 2, 1, 3)
        k = x @ wk
        v = x @ wv
        scores = torch.einsum("bhqd,bkd->bhqk", q, k) * (Dh ** -0.5)
        p = _softmax(scores, axis=-1)
        o = torch.einsum("bhqk,bkd->bhqd", p, v)
        return (lambda x, *d: x.permute(*d))(o, 0, 2, 1, 3).reshape(B, S, H * Dh) @ wo

    def _B_ref(x, w1, b1, w2, b2):
        h = torch.einsum("bsd,df->bsf", x, w1) + b1
        zero = torch.zeros_like(h)
        r = torch.where(h > zero, h, zero)
        h = r * r
        out = torch.einsum("bsf,fd->bsd", h, w2) + b2
        return out

    x = A_x
    h = x + _A_ref(_layernorm(x, axis=-1), A_wq, A_wk, A_wv, A_wo)
    h2 = h + _B_ref(_layernorm(h, axis=-1), B_w1, B_b1, B_w2, B_b2)
    return h2
```

```cpp
#include <hip/hip_runtime.h>
#include <hip/hip_bf16.h>
typedef unsigned short u16;
__device__ __forceinline__ float bf2f(u16 b){ return __builtin_bit_cast(float, (unsigned)b<<16); }
__device__ __forceinline__ u16 f2bf(float f){ __hip_bfloat16 h = __float2bfloat16(f); return *(u16*)&h; }
constexpr int S=2048,D=576,H=32,DH=18,F=4608;
__global__ void ln_k(const u16* x, u16* y){
  int r=blockIdx.x; int t=threadIdx.x;
  float v[9]; float s=0;
  for(int i=0;i<9;i++){ v[i]=bf2f(x[r*D+t+64*i]); s+=v[i]; }
  for(int o=32;o;o>>=1) s+=__shfl_xor(s,o);
  float m=s/D; float q=0;
  for(int i=0;i<9;i++){ float d=v[i]-m; q+=d*d; }
  for(int o=32;o;o>>=1) q+=__shfl_xor(q,o);
  float rs=rsqrtf(q/D+1e-5f);
  for(int i=0;i<9;i++) y[r*D+t+64*i]=f2bf((v[i]-m)*rs);
}
__global__ void gemm_k(const u16* A,const u16* B,u16* C,const u16* bias,const u16* res,int N,int K,int mode){
  int n=blockIdx.x*64+threadIdx.x; int m=blockIdx.y;
  if(n>=N) return;
  float acc=0;
  for(int k=0;k<K;k++) acc+=bf2f(A[m*K+k])*bf2f(B[(size_t)k*N+n]);
  if(mode==1) acc=bf2f(f2bf(acc))+bf2f(res[m*N+n]);
  if(mode==2){ acc=bf2f(f2bf(acc))+bf2f(bias[n]); acc=bf2f(f2bf(acc)); acc=acc>0?acc*acc:0; }
  if(mode==3){ acc=bf2f(f2bf(acc))+bf2f(bias[n]); acc=bf2f(f2bf(acc))+bf2f(res[m*N+n]); }
  C[(size_t)m*N+n]=f2bf(acc);
}
__global__ void attn_k(const u16* q,const u16* k,const u16* v,u16* o){
  int idx=blockIdx.x*64+threadIdx.x; int h=idx%H; int r=idx/H;
  float qq[DH]; for(int d=0;d<DH;d++) qq[d]=bf2f(q[r*D+h*DH+d]);
  float m=-1e30f,l=0; float acc[DH]; for(int d=0;d<DH;d++) acc[d]=0;
  for(int j=0;j<S;j++){
    float s=0; for(int d=0;d<DH;d++) s+=qq[d]*bf2f(k[j*DH+d]);
    s*=0.23570226f;
    float mn=fmaxf(m,s); float a=__expf(m-mn); float p=__expf(s-mn);
    l=l*a+p; for(int d=0;d<DH;d++) acc[d]=acc[d]*a+p*bf2f(v[j*DH+d]);
    m=mn;
  }
  for(int d=0;d<DH;d++) o[r*D+h*DH+d]=f2bf(acc[d]/l);
}
extern "C" void kernel_launch(void* const* d_in, const int* in_sizes, int n_in, void* d_out, int out_size, void* d_ws, size_t ws_size, hipStream_t stream){
  const u16* x=(const u16*)d_in[0]; const u16* wq=(const u16*)d_in[1]; const u16* wk=(const u16*)d_in[2]; const u16* wv=(const u16*)d_in[3]; const u16* wo=(const u16*)d_in[4];
  const u16* w1=(const u16*)d_in[6]; const u16* b1=(const u16*)d_in[7]; const u16* w2=(const u16*)d_in[8]; const u16* b2=(const u16*)d_in[9];
  u16* ws=(u16*)d_ws;
  u16* xn=ws; u16* q=xn+S*D; u16* k=q+S*D; u16* v=k+S*32; u16* o=v+S*32; u16* h=o+S*D; u16* hn=h+S*D; u16* h1=hn+S*D;
  ln_k<<<S,64,0,stream>>>(x,xn);
  gemm_k<<<dim3(9,S),64,0,stream>>>(xn,wq,q,0,0,D,D,0);
  gemm_k<<<dim3(1,S),64,0,stream>>>(xn,wk,k,0,0,DH,D,0);
  gemm_k<<<dim3(1,S),64,0,stream>>>(xn,wv,v,0,0,DH,D,0);
  attn_k<<<S*H/64,64,0,stream>>>(q,k,v,o);
  gemm_k<<<dim3(9,S),64,0,stream>>>(o,wo,h,0,x,D,D,1);
  ln_k<<<S,64,0,stream>>>(h,hn);
  gemm_k<<<dim3(72,S),64,0,stream>>>(hn,w1,h1,b1,0,F,D,2);
  gemm_k<<<dim3(9,S),64,0,stream>>>(h1,w2,(u16*)d_out,b2,h,D,F,3);
}
```

```cpp
#include <hip/hip_runtime.h>
constexpr unsigned WS_KV = 65536;
constexpr unsigned CHUNK = 18944;
constexpr unsigned WS_O = 229376;
constexpr unsigned WS_H = 2588672;
constexpr unsigned WS_ST = 4947968;
constexpr unsigned WS_WT = 5079040;
constexpr unsigned WS_PART = 15695872;
constexpr unsigned ROUNDS[] = {5, 2, 2, 3, 3, 1, 1, 1};

struct WaveTab { unsigned v[1024][48]; };
constexpr unsigned quad_off(unsigned Q) {
  const unsigned j = Q / 18, r = Q % 18;
  return r < 16 ? (2 * j + (r >> 3)) * 4096 + (r & 3) * 256 + ((r >> 2) & 1) * 8 : (16 + (j >> 2)) * 4096 + (j & 3) * 256 + (r - 16) * 8;
}
constexpr WaveTab make_tab() {
  WaveTab T{};
  for (unsigned b = 0; b < 256; ++b) for (unsigned w = 0; w < 4; ++w) {
    unsigned* e = T.v[4 * b + w];
    const unsigned x8 = b & 7, q5 = b >> 3, tb = 32 * x8 + q5, iA = tb >> 3, jA = tb & 7;
    const unsigned jF = 2 * (x8 & 3) + (q5 & 1), iF = 16 * (x8 >> 2) + (q5 >> 1), bF = 8 * iF + jF;
    for (unsigned s = 0; s < 3; ++s) {
      unsigned* p = e + 5 * s; const unsigned n = 1024 * s + 4 * b + w;
      if (n >= 2592) continue;
      const unsigned blk = n / 162, r = n % 162, ka = r / 9, U = r % 9, j = blk & 7; const bool m2 = blk >= 8, tail = !m2 && ka >= 16;
      const unsigned stride = m2 ? 1152 : 9216;
      const unsigned row = m2 ? 576 * j + 32 * ka : (tail ? 288 * (ka - 16) + 64 : 72 * (ka >> 1) + 32 * (ka & 1));
      const unsigned t = m2 ? (U == 8 ? 9 : 10 + ((U + 15 - j) & 7)) : U;
      unsigned t0 = 0, nt = 0; for (unsigned q = 0; t0 + (nt = ROUNDS[q]) <= t; ++q) t0 += nt;
      const unsigned pos = 18 * t0 + nt * ka + (t - t0);
      p[0] = stride; p[1] = stride * (tail ? 4 : 16); p[2] = stride * (tail ? 72 : 4);
      p[3] = row * stride + 128 * U + (m2 ? 0 : 1152 * j); p[4] = WS_WT + (4 * j * 324 + pos) * 1024;
    }
    const unsigned T7 = 2 * (iA & 3) + (w >> 1), kvb = WS_KV + (iA >> 2) * CHUNK;
    e[15] = jA < 2 ? kvb + T7 * 1024 + jA * 512 + (w & 1) * 256 : jA == 2 ? kvb + 8192 + (T7 >> 1) * 256 + (w & 1) * 128 + (T7 & 1) * 4 : kvb + 9216 + (2 * T7 + (w & 1)) * 608;
    e[16] = WS_O + (4 * b + w) * 4736;
    e[17] = WS_PART + (4 * bF + w) * 2048;
    e[18] = WS_O + iA * 73728 + w * 1024;
    e[19] = WS_WT + (4 * jF + w) * 324 * 1024;
    e[20] = WS_H + iA * 73728 + 2 * jA * 4096 + w * 1024 + 2048;
    e[21] = WS_H + iA * 73728 + (16 + (jA >> 2)) * 4096 + w * 1024 + (jA & 3) * 256;
    e[22] = WS_ST + iA * 4096 + w * 1024 + jA * 128;
    e[23] = WS_H + iF * 73728;
    e[24] = WS_ST + iF * 4096 + w * 1024;
    e[25] = bF * 9216 + 32 * w + 1024;
    e[26] = WS_PART + iF * 65536 + w * 2048;
    e[27] = (jF >> 2) * 1024 + (jF & 1) * 128 + (jF & 2) * 4;
    e[28] = iF * 73728 + jF * 128 + w * 18432;
    e[29] = jF; e[30] = (iF & 15) * 1024;
    for (unsigned q = 0; q < 4; ++q) { e[32 + q] = quad_off(4 * (4 * jF + w) + q); e[36 + q] = quad_off(4 * (32 + w) + q) + (jF >> 1) * 1024 + (jF & 1) * 128; }
  }
  return T;
}
__device__ const WaveTab wave_tab = make_tab();

__global__ void __launch_bounds__(256, 1) __attribute__((aligned(4096))) fused_block(const void* x, const void* wq, const void* wk, const void* wv, const void* wo, const void* w1, const void* b1, const void* w2, const void* b2, void* out, void* ws) {
  __shared__ __attribute__((aligned(16))) char lds[163840];
  const void* tab = &wave_tab;
  asm volatile(
R"(s_mov_b32 s36, %1
s_load_dwordx16 s[12:27], %2, 0x0
s_load_dwordx4 s[28:31], %2, 0x40
s_load_dwordx2 s[50:51], %2, 0x50
; pull this kernel's own code into L2: blocks b, b+8, .. share it out, one dword per 128-B line, 20 lines per block
s_getpc_b64 s[48:49]
LPC_%=:
s_lshr_b32 s42, s36, 3
v_lshlrev_b32 v2, 12, v0
s_lshl_b32 s43, s42, 7
v_add_u32 v2, s43, v2
v_cmp_gt_u32 vcc, LEND_%=-LPC_%=-4, v2
s_nop 0
s_and_saveexec_b64 s[46:47], vcc
s_nop 4
s_cbranch_execz Lct_1_%=
global_load_dword v156, v2, s[48:49]
Lct_1_%=:
s_mov_b64 exec, -1
v_lshrrev_b32 v1, 6, v0
s_and_b32 s33, s36, 7
s_lshl_b32 s33, s33, 5
s_lshr_b32 s42, s36, 3
v_readfirstlane_b32 s39, v1
s_add_u32 s33, s33, s42
s_lshr_b32 s37, s33, 3
s_and_b32 s38, s33, 7
v_and_b32 v1, 63, v0
; ===== phase A: LayerNorm 1 + q/k/v projection of 64 tokens x (72 q columns + 8 k/v columns)
v_lshrrev_b32 v160, 3, v1
v_and_b32 v161, 7, v1
v_xor_b32 v161, v161, v160
v_mul_u32_u24 v162, 1152, v160
v_lshl_add_u32 v176, v161, 4, v162
v_mul_u32_u24 v177, 36, v1
s_mul_i32 s63, s37, 4
s_add_u32 s63, s63, s39
s_mul_i32 s63, s63, 18432
s_mul_i32 s43, s39, 18432
s_cmp_ge_u32 s38, 3
s_cselect_b32 s46, 3, 0
s_cmp_ge_u32 s38, 6
s_cselect_b32 s46, 6, s46
s_sub_u32 s60, s38, s46
s_lshl_b32 s60, s60, 4
s_min_u32 s60, s60, 20
s_mul_i32 s62, s39, 2304
s_add_u32 s60, s60, s62
s_lshl_b32 s47, s39, 10
s_add_u32 s47, s47, 73728
s_lshl_b32 s62, s36, 2
s_add_u32 s62, s62, s39
s_mul_i32 s62, s62, 192
s_add_u32 s34, s34, s62
s_addc_u32 s35, s35, 0
s_mov_b32 s54, 2359296
s_mov_b32 s55, 131072
s_mov_b32 m0, s43
s_waitcnt lgkmcnt(0)
s_mov_b32 s52, s12
s_and_b32 s53, s13, 0xffff
buffer_load_dwordx4 v176, s[52:55], s63 offen lds
s_add_u32 m0, m0, 1024
s_add_u32 s63, s63, 9216
buffer_load_dwordx4 v176, s[52:55], s63 offen lds
s_add_u32 m0, m0, 1024
s_sub_u32 s63, s63, 9088
buffer_load_dwordx4 v176, s[52:55], s63 offen lds
s_add_u32 m0, m0, 1024
s_add_u32 s63, s63, 9216
buffer_load_dwordx4 v176, s[52:55], s63 offen lds
s_add_u32 m0, m0, 1024
s_sub_u32 s63, s63, 9088
s_load_dwordx16 s[64:79], s[34:35], 0x0
s_load_dwordx16 s[80:95], s[34:35], 0x40
s_load_dword s96, s[34:35], 0x80
s_load_dword s97, s[34:35], 0xbc
; address-translation warm-up: touch every buffer (results unused)
s_and_b32 s8, s33, 31
s_lshl_b32 s8, s8, 2
s_add_u32 s8, s8, s39
s_mul_i32 s9, s8, 5184
s_load_dword s10, s[14:15], s9
s_load_dword s10, s[20:21], s9
s_lshl_b32 s9, s9, 3
s_load_dword s10, s[22:23], s9
s_load_dword s10, s[26:27], s9
s_mul_i32 s9, s8, 253696
s_load_dword s10, s[50:51], s9
s_mul_i32 s9, s8, 18432
s_load_dword s10, s[30:31], s9
s_load_dword s10, s[24:25], 0x0
s_load_dword s10, s[28:29], 0x0
s_mov_b32 s42, m0
s_cmp_eq_u32 s46, 3
s_cselect_b64 s[48:49], s[18:19], s[16:17]
s_mov_b32 s4, s48
s_and_b32 s5, s49, 0xffff
s_mov_b32 s6, 20736
s_mov_b32 s7, 131072
s_mov_b32 m0, s47
s_nop 0
buffer_load_dwordx4 v177, s[4:7], s60 offen lds
s_add_u32 s47, s47, 4096
s_add_u32 s60, s60, 9216
s_mov_b32 m0, s47
s_nop 0
buffer_load_dwordx4 v177, s[4:7], s60 offen lds
s_add_u32 s47, s47, 4096
s_add_u32 s60, s60, 9216
s_mov_b32 m0, s47
s_cmp_eq_u32 s39, 0
s_cbranch_scc0 Lc_2_%=
buffer_load_dwordx4 v177, s[4:7], s60 offen lds
Lc_2_%=:
s_mov_b32 m0, s42
s_nop 0
buffer_load_dwordx4 v176, s[52:55], s63 offen lds
s_add_u32 m0, m0, 1024
s_add_u32 s63, s63, 9216
buffer_load_dwordx4 v176, s[52:55], s63 offen lds
s_add_u32 m0, m0, 1024
s_sub_u32 s63, s63, 9088
buffer_load_dwordx4 v176, s[52:55], s63 offen lds
s_add_u32 m0, m0, 1024
s_add_u32 s63, s63, 9216
buffer_load_dwordx4 v176, s[52:55], s63 offen lds
s_add_u32 m0, m0, 1024
s_sub_u32 s63, s63, 9088
buffer_load_dwordx4 v176, s[52:55], s63 offen lds
s_add_u32 m0, m0, 1024
s_add_u32 s63, s63, 9216
buffer_load_dwordx4 v176, s[52:55], s63 offen lds
s_add_u32 m0, m0, 1024
s_sub_u32 s63, s63, 9088
; lane constants (the first loads are in flight)
v_lshrrev_b32 v2, 4, v1
v_and_b32 v3, 15, v1
s_mov_b32 s56, s14
s_and_b32 s57, s15, 0xffff
s_mov_b32 s58, 663552
s_mov_b32 s59, 131072
v_add_u32 v160, 0, v1
v_mul_u32_u24 v161, 171, v160
v_lshrrev_b32 v161, 11, v161
v_lshrrev_b32 v162, 3, v161
v_mul_u32_u24 v162, 9216, v162
v_mul_u32_u24 v161, 960, v161
v_lshl_add_u32 v161, v160, 4, v161
v_add_u32 v214, v161, v162
v_add_u32 v160, 64, v1
v_mul_u32_u24 v161, 171, v160
v_lshrrev_b32 v161, 11, v161
v_lshrrev_b32 v162, 3, v161
v_mul_u32_u24 v162, 9216, v162
v_mul_u32_u24 v161, 960, v161
v_lshl_add_u32 v161, v160, 4, v161
v_add_u32 v215, v161, v162
v_subrev_u32 v215, 1024, v215
v_add_u32 v160, 128, v1
v_mul_u32_u24 v161, 171, v160
v_lshrrev_b32 v161, 11, v161
v_lshrrev_b32 v162, 3, v161
v_mul_u32_u24 v162, 9216, v162
v_mul_u32_u24 v161, 960, v161
v_lshl_add_u32 v161, v160, 4, v161
v_add_u32 v216, v161, v162
v_subrev_u32 v216, 2048, v216
s_mul_i32 s61, s38, 144
s_and_b32 s62, s61, 63
s_sub_u32 s61, s61, s62
s_lshr_b32 s48, s39, 1
s_mul_i32 s48, s48, 36864
s_add_u32 s61, s61, s48
s_and_b32 s48, s39, 1
s_mul_i32 s48, s48, 9216
s_add_u32 s61, s61, s48
s_mul_i32 s60, s39, 3104
s_add_u32 s60, s60, 52096
s_add_u32 s62, s62, 52096
v_bfe_u32 v160, v1, 2, 2
v_and_b32 v161, 3, v1
v_and_b32 v162, 1, v2
v_lshrrev_b32 v163, 1, v2
v_and_b32 v164, 7, v1
v_lshl_add_u32 v166, v163, 3, v160
v_mul_u32_u24 v166, 192, v166
v_mul_u32_u24 v167, 3104, v162
v_add3_u32 v166, v166, v167, s62
v_lshl_add_u32 v217, v161, 3, v166
v_add_u32 v218, 55872, v217
v_and_b32 v167, 1, v161
v_lshl_add_u32 v219, v167, 3, v166
v_add_u32 v220, 55872, v219
v_lshl_add_u32 v166, v2, 3, v160
v_lshlrev_b32 v166, 4, v166
v_lshl_add_u32 v166, v167, 3, v166
v_add_u32 v178, 73728, v166
v_xor_b32 v166, v164, v2
v_lshlrev_b32 v167, 7, v3
v_add_u32 v167, s43, v167
v_lshl_add_u32 v179, v166, 4, v167
v_xor_b32 v180, 64, v179
v_mov_b32 v172, 0x3f803f80
v_mov_b32 v173, 0x3f803f80
v_mov_b32 v174, 0x3f803f80
v_mov_b32 v175, 0x3f803f80
v_lshl_add_u32 v181, v160, 4, v3
v_lshlrev_b32 v181, 2, v181
v_bfe_u32 v188, v1, 3, 2
v_lshlrev_b32 v13, 5, v1
v_add_u32 v13, 4096, v13
v_lshlrev_b32 v182, 4, v3
v_lshl_add_u32 v182, v2, 3, v182
s_mov_b32 s44, -1
s_cmp_eq_u32 s38, 2
s_cbranch_scc0 Lc_3_%=
v_lshlrev_b32 v182, 3, v3
s_mov_b32 s44, 0xffff0000
Lc_3_%=:
s_cmp_eq_u32 s46, 3
s_cbranch_scc0 Lc_4_%=
s_mul_i32 s48, s39, 2560
s_add_u32 s48, s48, 40960
v_lshlrev_b32 v184, 4, v3
v_add_u32 v184, s48, v184
v_and_b32 v166, 1, v160
v_lshlrev_b32 v166, 4, v166
v_lshrrev_b32 v167, 3, v3
v_lshl_add_u32 v166, v167, 3, v166
v_lshl_add_u32 v166, v161, 1, v166
v_lshl_add_u32 v166, v2, 7, v166
v_add_u32 v183, s48, v166
v_bfe_u32 v166, v3, 1, 2
v_lshl_add_u32 v166, v167, 3, v166
s_sub_u32 s48, s38, 3
s_lshl_b32 s48, s48, 2
v_add_u32 v166, s48, v166
s_mov_b32 s44, 0xffff
s_cmp_eq_u32 s38, 5
s_cbranch_scc0 Lv5_5_%=
v_lshrrev_b32 v166, 1, v3
v_add_u32 v166, 10, v166
v_cmp_eq_u32 vcc, 15, v166
v_mov_b32 v167, 18
s_nop 0
v_cndmask_b32 v166, v166, v167, vcc
s_mov_b32 s44, 0xfc00
Lv5_5_%=:
v_lshlrev_b32 v166, 4, v166
v_and_b32 v167, 1, v3
v_mul_u32_u24 v167, 304, v167
v_add_u32 v182, v166, v167
Lc_4_%=:
s_mov_b32 s42, m0
buffer_load_dwordx4 v176, s[52:55], s63 offen lds
s_add_u32 m0, m0, 1024
s_add_u32 s63, s63, 9216
buffer_load_dwordx4 v176, s[52:55], s63 offen lds
s_add_u32 m0, m0, 1024
s_sub_u32 s63, s63, 9088
buffer_load_dwordx4 v176, s[52:55], s63 offen lds
s_add_u32 m0, m0, 1024
s_add_u32 s63, s63, 9216
buffer_load_dwordx4 v176, s[52:55], s63 offen lds
s_add_u32 m0, m0, 1024
s_sub_u32 s63, s63, 9088
buffer_load_dwordx4 v176, s[52:55], s63 offen lds
s_add_u32 m0, m0, 1024
s_add_u32 s63, s63, 9216
buffer_load_dwordx4 v176, s[52:55], s63 offen lds
s_add_u32 m0, m0, 1024
s_sub_u32 s63, s63, 9088
buffer_load_dwordx4 v176, s[52:55], s63 offen lds
s_add_u32 m0, m0, 1024
s_add_u32 s63, s63, 9216
buffer_load_dwordx4 v176, s[52:55], s63 offen lds
v_mov_b32 v166, 0
global_load_dwordx2 v[152:153], v166, s[50:51] sc1
s_mov_b32 s49, 9216
v_mul_u32_u24 v222, 160, v3
s_mul_i32 s48, s39, 2560
s_add_u32 s48, s48, 40960
v_add_u32 v222, s48, v222
v_lshl_add_u32 v185, v2, 3, v222
v_mul_u32_u24 v166, 36, v162
v_add_u32 v222, v222, v166
v_lshl_add_u32 v221, v163, 4, v222
v_lshlrev_b32 v186, 4, v2
v_add_u32 v186, 51200, v186
v_lshl_add_u32 v223, v2, 3, v185
v_add_u32 v223, 110592, v223
s_mul_i32 s48, s38, 9
v_add_u32 v166, s48, v2
v_lshrrev_b32 v167, 2, v166
v_lshlrev_b32 v167, 12, v167
v_and_b32 v166, 3, v166
v_lshl_add_u32 v167, v166, 8, v167
v_lshl_add_u32 v14, v3, 4, v167
v_lshlrev_b32 v187, 5, v0
v_add_u32 v187, 8192, v187
v_lshlrev_b32 v4, 4, v1
v_and_b32 v166, 31, v1
v_lshlrev_b32 v189, 4, v166
v_min_u32 v167, 18, v166
v_lshlrev_b32 v167, 4, v167
v_mul_u32_u24 v168, 304, v163
v_add3_u32 v7, v167, v168, s49
v_mov_b32 v167, 262144
s_mov_b32 vcc_lo, 0x0047ffff
s_mov_b32 vcc_hi, 0x0047ffff
v_cndmask_b32 v7, v167, v7, vcc
v_lshlrev_b32 v167, 3, v1
v_add_u32 v10, 8192, v167
v_add_u32 v5, 56832, v4
v_add_u32 v8, 56832, v7
v_add_u32 v11, 56832, v10
v_add_u32 v6, 113664, v4
v_add_u32 v9, 113664, v7
v_add_u32 v12, 113664, v10
s_lshl_b32 s48, s39, 11
s_add_u32 s48, s48, 151552
v_add_u32 v242, s48, v4
v_lshlrev_b32 v166, 10, v162
v_lshl_add_u32 v166, v160, 7, v166
v_lshl_add_u32 v166, v163, 5, v166
v_lshl_add_u32 v166, v161, 3, v166
v_add_u32 v243, s48, v166
s_mul_i32 s48, s38, 9
v_add_u32 v168, s48, v163
v_lshlrev_b32 v165, 7, v3
v_lshl_add_u32 v165, v162, 3, v165
v_add_u32 v165, s43, v165
v_add_u32 v166, 0, v168
v_lshrrev_b32 v167, 3, v166
v_lshl_add_u32 v167, v167, 11, v165
v_xor_b32 v166, v166, v164
v_and_b32 v166, 7, v166
v_lshl_add_u32 v198, v166, 4, v167
v_add_u32 v166, 2, v168
v_lshrrev_b32 v167, 3, v166
v_lshl_add_u32 v167, v167, 11, v165
v_xor_b32 v166, v166, v164
v_and_b32 v166, 7, v166
v_lshl_add_u32 v199, v166, 4, v167
v_add_u32 v166, 4, v168
s_waitcnt vmcnt(14)
s_barrier
ds_read_b64_tr_b16 a[176:177], v178
ds_read_b64_tr_b16 a[178:179], v178 offset:64
ds_read_b64_tr_b16 a[180:181], v178 offset:512
ds_read_b64_tr_b16 a[182:183], v178 offset:576
ds_read_b64_tr_b16 a[184:185], v178 offset:1024
ds_read_b64_tr_b16 a[186:187], v178 offset:1088
ds_read_b64_tr_b16 a[188:189], v178 offset:1536
ds_read_b64_tr_b16 a[190:191], v178 offset:1600
ds_read_b64_tr_b16 a[192:193], v178 offset:2048
ds_read_b64_tr_b16 a[194:195], v178 offset:2112
ds_read_b64_tr_b16 a[196:197], v178 offset:2560
ds_read_b64_tr_b16 a[198:199], v178 offset:2624
s_add_u32 m0, s60, 37248
s_add_u32 s62, s61, 221184
buffer_load_dwordx4 v214, s[56:59], s62 offen lds
buffer_load_dwordx4 v215, s[56:59], s62 offen offset:1024 lds
buffer_load_dwordx4 v216, s[56:59], s62 offen offset:2048 lds
s_waitcnt lgkmcnt(10)
v_mfma_f32_16x16x32_bf16 v[32:35], a[176:179], v[172:175], 0
ds_read_b64_tr_b16 a[200:201], v178 offset:3072
ds_read_b64_tr_b16 a[202:203], v178 offset:3136
s_waitcnt lgkmcnt(10)
v_mfma_f32_16x16x32_bf16 v[32:35], a[180:183], v[172:175], v[32:35]
ds_read_b64_tr_b16 a[204:205], v178 offset:3584
ds_read_b64_tr_b16 a[206:207], v178 offset:3648
s_waitcnt lgkmcnt(10)
v_mfma_f32_16x16x32_bf16 v[32:35], a[184:187], v[172:175], v[32:35]
ds_read_b64_tr_b16 a[208:209], v178 offset:4096
ds_read_b64_tr_b16 a[210:211], v178 offset:4160
s_waitcnt lgkmcnt(10)
v_mfma_f32_16x16x32_bf16 v[32:35], a[188:191], v[172:175], v[32:35]
ds_read_b64_tr_b16 a[212:213], v178 offset:4608
ds_read_b64_tr_b16 a[214:215], v178 offset:4672
s_waitcnt lgkmcnt(10)
v_mfma_f32_16x16x32_bf16 v[32:35], a[192:195], v[172:175], v[32:35]
ds_read_b64_tr_b16 a[216:217], v178 offset:5120
ds_read_b64_tr_b16 a[218:219], v178 offset:5184
s_waitcnt lgkmcnt(10)
v_mfma_f32_16x16x32_bf16 v[32:35], a[196:199], v[172:175], v[32:35]
ds_read_b64_tr_b16 a[220:221], v178 offset:5632
ds_read_b64_tr_b16 a[222:223], v178 offset:5696
s_waitcnt lgkmcnt(10)
v_mfma_f32_16x16x32_bf16 v[32:35], a[200:203], v[172:175], v[32:35]
ds_read_b64_tr_b16 a[224:225], v178 offset:6144
ds_read_b64_tr_b16 a[226:227], v178 offset:6208
s_waitcnt lgkmcnt(10)
v_mfma_f32_16x16x32_bf16 v[32:35], a[204:207], v[172:175], v[32:35]
ds_read_b64_tr_b16 a[228:229], v178 offset:6656
ds_read_b64_tr_b16 a[230:231], v178 offset:6720
s_waitcnt lgkmcnt(10)
v_mfma_f32_16x16x32_bf16 v[32:35], a[208:211], v[172:175], v[32:35]
ds_read_b64_tr_b16 a[232:233], v178 offset:7168
)"
R"(ds_read_b64_tr_b16 a[234:235], v178 offset:7232
s_waitcnt lgkmcnt(10)
v_mfma_f32_16x16x32_bf16 v[32:35], a[212:215], v[172:175], v[32:35]
ds_read_b64_tr_b16 a[236:237], v178 offset:7680
ds_read_b64_tr_b16 a[238:239], v178 offset:7744
s_waitcnt lgkmcnt(10)
v_mfma_f32_16x16x32_bf16 v[32:35], a[216:219], v[172:175], v[32:35]
ds_read_b64_tr_b16 a[240:241], v178 offset:8192
ds_read_b64_tr_b16 a[242:243], v178 offset:8256
s_waitcnt lgkmcnt(10)
v_mfma_f32_16x16x32_bf16 v[32:35], a[220:223], v[172:175], v[32:35]
ds_read_b64_tr_b16 a[244:245], v178 offset:8704
ds_read_b64_tr_b16 a[246:247], v178 offset:8768
s_waitcnt lgkmcnt(10)
v_mfma_f32_16x16x32_bf16 v[32:35], a[224:227], v[172:175], v[32:35]
v_lshrrev_b32 v167, 3, v166
v_lshl_add_u32 v167, v167, 11, v165
s_waitcnt lgkmcnt(8)
v_mfma_f32_16x16x32_bf16 v[32:35], a[228:231], v[172:175], v[32:35]
v_xor_b32 v166, v166, v164
v_and_b32 v166, 7, v166
s_waitcnt lgkmcnt(6)
v_mfma_f32_16x16x32_bf16 v[32:35], a[232:235], v[172:175], v[32:35]
v_lshl_add_u32 v200, v166, 4, v167
v_add_u32 v166, 6, v168
s_waitcnt lgkmcnt(4)
v_mfma_f32_16x16x32_bf16 v[32:35], a[236:239], v[172:175], v[32:35]
v_lshrrev_b32 v167, 3, v166
v_lshl_add_u32 v167, v167, 11, v165
s_waitcnt lgkmcnt(2)
v_mfma_f32_16x16x32_bf16 v[32:35], a[240:243], v[172:175], v[32:35]
v_xor_b32 v166, v166, v164
v_and_b32 v166, 7, v166
s_waitcnt lgkmcnt(0)
v_mfma_f32_16x16x32_bf16 v[32:35], a[244:247], v[172:175], v[32:35]
v_lshl_add_u32 v201, v166, 4, v167
v_add_u32 v166, 8, v168
s_add_u32 m0, s60, 49664
s_add_u32 s62, s61, 294912
buffer_load_dwordx4 v214, s[56:59], s62 offen lds
buffer_load_dwordx4 v215, s[56:59], s62 offen offset:1024 lds
buffer_load_dwordx4 v216, s[56:59], s62 offen offset:2048 lds
ds_read_b128 v[64:67], v179
ds_read_b128 v[68:71], v180
ds_read_b128 v[72:75], v179 offset:2048
ds_read_b128 v[76:79], v180 offset:2048
s_waitcnt lgkmcnt(3)
v_mfma_f32_16x16x32_bf16 v[60:63], v[64:67], v[64:67], 0
s_waitcnt vmcnt(19)
ds_read_b128 v[80:83], v179 offset:4096
ds_read_b128 v[84:87], v180 offset:4096
v_mfma_f32_16x16x32_bf16 v[56:59], v[172:175], v[64:67], 0
v_lshrrev_b32 v167, 3, v166
v_lshl_add_u32 v167, v167, 11, v165
v_mfma_f32_16x16x32_bf16 v[28:31], a[176:179], v[64:67], 0
v_xor_b32 v166, v166, v164
v_and_b32 v166, 7, v166
s_waitcnt lgkmcnt(4)
v_mfma_f32_16x16x32_bf16 v[60:63], v[68:71], v[68:71], v[60:63]
v_lshl_add_u32 v202, v166, 4, v167
s_xor_b32 s48, s39, 0
v_mfma_f32_16x16x32_bf16 v[56:59], v[172:175], v[68:71], v[56:59]
s_lshl_b32 s48, s48, 5
v_add_u32 v190, s48, v217
v_mfma_f32_16x16x32_bf16 v[28:31], a[180:183], v[68:71], v[28:31]
s_add_u32 m0, s60, 62080
s_add_u32 s62, s61, 368640
buffer_load_dwordx4 v214, s[56:59], s62 offen lds
s_waitcnt lgkmcnt(3)
v_mfma_f32_16x16x32_bf16 v[60:63], v[72:75], v[72:75], v[60:63]
s_waitcnt vmcnt(18)
ds_read_b128 v[88:91], v179 offset:6144
ds_read_b128 v[92:95], v180 offset:6144
v_mfma_f32_16x16x32_bf16 v[56:59], v[172:175], v[72:75], v[56:59]
v_add_u32 v194, s48, v218
v_add_u32 v148, s48, v185
v_mfma_f32_16x16x32_bf16 v[28:31], a[184:187], v[72:75], v[28:31]
s_lshl_b32 s48, s48, 1
v_add_u32 v144, s48, v186
s_waitcnt lgkmcnt(4)
v_mfma_f32_16x16x32_bf16 v[60:63], v[76:79], v[76:79], v[60:63]
s_xor_b32 s48, s39, 1
s_lshl_b32 s48, s48, 5
v_mfma_f32_16x16x32_bf16 v[56:59], v[172:175], v[76:79], v[56:59]
v_add_u32 v191, s48, v217
v_add_u32 v195, s48, v218
v_mfma_f32_16x16x32_bf16 v[28:31], a[188:191], v[76:79], v[28:31]
v_add_u32 v149, s48, v185
s_lshl_b32 s48, s48, 1
s_waitcnt lgkmcnt(3)
v_mfma_f32_16x16x32_bf16 v[60:63], v[80:83], v[80:83], v[60:63]
s_waitcnt vmcnt(16)
ds_read_b128 v[96:99], v179 offset:8192
ds_read_b128 v[100:103], v180 offset:8192
v_mfma_f32_16x16x32_bf16 v[56:59], v[172:175], v[80:83], v[56:59]
v_add_u32 v145, s48, v186
s_xor_b32 s48, s39, 2
v_mfma_f32_16x16x32_bf16 v[28:31], a[192:195], v[80:83], v[28:31]
buffer_load_dwordx4 v215, s[56:59], s62 offen offset:1024 lds
s_waitcnt lgkmcnt(4)
v_mfma_f32_16x16x32_bf16 v[60:63], v[84:87], v[84:87], v[60:63]
s_lshl_b32 s48, s48, 5
v_add_u32 v192, s48, v217
v_mfma_f32_16x16x32_bf16 v[56:59], v[172:175], v[84:87], v[56:59]
v_add_u32 v196, s48, v218
v_add_u32 v150, s48, v185
v_mfma_f32_16x16x32_bf16 v[28:31], a[196:199], v[84:87], v[28:31]
s_lshl_b32 s48, s48, 1
v_add_u32 v146, s48, v186
s_waitcnt lgkmcnt(3)
v_mfma_f32_16x16x32_bf16 v[60:63], v[88:91], v[88:91], v[60:63]
s_waitcnt vmcnt(15)
ds_read_b128 v[104:107], v179 offset:10240
ds_read_b128 v[108:111], v180 offset:10240
v_mfma_f32_16x16x32_bf16 v[56:59], v[172:175], v[88:91], v[56:59]
s_xor_b32 s48, s39, 3
s_lshl_b32 s48, s48, 5
v_mfma_f32_16x16x32_bf16 v[28:31], a[200:203], v[88:91], v[28:31]
v_add_u32 v193, s48, v217
v_add_u32 v197, s48, v218
s_waitcnt lgkmcnt(4)
v_mfma_f32_16x16x32_bf16 v[60:63], v[92:95], v[92:95], v[60:63]
v_add_u32 v151, s48, v185
s_lshl_b32 s48, s48, 1
v_mfma_f32_16x16x32_bf16 v[56:59], v[172:175], v[92:95], v[56:59]
v_add_u32 v147, s48, v186
v_lshlrev_b32 v168, 4, v164
v_mfma_f32_16x16x32_bf16 v[28:31], a[204:207], v[92:95], v[28:31]
buffer_load_dwordx4 v216, s[56:59], s62 offen offset:2048 lds
s_waitcnt lgkmcnt(3)
v_mfma_f32_16x16x32_bf16 v[60:63], v[96:99], v[96:99], v[60:63]
s_waitcnt vmcnt(14)
ds_read_b128 v[112:115], v179 offset:12288
ds_read_b128 v[116:119], v180 offset:12288
v_mfma_f32_16x16x32_bf16 v[56:59], v[172:175], v[96:99], v[56:59]
v_mul_u32_u24 v160, 331776, v163
v_add_u32 v160, v160, v189
v_mfma_f32_16x16x32_bf16 v[28:31], a[208:211], v[96:99], v[28:31]
v_mul_u32_u24 v165, s64, v188
v_mul_u32_u24 v162, s65, v163
s_waitcnt lgkmcnt(4)
v_mfma_f32_16x16x32_bf16 v[60:63], v[100:103], v[100:103], v[60:63]
v_add3_u32 v165, v165, v162, v168
v_add_u32 v236, s68, v160
v_mfma_f32_16x16x32_bf16 v[56:59], v[172:175], v[100:103], v[56:59]
v_add_u32 v237, 663552, v236
v_add_u32 v224, s67, v165
v_mfma_f32_16x16x32_bf16 v[28:31], a[212:215], v[100:103], v[28:31]
v_add_u32 v225, s66, v224
v_add_u32 v226, s66, v225
s_waitcnt lgkmcnt(3)
v_mfma_f32_16x16x32_bf16 v[60:63], v[104:107], v[104:107], v[60:63]
s_waitcnt vmcnt(12)
ds_read_b128 v[120:123], v179 offset:14336
ds_read_b128 v[124:127], v180 offset:14336
v_mfma_f32_16x16x32_bf16 v[56:59], v[172:175], v[104:107], v[56:59]
v_add_u32 v227, s66, v226
v_mul_u32_u24 v165, s69, v188
v_mfma_f32_16x16x32_bf16 v[28:31], a[216:219], v[104:107], v[28:31]
s_add_u32 m0, s60, 74496
s_add_u32 s62, s61, 442368
buffer_load_dwordx4 v214, s[56:59], s62 offen lds
s_waitcnt lgkmcnt(4)
v_mfma_f32_16x16x32_bf16 v[60:63], v[108:111], v[108:111], v[60:63]
v_mul_u32_u24 v162, s70, v163
v_add3_u32 v165, v165, v162, v168
v_mfma_f32_16x16x32_bf16 v[56:59], v[172:175], v[108:111], v[56:59]
v_add_u32 v238, s73, v160
v_add_u32 v239, 663552, v238
v_mfma_f32_16x16x32_bf16 v[28:31], a[220:223], v[108:111], v[28:31]
v_add_u32 v228, s72, v165
v_add_u32 v229, s71, v228
s_waitcnt lgkmcnt(3)
v_mfma_f32_16x16x32_bf16 v[60:63], v[112:115], v[112:115], v[60:63]
s_waitcnt vmcnt(11)
ds_read_b128 v[128:131], v179 offset:16384
ds_read_b128 v[132:135], v180 offset:16384
v_mfma_f32_16x16x32_bf16 v[56:59], v[172:175], v[112:115], v[56:59]
v_add_u32 v230, s71, v229
v_add_u32 v231, s71, v230
v_mfma_f32_16x16x32_bf16 v[28:31], a[224:227], v[112:115], v[28:31]
v_mul_u32_u24 v165, s74, v188
v_mul_u32_u24 v162, s75, v163
s_waitcnt lgkmcnt(4)
v_mfma_f32_16x16x32_bf16 v[60:63], v[116:119], v[116:119], v[60:63]
v_add3_u32 v165, v165, v162, v168
v_add_u32 v240, s78, v160
v_mfma_f32_16x16x32_bf16 v[56:59], v[172:175], v[116:119], v[56:59]
v_add_u32 v241, 663552, v240
v_add_u32 v232, s77, v165
v_mfma_f32_16x16x32_bf16 v[28:31], a[228:231], v[116:119], v[28:31]
buffer_load_dwordx4 v215, s[56:59], s62 offen offset:1024 lds
s_waitcnt lgkmcnt(3)
v_mfma_f32_16x16x32_bf16 v[60:63], v[120:123], v[120:123], v[60:63]
v_add_u32 v233, s76, v232
v_add_u32 v234, s76, v233
v_mfma_f32_16x16x32_bf16 v[56:59], v[172:175], v[120:123], v[56:59]
v_add_u32 v235, s76, v234
s_cmp_eq_u32 s69, 9216
s_cselect_b64 s[6:7], s[22:23], s[26:27]
v_mfma_f32_16x16x32_bf16 v[28:31], a[232:235], v[120:123], v[28:31]
ds_read_b64 v[204:205], v198
ds_read_b64 v[206:207], v199
ds_read_b64 v[208:209], v200
ds_read_b64 v[210:211], v201
ds_read_b64 v[212:213], v202
s_waitcnt lgkmcnt(7)
v_mfma_f32_16x16x32_bf16 v[60:63], v[124:127], v[124:127], v[60:63]
s_cmp_lg_u32 s74, 0
s_cselect_b32 s45, -1, 0
v_add_u32 v182, s79, v182
v_mfma_f32_16x16x32_bf16 v[56:59], v[172:175], v[124:127], v[56:59]
v_mfma_f32_16x16x32_bf16 v[28:31], a[236:239], v[124:127], v[28:31]
s_waitcnt lgkmcnt(6)
v_mfma_f32_16x16x32_bf16 v[60:63], v[128:131], v[128:131], v[60:63]
v_mfma_f32_16x16x32_bf16 v[56:59], v[172:175], v[128:131], v[56:59]
v_mfma_f32_16x16x32_bf16 v[28:31], a[240:243], v[128:131], v[28:31]
buffer_load_dwordx4 v216, s[56:59], s62 offen offset:2048 lds
s_waitcnt lgkmcnt(5)
v_mfma_f32_16x16x32_bf16 v[60:63], v[132:135], v[132:135], v[60:63]
v_mfma_f32_16x16x32_bf16 v[56:59], v[172:175], v[132:135], v[56:59]
v_mfma_f32_16x16x32_bf16 v[28:31], a[244:247], v[132:135], v[28:31]
s_waitcnt lgkmcnt(0)
s_barrier
; LayerNorm statistics (bf16-rounded like the reference), applied in the epilogue
s_nop 3
v_mov_b32 v166, v60
s_mov_b32 vcc_lo, 0x22222222
s_mov_b32 vcc_hi, 0x22222222
v_cndmask_b32 v166, v166, v61, vcc
s_mov_b32 vcc_lo, 0x44444444
s_mov_b32 vcc_hi, 0x44444444
v_cndmask_b32 v166, v166, v62, vcc
s_mov_b32 vcc_lo, 0x88888888
s_mov_b32 vcc_hi, 0x88888888
v_cndmask_b32 v166, v166, v63, vcc
s_add_u32 m0, s60, 86912
s_add_u32 s62, s61, 516096
buffer_load_dwordx4 v214, s[56:59], s62 offen lds
ds_bpermute_b32 v166, v181, v166
v_mul_f32 v169, 0x3ae38e39, v56
s_waitcnt lgkmcnt(0)
v_mul_f32 v170, 0x3ae38e39, v166
v_fma_f32 v170, -v169, v169, v170
v_max_f32 v170, 0, v170
buffer_load_dwordx4 v215, s[56:59], s62 offen offset:1024 lds
v_cvt_pk_bf16_f32 v167, v170, v169
v_lshlrev_b32 v170, 16, v167
v_and_b32 v169, 0xffff0000, v167
v_add_f32 v170, 0x3727c5ac, v170
buffer_load_dwordx4 v216, s[56:59], s62 offen offset:2048 lds
v_cvt_pk_bf16_f32 v170, v170, v170
v_lshlrev_b32 v170, 16, v170
v_sqrt_f32 v170, v170
s_nop 0
v_cvt_pk_bf16_f32 v170, v170, v170
s_add_u32 m0, s60, 99328
s_add_u32 s62, s61, 589824
buffer_load_dwordx4 v214, s[56:59], s62 offen lds
v_lshlrev_b32 v170, 16, v170
v_rcp_f32 v170, v170
v_pk_fma_f32 v[28:29], v[32:33], v[168:169], v[28:29] op_sel:[0,1,0] op_sel_hi:[1,1,1] neg_lo:[0,1,0] neg_hi:[0,1,0]
v_pk_mul_f32 v[28:29], v[28:29], v[170:171] op_sel:[0,0] op_sel_hi:[1,0]
buffer_load_dwordx4 v215, s[56:59], s62 offen offset:1024 lds
v_pk_fma_f32 v[30:31], v[34:35], v[168:169], v[30:31] op_sel:[0,1,0] op_sel_hi:[1,1,1] neg_lo:[0,1,0] neg_hi:[0,1,0]
v_pk_mul_f32 v[30:31], v[30:31], v[170:171] op_sel:[0,0] op_sel_hi:[1,0]
v_cvt_pk_bf16_f32 v32, v28, v29
v_cvt_pk_bf16_f32 v33, v30, v31
buffer_load_dwordx4 v216, s[56:59], s62 offen offset:2048 lds
s_cmp_ge_u32 s38, 6
s_cbranch_scc0 Lc_6_%=
global_load_dword v157, v4, s[50:51]
Lc_6_%=:
s_cmp_lt_u32 s38, 2
s_cbranch_scc0 Lc_7_%=
s_mov_b32 exec_lo, s44
s_mov_b32 exec_hi, 0
global_store_dwordx2 v182, v[32:33], s[50:51] sc1
Lc_7_%=:
s_cmp_eq_u32 s38, 2
s_cbranch_scc0 Lc_8_%=
s_mov_b32 exec_lo, s44
s_mov_b32 exec_hi, 0
global_store_dword v182, v33, s[50:51] sc1
Lc_8_%=:
s_cmp_eq_u32 s46, 3
s_cbranch_scc0 Lc_9_%=
ds_write_b16 v183, v32
ds_write_b16_d16_hi v183, v32 offset:32
ds_write_b16 v183, v33 offset:64
ds_write_b16_d16_hi v183, v33 offset:96
ds_read_b128 v[28:31], v184
s_cmp_eq_u32 s38, 5
s_cselect_b32 vcc_lo, 0x0c00, 0
s_mov_b32 vcc_hi, 0
s_waitcnt lgkmcnt(0)
v_cndmask_b32 v28, v28, v172, vcc
v_cndmask_b32 v29, v29, v173, vcc
v_cndmask_b32 v30, v30, v174, vcc
v_cndmask_b32 v31, v31, v175, vcc
s_mov_b32 exec_lo, s44
s_mov_b32 exec_hi, 0
global_store_dwordx4 v182, v[28:31], s[50:51] sc1
Lc_9_%=:
s_mov_b64 exec, -1
s_waitcnt vmcnt(7)
s_barrier
ds_read_b64_tr_b16 a[64:65], v190 offset:37248
ds_read_b64_tr_b16 a[66:67], v190 offset:38016
ds_read_b64_tr_b16 a[68:69], v191 offset:37248
ds_read_b64_tr_b16 a[70:71], v191 offset:38016
ds_read_b64_tr_b16 a[72:73], v192 offset:37248
ds_read_b64_tr_b16 a[74:75], v192 offset:38016
ds_read_b64_tr_b16 a[76:77], v193 offset:37248
ds_read_b64_tr_b16 a[78:79], v193 offset:38016
ds_read_b64_tr_b16 a[80:81], v219 offset:37376
ds_read_b64_tr_b16 a[82:83], v219 offset:38144
ds_read_b64_tr_b16 a[84:85], v190 offset:43456
ds_read_b64_tr_b16 a[86:87], v190 offset:44224
ds_read_b64_tr_b16 a[88:89], v191 offset:43456
ds_read_b64_tr_b16 a[90:91], v191 offset:44224
ds_read_b64_tr_b16 a[92:93], v192 offset:43456
ds_read_b64_tr_b16 a[94:95], v192 offset:44224
ds_read_b64_tr_b16 a[96:97], v193 offset:43456
ds_read_b64_tr_b16 a[98:99], v193 offset:44224
ds_read_b64_tr_b16 a[100:101], v219 offset:43584
ds_read_b64_tr_b16 a[102:103], v219 offset:44352
v_mul_f32 v171, 0x3eae1a93, v170
; launch tag: derived from the word the previous call left (or garbage); only tells this call's flags from older ones
v_readfirstlane_b32 s40, v152
v_readfirstlane_b32 s41, v153
s_mul_i32 s42, s40, 0x85ebca6b
s_mul_i32 s41, s41, 0x9e3779b1
s_add_u32 s41, s41, s42
s_add_u32 s41, s41, 0x165667b1
s_add_u32 s40, s40, 0x7f4a7c15
s_add_u32 s52, s50, 65536
s_addc_u32 s53, s51, 0
s_and_b32 s53, s53, 0xffff
s_mov_b32 s54, 151552
s_mov_b32 s55, 131072
s_lshl_b32 s63, s36, 2
s_add_u32 s63, s63, s39
s_waitcnt lgkmcnt(15)
v_mfma_f32_16x16x32_bf16 v[36:39], a[64:67], v[172:175], 0
ds_read_b64_tr_b16 a[104:105], v190 offset:49664
ds_read_b64_tr_b16 a[106:107], v190 offset:50432
v_mfma_f32_16x16x32_bf16 v[16:19], a[64:67], v[88:91], 0
ds_read_b64_tr_b16 a[108:109], v191 offset:49664
ds_read_b64_tr_b16 a[110:111], v191 offset:50432
v_mfma_f32_16x16x32_bf16 v[20:23], a[68:71], v[88:91], 0
ds_read_b64_tr_b16 a[112:113], v192 offset:49664
ds_read_b64_tr_b16 a[114:115], v192 offset:50432
s_waitcnt lgkmcnt(15)
v_mfma_f32_16x16x32_bf16 v[24:27], a[72:75], v[88:91], 0
ds_read_b64_tr_b16 a[116:117], v193 offset:49664
ds_read_b64_tr_b16 a[118:119], v193 offset:50432
v_mfma_f32_16x16x32_bf16 v[28:31], a[76:79], v[88:91], 0
ds_read_b64_tr_b16 a[120:121], v219 offset:49792
ds_read_b64_tr_b16 a[122:123], v219 offset:50560
v_mfma_f32_16x16x32_bf16 v[32:35], a[80:83], v[88:91], 0
s_add_u32 m0, s60, 0
s_add_u32 s62, s61, 0
buffer_load_dwordx4 v214, s[56:59], s62 offen lds
v_mfma_f32_16x16x32_bf16 v[52:55], a[80:83], v[172:175], 0
s_waitcnt lgkmcnt(10)
buffer_load_dwordx4 v215, s[56:59], s62 offen offset:1024 lds
v_mfma_f32_16x16x32_bf16 v[36:39], a[84:87], v[172:175], v[36:39]
ds_read_b64_tr_b16 a[124:125], v194
ds_read_b64_tr_b16 a[126:127], v194 offset:768
v_mfma_f32_16x16x32_bf16 v[16:19], a[84:87], v[92:95], v[16:19]
ds_read_b64_tr_b16 a[128:129], v195
ds_read_b64_tr_b16 a[130:131], v195 offset:768
v_mfma_f32_16x16x32_bf16 v[20:23], a[88:91], v[92:95], v[20:23]
ds_read_b64_tr_b16 a[132:133], v196
ds_read_b64_tr_b16 a[134:135], v196 offset:768
v_mfma_f32_16x16x32_bf16 v[24:27], a[92:95], v[92:95], v[24:27]
ds_read_b64_tr_b16 a[136:137], v197
ds_read_b64_tr_b16 a[138:139], v197 offset:768
v_mfma_f32_16x16x32_bf16 v[28:31], a[96:99], v[92:95], v[28:31]
ds_read_b64_tr_b16 a[140:141], v220 offset:128
ds_read_b64_tr_b16 a[142:143], v220 offset:896
v_mfma_f32_16x16x32_bf16 v[32:35], a[100:103], v[92:95], v[32:35]
buffer_load_dwordx4 v216, s[56:59], s62 offen offset:2048 lds
v_mfma_f32_16x16x32_bf16 v[52:55], a[100:103], v[172:175], v[52:55]
s_waitcnt lgkmcnt(10)
s_add_u32 m0, s60, 12416
s_add_u32 s62, s61, 73728
buffer_load_dwordx4 v214, s[56:59], s62 offen lds
v_mfma_f32_16x16x32_bf16 v[36:39], a[104:107], v[172:175], v[36:39]
ds_read_b64_tr_b16 a[64:65], v194 offset:6208
ds_read_b64_tr_b16 a[66:67], v194 offset:6976
v_mfma_f32_16x16x32_bf16 v[16:19], a[104:107], v[96:99], v[16:19]
ds_read_b64_tr_b16 a[68:69], v195 offset:6208
ds_read_b64_tr_b16 a[70:71], v195 offset:6976
v_mfma_f32_16x16x32_bf16 v[20:23], a[108:111], v[96:99], v[20:23]
ds_read_b64_tr_b16 a[72:73], v196 offset:6208
ds_read_b64_tr_b16 a[74:75], v196 offset:6976
v_mfma_f32_16x16x32_bf16 v[24:27], a[112:115], v[96:99], v[24:27]
ds_read_b64_tr_b16 a[76:77], v197 offset:6208
)"
R"(ds_read_b64_tr_b16 a[78:79], v197 offset:6976
v_mfma_f32_16x16x32_bf16 v[28:31], a[116:119], v[96:99], v[28:31]
ds_read_b64_tr_b16 a[80:81], v220 offset:6336
ds_read_b64_tr_b16 a[82:83], v220 offset:7104
v_mfma_f32_16x16x32_bf16 v[32:35], a[120:123], v[96:99], v[32:35]
buffer_load_dwordx4 v215, s[56:59], s62 offen offset:1024 lds
v_mfma_f32_16x16x32_bf16 v[52:55], a[120:123], v[172:175], v[52:55]
s_waitcnt lgkmcnt(10)
buffer_load_dwordx4 v216, s[56:59], s62 offen offset:2048 lds
v_mfma_f32_16x16x32_bf16 v[36:39], a[124:127], v[172:175], v[36:39]
ds_read_b64_tr_b16 a[84:85], v194 offset:12416
ds_read_b64_tr_b16 a[86:87], v194 offset:13184
v_mfma_f32_16x16x32_bf16 v[16:19], a[124:127], v[100:103], v[16:19]
ds_read_b64_tr_b16 a[88:89], v195 offset:12416
ds_read_b64_tr_b16 a[90:91], v195 offset:13184
v_mfma_f32_16x16x32_bf16 v[20:23], a[128:131], v[100:103], v[20:23]
ds_read_b64_tr_b16 a[92:93], v196 offset:12416
ds_read_b64_tr_b16 a[94:95], v196 offset:13184
v_mfma_f32_16x16x32_bf16 v[24:27], a[132:135], v[100:103], v[24:27]
ds_read_b64_tr_b16 a[96:97], v197 offset:12416
ds_read_b64_tr_b16 a[98:99], v197 offset:13184
v_mfma_f32_16x16x32_bf16 v[28:31], a[136:139], v[100:103], v[28:31]
ds_read_b64_tr_b16 a[100:101], v220 offset:12544
ds_read_b64_tr_b16 a[102:103], v220 offset:13312
v_mfma_f32_16x16x32_bf16 v[32:35], a[140:143], v[100:103], v[32:35]
s_add_u32 m0, s60, 24832
s_add_u32 s62, s61, 147456
buffer_load_dwordx4 v214, s[56:59], s62 offen lds
v_mfma_f32_16x16x32_bf16 v[52:55], a[140:143], v[172:175], v[52:55]
s_waitcnt lgkmcnt(10)
buffer_load_dwordx4 v215, s[56:59], s62 offen offset:1024 lds
v_mfma_f32_16x16x32_bf16 v[36:39], a[64:67], v[172:175], v[36:39]
ds_read_b64_tr_b16 a[104:105], v194 offset:18624
ds_read_b64_tr_b16 a[106:107], v194 offset:19392
v_mfma_f32_16x16x32_bf16 v[16:19], a[64:67], v[104:107], v[16:19]
ds_read_b64_tr_b16 a[108:109], v195 offset:18624
ds_read_b64_tr_b16 a[110:111], v195 offset:19392
v_mfma_f32_16x16x32_bf16 v[20:23], a[68:71], v[104:107], v[20:23]
ds_read_b64_tr_b16 a[112:113], v196 offset:18624
ds_read_b64_tr_b16 a[114:115], v196 offset:19392
v_mfma_f32_16x16x32_bf16 v[24:27], a[72:75], v[104:107], v[24:27]
ds_read_b64_tr_b16 a[116:117], v197 offset:18624
ds_read_b64_tr_b16 a[118:119], v197 offset:19392
v_mfma_f32_16x16x32_bf16 v[28:31], a[76:79], v[104:107], v[28:31]
ds_read_b64_tr_b16 a[120:121], v220 offset:18752
ds_read_b64_tr_b16 a[122:123], v220 offset:19520
v_mfma_f32_16x16x32_bf16 v[32:35], a[80:83], v[104:107], v[32:35]
buffer_load_dwordx4 v216, s[56:59], s62 offen offset:2048 lds
v_mfma_f32_16x16x32_bf16 v[52:55], a[80:83], v[172:175], v[52:55]
s_waitcnt lgkmcnt(10)
v_mov_b32 v153, 0
v_mov_b32 v154, 0
v_mfma_f32_16x16x32_bf16 v[36:39], a[84:87], v[172:175], v[36:39]
ds_read_b64_tr_b16 a[124:125], v194 offset:24832
ds_read_b64_tr_b16 a[126:127], v194 offset:25600
v_mfma_f32_16x16x32_bf16 v[16:19], a[84:87], v[108:111], v[16:19]
ds_read_b64_tr_b16 a[128:129], v195 offset:24832
ds_read_b64_tr_b16 a[130:131], v195 offset:25600
v_mfma_f32_16x16x32_bf16 v[20:23], a[88:91], v[108:111], v[20:23]
ds_read_b64_tr_b16 a[132:133], v196 offset:24832
ds_read_b64_tr_b16 a[134:135], v196 offset:25600
v_mfma_f32_16x16x32_bf16 v[24:27], a[92:95], v[108:111], v[24:27]
ds_read_b64_tr_b16 a[136:137], v197 offset:24832
ds_read_b64_tr_b16 a[138:139], v197 offset:25600
v_mfma_f32_16x16x32_bf16 v[28:31], a[96:99], v[108:111], v[28:31]
ds_read_b64_tr_b16 a[140:141], v220 offset:24960
ds_read_b64_tr_b16 a[142:143], v220 offset:25728
v_mfma_f32_16x16x32_bf16 v[32:35], a[100:103], v[108:111], v[32:35]
v_mov_b32 v158, 0
v_mfma_f32_16x16x32_bf16 v[52:55], a[100:103], v[172:175], v[52:55]
s_waitcnt lgkmcnt(10)
s_waitcnt vmcnt(10)
s_barrier
v_mfma_f32_16x16x32_bf16 v[36:39], a[104:107], v[172:175], v[36:39]
ds_read_b64_tr_b16 a[64:65], v194 offset:31040
ds_read_b64_tr_b16 a[66:67], v194 offset:31808
v_mfma_f32_16x16x32_bf16 v[16:19], a[104:107], v[112:115], v[16:19]
ds_read_b64_tr_b16 a[68:69], v195 offset:31040
ds_read_b64_tr_b16 a[70:71], v195 offset:31808
v_mfma_f32_16x16x32_bf16 v[20:23], a[108:111], v[112:115], v[20:23]
ds_read_b64_tr_b16 a[72:73], v196 offset:31040
ds_read_b64_tr_b16 a[74:75], v196 offset:31808
v_mfma_f32_16x16x32_bf16 v[24:27], a[112:115], v[112:115], v[24:27]
ds_read_b64_tr_b16 a[76:77], v197 offset:31040
ds_read_b64_tr_b16 a[78:79], v197 offset:31808
v_mfma_f32_16x16x32_bf16 v[28:31], a[116:119], v[112:115], v[28:31]
ds_read_b64_tr_b16 a[80:81], v220 offset:31168
ds_read_b64_tr_b16 a[82:83], v220 offset:31936
v_mfma_f32_16x16x32_bf16 v[32:35], a[120:123], v[112:115], v[32:35]
v_mfma_f32_16x16x32_bf16 v[52:55], a[120:123], v[172:175], v[52:55]
s_waitcnt lgkmcnt(10)
v_mfma_f32_16x16x32_bf16 v[36:39], a[124:127], v[172:175], v[36:39]
ds_read_b64_tr_b16 a[84:85], v194 offset:37248
ds_read_b64_tr_b16 a[86:87], v194 offset:38016
v_mfma_f32_16x16x32_bf16 v[16:19], a[124:127], v[116:119], v[16:19]
ds_read_b64_tr_b16 a[88:89], v195 offset:37248
ds_read_b64_tr_b16 a[90:91], v195 offset:38016
v_mfma_f32_16x16x32_bf16 v[20:23], a[128:131], v[116:119], v[20:23]
ds_read_b64_tr_b16 a[92:93], v196 offset:37248
ds_read_b64_tr_b16 a[94:95], v196 offset:38016
v_mfma_f32_16x16x32_bf16 v[24:27], a[132:135], v[116:119], v[24:27]
ds_read_b64_tr_b16 a[96:97], v197 offset:37248
ds_read_b64_tr_b16 a[98:99], v197 offset:38016
v_mfma_f32_16x16x32_bf16 v[28:31], a[136:139], v[116:119], v[28:31]
ds_read_b64_tr_b16 a[100:101], v220 offset:37376
ds_read_b64_tr_b16 a[102:103], v220 offset:38144
v_mfma_f32_16x16x32_bf16 v[32:35], a[140:143], v[116:119], v[32:35]
v_mfma_f32_16x16x32_bf16 v[52:55], a[140:143], v[172:175], v[52:55]
s_waitcnt lgkmcnt(10)
s_waitcnt vmcnt(9)
s_lshl_b32 s96, s63, 3
s_add_u32 s96, s96, 8192
v_mov_b32 v248, s40
v_mov_b32 v249, s41
v_mov_b32 v167, s96
s_mov_b32 exec_lo, 1
s_mov_b32 exec_hi, 0
global_store_dwordx2 v167, v[248:249], s[50:51] sc1
s_mov_b64 exec, -1
v_mfma_f32_16x16x32_bf16 v[36:39], a[64:67], v[172:175], v[36:39]
ds_read_b64_tr_b16 a[104:105], v194 offset:43456
ds_read_b64_tr_b16 a[106:107], v194 offset:44224
v_mfma_f32_16x16x32_bf16 v[16:19], a[64:67], v[120:123], v[16:19]
ds_read_b64_tr_b16 a[108:109], v195 offset:43456
ds_read_b64_tr_b16 a[110:111], v195 offset:44224
v_mfma_f32_16x16x32_bf16 v[20:23], a[68:71], v[120:123], v[20:23]
ds_read_b64_tr_b16 a[112:113], v196 offset:43456
ds_read_b64_tr_b16 a[114:115], v196 offset:44224
v_mfma_f32_16x16x32_bf16 v[24:27], a[72:75], v[120:123], v[24:27]
ds_read_b64_tr_b16 a[116:117], v197 offset:43456
ds_read_b64_tr_b16 a[118:119], v197 offset:44224
v_mfma_f32_16x16x32_bf16 v[28:31], a[76:79], v[120:123], v[28:31]
ds_read_b64_tr_b16 a[120:121], v220 offset:43584
ds_read_b64_tr_b16 a[122:123], v220 offset:44352
v_mfma_f32_16x16x32_bf16 v[32:35], a[80:83], v[120:123], v[32:35]
v_mfma_f32_16x16x32_bf16 v[52:55], a[80:83], v[172:175], v[52:55]
s_waitcnt lgkmcnt(10)
v_mfma_f32_16x16x32_bf16 v[36:39], a[84:87], v[172:175], v[36:39]
ds_read_b64_tr_b16 a[124:125], v194 offset:49664
ds_read_b64_tr_b16 a[126:127], v194 offset:50432
v_mfma_f32_16x16x32_bf16 v[16:19], a[84:87], v[124:127], v[16:19]
ds_read_b64_tr_b16 a[128:129], v195 offset:49664
ds_read_b64_tr_b16 a[130:131], v195 offset:50432
v_mfma_f32_16x16x32_bf16 v[20:23], a[88:91], v[124:127], v[20:23]
ds_read_b64_tr_b16 a[132:133], v196 offset:49664
ds_read_b64_tr_b16 a[134:135], v196 offset:50432
v_mfma_f32_16x16x32_bf16 v[24:27], a[92:95], v[124:127], v[24:27]
ds_read_b64_tr_b16 a[136:137], v197 offset:49664
ds_read_b64_tr_b16 a[138:139], v197 offset:50432
v_mfma_f32_16x16x32_bf16 v[28:31], a[96:99], v[124:127], v[28:31]
ds_read_b64_tr_b16 a[140:141], v220 offset:49792
ds_read_b64_tr_b16 a[142:143], v220 offset:50560
v_mfma_f32_16x16x32_bf16 v[32:35], a[100:103], v[124:127], v[32:35]
v_mfma_f32_16x16x32_bf16 v[52:55], a[100:103], v[172:175], v[52:55]
s_waitcnt lgkmcnt(10)
; instruction-cache warm-up: with EXEC = 0 hop over the branch stubs placed at the head of the coming 4-KiB code pages
s_mov_b64 exec, 0
s_cmp_eq_u32 s39, 0
s_cbranch_scc1 LW0_0_%=
s_cmp_eq_u32 s39, 1
s_cbranch_scc1 LW0_1_%=
s_cmp_eq_u32 s39, 2
s_cbranch_scc1 LW0_2_%=
s_branch LW0_3_%=
LWEND0_%=:
s_mov_b64 exec, -1
s_waitcnt vmcnt(1)
s_barrier
s_load_dwordx8 s[64:71], s[34:35], 0x80
v_mfma_f32_16x16x32_bf16 v[36:39], a[104:107], v[172:175], v[36:39]
ds_read_b64_tr_b16 a[64:65], v190
ds_read_b64_tr_b16 a[66:67], v190 offset:768
v_mfma_f32_16x16x32_bf16 v[16:19], a[104:107], v[128:131], v[16:19]
ds_read_b64_tr_b16 a[68:69], v191
ds_read_b64_tr_b16 a[70:71], v191 offset:768
v_mfma_f32_16x16x32_bf16 v[20:23], a[108:111], v[128:131], v[20:23]
ds_read_b64_tr_b16 a[72:73], v192
ds_read_b64_tr_b16 a[74:75], v192 offset:768
v_mfma_f32_16x16x32_bf16 v[24:27], a[112:115], v[128:131], v[24:27]
ds_read_b64_tr_b16 a[76:77], v193
ds_read_b64_tr_b16 a[78:79], v193 offset:768
v_mfma_f32_16x16x32_bf16 v[28:31], a[116:119], v[128:131], v[28:31]
ds_read_b64_tr_b16 a[80:81], v219 offset:128
ds_read_b64_tr_b16 a[82:83], v219 offset:896
v_mfma_f32_16x16x32_bf16 v[32:35], a[120:123], v[128:131], v[32:35]
v_mfma_f32_16x16x32_bf16 v[52:55], a[120:123], v[172:175], v[52:55]
s_waitcnt lgkmcnt(10)
v_mfma_f32_16x16x32_bf16 v[36:39], a[124:127], v[172:175], v[36:39]
ds_read_b64_tr_b16 a[84:85], v190 offset:6208
ds_read_b64_tr_b16 a[86:87], v190 offset:6976
v_mfma_f32_16x16x32_bf16 v[16:19], a[124:127], v[132:135], v[16:19]
ds_read_b64_tr_b16 a[88:89], v191 offset:6208
ds_read_b64_tr_b16 a[90:91], v191 offset:6976
v_mfma_f32_16x16x32_bf16 v[20:23], a[128:131], v[132:135], v[20:23]
ds_read_b64_tr_b16 a[92:93], v192 offset:6208
ds_read_b64_tr_b16 a[94:95], v192 offset:6976
v_mfma_f32_16x16x32_bf16 v[24:27], a[132:135], v[132:135], v[24:27]
ds_read_b64_tr_b16 a[96:97], v193 offset:6208
ds_read_b64_tr_b16 a[98:99], v193 offset:6976
v_mfma_f32_16x16x32_bf16 v[28:31], a[136:139], v[132:135], v[28:31]
ds_read_b64_tr_b16 a[100:101], v219 offset:6336
ds_read_b64_tr_b16 a[102:103], v219 offset:7104
v_mfma_f32_16x16x32_bf16 v[32:35], a[140:143], v[132:135], v[32:35]
v_mfma_f32_16x16x32_bf16 v[52:55], a[140:143], v[172:175], v[52:55]
s_waitcnt lgkmcnt(10)
v_mfma_f32_16x16x32_bf16 v[36:39], a[64:67], v[172:175], v[36:39]
ds_read_b64_tr_b16 a[104:105], v190 offset:12416
ds_read_b64_tr_b16 a[106:107], v190 offset:13184
v_mfma_f32_16x16x32_bf16 v[16:19], a[64:67], v[64:67], v[16:19]
ds_read_b64_tr_b16 a[108:109], v191 offset:12416
ds_read_b64_tr_b16 a[110:111], v191 offset:13184
v_mfma_f32_16x16x32_bf16 v[20:23], a[68:71], v[64:67], v[20:23]
ds_read_b64_tr_b16 a[112:113], v192 offset:12416
ds_read_b64_tr_b16 a[114:115], v192 offset:13184
v_mfma_f32_16x16x32_bf16 v[24:27], a[72:75], v[64:67], v[24:27]
ds_read_b64_tr_b16 a[116:117], v193 offset:12416
ds_read_b64_tr_b16 a[118:119], v193 offset:13184
v_mfma_f32_16x16x32_bf16 v[28:31], a[76:79], v[64:67], v[28:31]
ds_read_b64_tr_b16 a[120:121], v219 offset:12544
ds_read_b64_tr_b16 a[122:123], v219 offset:13312
v_mfma_f32_16x16x32_bf16 v[32:35], a[80:83], v[64:67], v[32:35]
v_mfma_f32_16x16x32_bf16 v[52:55], a[80:83], v[172:175], v[52:55]
s_waitcnt lgkmcnt(10)
v_mfma_f32_16x16x32_bf16 v[36:39], a[84:87], v[172:175], v[36:39]
ds_read_b64_tr_b16 a[124:125], v190 offset:18624
ds_read_b64_tr_b16 a[126:127], v190 offset:19392
v_mfma_f32_16x16x32_bf16 v[16:19], a[84:87], v[68:71], v[16:19]
ds_read_b64_tr_b16 a[128:129], v191 offset:18624
ds_read_b64_tr_b16 a[130:131], v191 offset:19392
v_mfma_f32_16x16x32_bf16 v[20:23], a[88:91], v[68:71], v[20:23]
ds_read_b64_tr_b16 a[132:133], v192 offset:18624
ds_read_b64_tr_b16 a[134:135], v192 offset:19392
v_mfma_f32_16x16x32_bf16 v[24:27], a[92:95], v[68:71], v[24:27]
ds_read_b64_tr_b16 a[136:137], v193 offset:18624
ds_read_b64_tr_b16 a[138:139], v193 offset:19392
v_mfma_f32_16x16x32_bf16 v[28:31], a[96:99], v[68:71], v[28:31]
ds_read_b64_tr_b16 a[140:141], v219 offset:18752
ds_read_b64_tr_b16 a[142:143], v219 offset:19520
v_mfma_f32_16x16x32_bf16 v[32:35], a[100:103], v[68:71], v[32:35]
v_mfma_f32_16x16x32_bf16 v[52:55], a[100:103], v[172:175], v[52:55]
s_waitcnt lgkmcnt(10)
global_load_dwordx4 v[248:251], v187, s[50:51] sc0 sc1
global_load_dwordx4 v[252:255], v187, s[50:51] offset:16 sc0 sc1
v_mfma_f32_16x16x32_bf16 v[36:39], a[104:107], v[172:175], v[36:39]
ds_read_b64_tr_b16 a[64:65], v190 offset:24832
ds_read_b64_tr_b16 a[66:67], v190 offset:25600
v_mfma_f32_16x16x32_bf16 v[16:19], a[104:107], v[72:75], v[16:19]
ds_read_b64_tr_b16 a[68:69], v191 offset:24832
ds_read_b64_tr_b16 a[70:71], v191 offset:25600
v_mfma_f32_16x16x32_bf16 v[20:23], a[108:111], v[72:75], v[20:23]
ds_read_b64_tr_b16 a[72:73], v192 offset:24832
ds_read_b64_tr_b16 a[74:75], v192 offset:25600
v_mfma_f32_16x16x32_bf16 v[24:27], a[112:115], v[72:75], v[24:27]
ds_read_b64_tr_b16 a[76:77], v193 offset:24832
ds_read_b64_tr_b16 a[78:79], v193 offset:25600
v_mfma_f32_16x16x32_bf16 v[28:31], a[116:119], v[72:75], v[28:31]
ds_read_b64_tr_b16 a[80:81], v219 offset:24960
ds_read_b64_tr_b16 a[82:83], v219 offset:25728
v_mfma_f32_16x16x32_bf16 v[32:35], a[120:123], v[72:75], v[32:35]
v_mfma_f32_16x16x32_bf16 v[52:55], a[120:123], v[172:175], v[52:55]
s_waitcnt lgkmcnt(10)
v_mfma_f32_16x16x32_bf16 v[36:39], a[124:127], v[172:175], v[36:39]
ds_read_b64_tr_b16 a[84:85], v190 offset:31040
ds_read_b64_tr_b16 a[86:87], v190 offset:31808
v_mfma_f32_16x16x32_bf16 v[16:19], a[124:127], v[76:79], v[16:19]
ds_read_b64_tr_b16 a[88:89], v191 offset:31040
ds_read_b64_tr_b16 a[90:91], v191 offset:31808
v_mfma_f32_16x16x32_bf16 v[20:23], a[128:131], v[76:79], v[20:23]
ds_read_b64_tr_b16 a[92:93], v192 offset:31040
ds_read_b64_tr_b16 a[94:95], v192 offset:31808
v_mfma_f32_16x16x32_bf16 v[24:27], a[132:135], v[76:79], v[24:27]
ds_read_b64_tr_b16 a[96:97], v193 offset:31040
ds_read_b64_tr_b16 a[98:99], v193 offset:31808
v_mfma_f32_16x16x32_bf16 v[28:31], a[136:139], v[76:79], v[28:31]
ds_read_b64_tr_b16 a[100:101], v219 offset:31168
ds_read_b64_tr_b16 a[102:103], v219 offset:31936
v_mfma_f32_16x16x32_bf16 v[32:35], a[140:143], v[76:79], v[32:35]
v_mfma_f32_16x16x32_bf16 v[52:55], a[140:143], v[172:175], v[52:55]
s_waitcnt lgkmcnt(10)
v_mfma_f32_16x16x32_bf16 v[36:39], a[64:67], v[172:175], v[36:39]
v_mfma_f32_16x16x32_bf16 v[16:19], a[64:67], v[80:83], v[16:19]
v_mfma_f32_16x16x32_bf16 v[20:23], a[68:71], v[80:83], v[20:23]
v_mfma_f32_16x16x32_bf16 v[24:27], a[72:75], v[80:83], v[24:27]
v_mfma_f32_16x16x32_bf16 v[28:31], a[76:79], v[80:83], v[28:31]
v_mfma_f32_16x16x32_bf16 v[32:35], a[80:83], v[80:83], v[32:35]
v_mfma_f32_16x16x32_bf16 v[52:55], a[80:83], v[172:175], v[52:55]
s_waitcnt lgkmcnt(0)
v_mfma_f32_16x16x32_bf16 v[36:39], a[84:87], v[172:175], v[36:39]
v_mfma_f32_16x16x32_bf16 v[16:19], a[84:87], v[84:87], v[16:19]
v_mfma_f32_16x16x32_bf16 v[20:23], a[88:91], v[84:87], v[20:23]
v_mfma_f32_16x16x32_bf16 v[24:27], a[92:95], v[84:87], v[24:27]
v_mfma_f32_16x16x32_bf16 v[28:31], a[96:99], v[84:87], v[28:31]
s_nop 3
ds_write_b128 v144, v[36:39]
v_mfma_f32_16x16x32_bf16 v[32:35], a[100:103], v[84:87], v[32:35]
v_mfma_f32_16x16x32_bf16 v[52:55], a[100:103], v[172:175], v[52:55]
; column sums: wave w summed tile w; exchange through LDS
s_lshl_b32 m0, s39, 10
v_pk_fma_f32 v[16:17], v[36:37], v[168:169], v[16:17] op_sel:[0,1,0] op_sel_hi:[1,1,1] neg_lo:[0,1,0] neg_hi:[0,1,0]
v_pk_mul_f32 v[16:17], v[16:17], v[170:171] op_sel:[0,1] op_sel_hi:[1,1]
v_pk_fma_f32 v[18:19], v[38:39], v[168:169], v[18:19] op_sel:[0,1,0] op_sel_hi:[1,1,1] neg_lo:[0,1,0] neg_hi:[0,1,0]
v_pk_mul_f32 v[18:19], v[18:19], v[170:171] op_sel:[0,1] op_sel_hi:[1,1]
v_cvt_pk_bf16_f32 v36, v16, v17
v_cvt_pk_bf16_f32 v37, v18, v19
ds_write_b64 v148, v[36:37]
v_pk_fma_f32 v[32:33], v[52:53], v[168:169], v[32:33] op_sel:[0,1,0] op_sel_hi:[1,1,1] neg_lo:[0,1,0] neg_hi:[0,1,0]
v_pk_mul_f32 v[32:33], v[32:33], v[170:171] op_sel:[0,1] op_sel_hi:[1,1]
v_pk_fma_f32 v[34:35], v[54:55], v[168:169], v[34:35] op_sel:[0,1,0] op_sel_hi:[1,1,1] neg_lo:[0,1,0] neg_hi:[0,1,0]
v_pk_mul_f32 v[34:35], v[34:35], v[170:171] op_sel:[0,1] op_sel_hi:[1,1]
v_cvt_pk_bf16_f32 v52, v32, v33
v_cvt_pk_bf16_f32 v53, v34, v35
ds_write_b64 v185, v[52:53] offset:128
s_waitcnt vmcnt(1)
v_xor_b32 v248, s40, v248
v_xor_b32 v249, s41, v249
v_xor_b32 v250, s40, v250
v_xor_b32 v251, s41, v251
s_waitcnt vmcnt(0)
v_xor_b32 v252, s40, v252
v_xor_b32 v253, s41, v253
v_xor_b32 v254, s40, v254
v_xor_b32 v255, s41, v255
v_or_b32 v167, v248, v249
v_or3_b32 v167, v167, v250, v251
v_or3_b32 v167, v167, v252, v253
v_or3_b32 v167, v167, v254, v255
v_cmp_ne_u32 vcc, 0, v167
s_nop 0
s_cmp_eq_u64 vcc, 0
s_cbranch_scc1 Lpd_13_%=
s_mov_b32 s97, 32768
Lpf_12_%=:
global_load_dwordx4 v[248:251], v187, s[50:51] sc0 sc1
global_load_dwordx4 v[252:255], v187, s[50:51] offset:16 sc0 sc1
s_waitcnt vmcnt(1)
v_xor_b32 v248, s40, v248
v_xor_b32 v249, s41, v249
v_xor_b32 v250, s40, v250
v_xor_b32 v251, s41, v251
s_waitcnt vmcnt(0)
v_xor_b32 v252, s40, v252
v_xor_b32 v253, s41, v253
v_xor_b32 v254, s40, v254
v_xor_b32 v255, s41, v255
v_or_b32 v167, v248, v249
v_or3_b32 v167, v167, v250, v251
v_or3_b32 v167, v167, v252, v253
v_or3_b32 v167, v167, v254, v255
v_cmp_ne_u32 vcc, 0, v167
s_nop 0
s_cmp_eq_u64 vcc, 0
s_cbranch_scc1 Lpd_13_%=
s_sub_u32 s97, s97, 1
s_cmp_lg_u32 s97, 0
s_cbranch_scc1 Lpf_12_%=
Lpd_13_%=:
s_waitcnt lgkmcnt(0)
s_barrier
buffer_load_dwordx4 v4, s[52:55], m0 offen sc1 lds
s_add_u32 m0, m0, 4096
ds_read_b128 v[40:43], v145
buffer_load_dwordx4 v4, s[52:55], m0 offen sc1 lds
s_add_u32 m0, m0, 4096
ds_read_b128 v[44:47], v146
buffer_load_dwordx4 v4, s[52:55], m0 offen sc1 lds
s_add_u32 m0, m0, 4096
ds_read_b128 v[48:51], v147
buffer_load_dwordx4 v4, s[52:55], m0 offen sc1 lds
s_add_u32 m0, m0, 4096
s_nop 0
buffer_load_dwordx4 v4, s[52:55], m0 offen sc1 lds
s_lshl_b32 s42, s39, 12
s_add_u32 m0, s42, 20480
s_waitcnt lgkmcnt(2)
v_pk_fma_f32 v[20:21], v[40:41], v[168:169], v[20:21] op_sel:[0,1,0] op_sel_hi:[1,1,1] neg_lo:[0,1,0] neg_hi:[0,1,0]
v_pk_mul_f32 v[20:21], v[20:21], v[170:171] op_sel:[0,1] op_sel_hi:[1,1]
v_pk_fma_f32 v[22:23], v[42:43], v[168:169], v[22:23] op_sel:[0,1,0] op_sel_hi:[1,1,1] neg_lo:[0,1,0] neg_hi:[0,1,0]
v_pk_mul_f32 v[22:23], v[22:23], v[170:171] op_sel:[0,1] op_sel_hi:[1,1]
v_cvt_pk_bf16_f32 v40, v20, v21
v_cvt_pk_bf16_f32 v41, v22, v23
ds_write_b64 v149, v[40:41]
s_waitcnt lgkmcnt(2)
v_pk_fma_f32 v[24:25], v[44:45], v[168:169], v[24:25] op_sel:[0,1,0] op_sel_hi:[1,1,1] neg_lo:[0,1,0] neg_hi:[0,1,0]
v_pk_mul_f32 v[24:25], v[24:25], v[170:171] op_sel:[0,1] op_sel_hi:[1,1]
v_pk_fma_f32 v[26:27], v[46:47], v[168:169], v[26:27] op_sel:[0,1,0] op_sel_hi:[1,1,1] neg_lo:[0,1,0] neg_hi:[0,1,0]
v_pk_mul_f32 v[26:27], v[26:27], v[170:171] op_sel:[0,1] op_sel_hi:[1,1]
v_cvt_pk_bf16_f32 v44, v24, v25
v_cvt_pk_bf16_f32 v45, v26, v27
ds_write_b64 v150, v[44:45]
s_waitcnt lgkmcnt(2)
v_pk_fma_f32 v[28:29], v[48:49], v[168:169], v[28:29] op_sel:[0,1,0] op_sel_hi:[1,1,1] neg_lo:[0,1,0] neg_hi:[0,1,0]
v_pk_mul_f32 v[28:29], v[28:29], v[170:171] op_sel:[0,1] op_sel_hi:[1,1]
v_pk_fma_f32 v[30:31], v[50:51], v[168:169], v[30:31] op_sel:[0,1,0] op_sel_hi:[1,1,1] neg_lo:[0,1,0] neg_hi:[0,1,0]
v_pk_mul_f32 v[30:31], v[30:31], v[170:171] op_sel:[0,1] op_sel_hi:[1,1]
v_cvt_pk_bf16_f32 v48, v28, v29
v_cvt_pk_bf16_f32 v49, v30, v31
ds_write_b64 v151, v[48:49]
ds_read_b32 v160, v222 offset:32
ds_read_b32 v136, v221
ds_read_b32 v137, v221 offset:4
)"
R"(ds_read_b32 v138, v221 offset:8
ds_read_b32 v139, v221 offset:12
ds_read_b32 v161, v222 offset:104
ds_read_b32 v140, v221 offset:72
ds_read_b32 v141, v221 offset:76
ds_read_b32 v142, v221 offset:80
ds_read_b32 v143, v221 offset:84
s_cmp_eq_u32 s63, 0
s_cbranch_scc0 Lc_14_%=
s_lshl_b32 s96, s36, 3
s_add_u32 s96, s96, 0
v_mov_b32 v248, s40
v_mov_b32 v249, s41
v_mov_b32 v167, s96
s_mov_b32 exec_lo, 1
s_mov_b32 exec_hi, 0
global_store_dwordx2 v167, v[248:249], s[50:51] sc1
s_mov_b64 exec, -1
Lc_14_%=:
; ===== attention: two q-tiles (2 heads x 16 tokens each) against all 2048 keys
s_mov_b32 vcc_lo, -1
s_mov_b32 vcc_hi, 0
v_mov_b32 v145, 0
v_mov_b32 v146, 0
v_mov_b32 v149, 0
v_mov_b32 v150, 0
v_mov_b32 v157, 0
s_waitcnt lgkmcnt(9)
v_cndmask_b32 v144, 0, v160, vcc
v_mov_b32 v147, v144
v_sub_u32 v148, v160, v144
v_mov_b32 v151, v148
s_waitcnt lgkmcnt(4)
v_cndmask_b32 v152, 0, v161, vcc
v_mov_b32 v155, v152
v_sub_u32 v156, v161, v152
v_mov_b32 v159, v156
s_waitcnt lgkmcnt(0)
s_waitcnt vmcnt(0)
s_barrier
; softmax reference point: the row max over the first 32 keys (checked after the last key)
ds_read_b64 v[132:133], v10
ds_read_b128 v[120:123], v4
ds_read_b128 v[124:127], v7
s_waitcnt lgkmcnt(2)
v_mfma_f32_32x32x8_bf16 v[32:47], v[132:133], v[152:153], 0
s_waitcnt lgkmcnt(1)
v_mfma_f32_32x32x16_bf16 v[32:47], v[120:123], v[140:143], v[32:47]
v_mfma_f32_32x32x8_bf16 v[16:31], v[132:133], v[144:145], 0
v_mfma_f32_32x32x16_bf16 v[16:31], v[120:123], v[136:139], v[16:31]
v_mov_b32 v244, -1
v_mov_b32 v245, -1
v_mov_b32 v246, -1
v_mov_b32 v247, -1
s_add_u32 s8, s50, s80
s_addc_u32 s9, s51, 0
s_add_u32 s10, s50, s81
s_addc_u32 s11, s51, 0
s_nop 1
v_max3_f32 v164, v32, v33, v34
v_max3_f32 v164, v164, v35, v36
v_max3_f32 v164, v164, v37, v38
v_max3_f32 v164, v164, v39, v40
v_max3_f32 v164, v164, v41, v42
v_max3_f32 v164, v164, v43, v44
v_max3_f32 v164, v164, v45, v46
v_max_f32 v164, v164, v47
v_mov_b32 v165, v164
s_nop 1
v_permlane32_swap_b32 v164, v165
v_max_f32 v164, v164, v165
LW0_0_%=:
s_cbranch_execz LW0_4_%=
v_pk_mul_f32 v[64:65], v[164:165], -1.0 op_sel:[0,0] op_sel_hi:[0,0]
v_pk_mul_f32 v[66:67], v[164:165], -1.0 op_sel:[0,0] op_sel_hi:[0,0]
v_pk_mul_f32 v[68:69], v[164:165], -1.0 op_sel:[0,0] op_sel_hi:[0,0]
v_pk_mul_f32 v[70:71], v[164:165], -1.0 op_sel:[0,0] op_sel_hi:[0,0]
v_pk_mul_f32 v[72:73], v[164:165], -1.0 op_sel:[0,0] op_sel_hi:[0,0]
v_pk_mul_f32 v[74:75], v[164:165], -1.0 op_sel:[0,0] op_sel_hi:[0,0]
v_pk_mul_f32 v[76:77], v[164:165], -1.0 op_sel:[0,0] op_sel_hi:[0,0]
v_pk_mul_f32 v[78:79], v[164:165], -1.0 op_sel:[0,0] op_sel_hi:[0,0]
v_max3_f32 v162, v16, v17, v18
v_max3_f32 v162, v162, v19, v20
v_max3_f32 v162, v162, v21, v22
v_max3_f32 v162, v162, v23, v24
v_max3_f32 v162, v162, v25, v26
v_max3_f32 v162, v162, v27, v28
v_max3_f32 v162, v162, v29, v30
v_max_f32 v162, v162, v31
v_mov_b32 v163, v162
s_nop 1
v_permlane32_swap_b32 v162, v163
v_max_f32 v162, v162, v163
v_pk_mul_f32 v[48:49], v[162:163], -1.0 op_sel:[0,0] op_sel_hi:[0,0]
v_pk_mul_f32 v[50:51], v[162:163], -1.0 op_sel:[0,0] op_sel_hi:[0,0]
v_pk_mul_f32 v[52:53], v[162:163], -1.0 op_sel:[0,0] op_sel_hi:[0,0]
v_pk_mul_f32 v[54:55], v[162:163], -1.0 op_sel:[0,0] op_sel_hi:[0,0]
v_pk_mul_f32 v[56:57], v[162:163], -1.0 op_sel:[0,0] op_sel_hi:[0,0]
v_pk_mul_f32 v[58:59], v[162:163], -1.0 op_sel:[0,0] op_sel_hi:[0,0]
v_pk_mul_f32 v[60:61], v[162:163], -1.0 op_sel:[0,0] op_sel_hi:[0,0]
v_pk_mul_f32 v[62:63], v[162:163], -1.0 op_sel:[0,0] op_sel_hi:[0,0]
v_pk_add_f32 v[16:17], v[16:17], v[48:49]
v_pk_add_f32 v[18:19], v[18:19], v[50:51]
v_pk_add_f32 v[20:21], v[20:21], v[52:53]
v_pk_add_f32 v[22:23], v[22:23], v[54:55]
v_pk_add_f32 v[24:25], v[24:25], v[56:57]
v_pk_add_f32 v[26:27], v[26:27], v[58:59]
v_pk_add_f32 v[28:29], v[28:29], v[60:61]
v_pk_add_f32 v[30:31], v[30:31], v[62:63]
v_mfma_f32_32x32x8_bf16 v[32:47], v[132:133], v[152:153], v[64:79]
v_exp_f32 v16, v16
v_exp_f32 v17, v17
v_exp_f32 v18, v18
v_exp_f32 v19, v19
ds_read_b128 v[128:131], v7 offset:608
buffer_load_dwordx4 v4, s[52:55], m0 offen sc1 lds
buffer_load_dwordx4 v4, s[52:55], m0 offen offset:1024 sc1 lds
v_exp_f32 v20, v20
v_exp_f32 v21, v21
v_cvt_pk_bf16_f32 v112, v16, v17
v_exp_f32 v22, v22
v_cvt_pk_bf16_f32 v113, v18, v19
v_exp_f32 v23, v23
v_mfma_f32_32x32x16_bf16 v[32:47], v[120:123], v[140:143], v[32:47]
s_waitcnt lgkmcnt(1)
ds_read_b128 v[120:123], v4 offset:1024
buffer_load_dwordx4 v4, s[52:55], m0 offen offset:2048 sc1 lds
buffer_load_dwordx4 v4, s[52:55], m0 offen offset:3072 sc1 lds
s_add_u32 m0, m0, 16384
v_exp_f32 v24, v24
v_exp_f32 v25, v25
v_cvt_pk_bf16_f32 v114, v20, v21
v_exp_f32 v26, v26
v_cvt_pk_bf16_f32 v115, v22, v23
v_exp_f32 v27, v27
v_exp_f32 v28, v28
v_mfma_f32_32x32x16_bf16 v[80:95], v[124:127], v[112:115], 0
s_waitcnt lgkmcnt(1)
v_exp_f32 v29, v29
v_exp_f32 v30, v30
v_cvt_pk_bf16_f32 v116, v24, v25
v_exp_f32 v31, v31
v_cvt_pk_bf16_f32 v117, v26, v27
v_cvt_pk_bf16_f32 v118, v28, v29
v_cvt_pk_bf16_f32 v119, v30, v31
v_mfma_f32_32x32x8_bf16 v[16:31], v[132:133], v[146:147], v[48:63]
v_exp_f32 v32, v32
v_exp_f32 v33, v33
v_exp_f32 v34, v34
v_exp_f32 v35, v35
v_mfma_f32_32x32x16_bf16 v[80:95], v[128:131], v[116:119], v[80:95]
s_waitcnt lgkmcnt(0)
buffer_load_dwordx4 v4, s[52:55], m0 offen sc1 lds
buffer_load_dwordx4 v4, s[52:55], m0 offen offset:1024 sc1 lds
v_exp_f32 v36, v36
v_exp_f32 v37, v37
v_cvt_pk_bf16_f32 v112, v32, v33
v_exp_f32 v38, v38
v_cvt_pk_bf16_f32 v113, v34, v35
v_exp_f32 v39, v39
v_mfma_f32_32x32x16_bf16 v[16:31], v[120:123], v[136:139], v[16:31]
v_exp_f32 v40, v40
v_exp_f32 v41, v41
v_cvt_pk_bf16_f32 v114, v36, v37
v_exp_f32 v42, v42
v_cvt_pk_bf16_f32 v115, v38, v39
v_exp_f32 v43, v43
v_exp_f32 v44, v44
v_mfma_f32_32x32x16_bf16 v[96:111], v[124:127], v[112:115], 0
ds_read_b128 v[124:127], v7 offset:1216
v_exp_f32 v45, v45
v_exp_f32 v46, v46
v_cvt_pk_bf16_f32 v116, v40, v41
v_exp_f32 v47, v47
v_cvt_pk_bf16_f32 v117, v42, v43
v_cvt_pk_bf16_f32 v118, v44, v45
v_cvt_pk_bf16_f32 v119, v46, v47
v_mfma_f32_32x32x8_bf16 v[32:47], v[132:133], v[154:155], v[64:79]
v_exp_f32 v16, v16
v_exp_f32 v17, v17
v_exp_f32 v18, v18
v_exp_f32 v19, v19
v_mfma_f32_32x32x16_bf16 v[96:111], v[128:131], v[116:119], v[96:111]
ds_read_b128 v[128:131], v7 offset:1824
buffer_load_dwordx4 v4, s[52:55], m0 offen offset:2048 sc1 lds
buffer_load_dwordx4 v4, s[52:55], m0 offen offset:3072 sc1 lds
s_add_u32 m0, m0, 16384
v_exp_f32 v20, v20
v_exp_f32 v21, v21
v_cvt_pk_bf16_f32 v112, v16, v17
v_exp_f32 v22, v22
v_cvt_pk_bf16_f32 v113, v18, v19
v_exp_f32 v23, v23
v_mfma_f32_32x32x16_bf16 v[32:47], v[120:123], v[140:143], v[32:47]
s_waitcnt lgkmcnt(1)
ds_read_b128 v[120:123], v4 offset:2048
v_exp_f32 v24, v24
v_exp_f32 v25, v25
v_cvt_pk_bf16_f32 v114, v20, v21
v_exp_f32 v26, v26
v_cvt_pk_bf16_f32 v115, v22, v23
v_exp_f32 v27, v27
v_exp_f32 v28, v28
v_mfma_f32_32x32x16_bf16 v[80:95], v[124:127], v[112:115], v[80:95]
s_waitcnt lgkmcnt(1)
v_exp_f32 v29, v29
v_exp_f32 v30, v30
v_cvt_pk_bf16_f32 v116, v24, v25
v_exp_f32 v31, v31
v_cvt_pk_bf16_f32 v117, v26, v27
v_cvt_pk_bf16_f32 v118, v28, v29
v_cvt_pk_bf16_f32 v119, v30, v31
v_mfma_f32_32x32x8_bf16 v[16:31], v[132:133], v[148:149], v[48:63]
v_exp_f32 v32, v32
v_exp_f32 v33, v33
v_exp_f32 v34, v34
v_exp_f32 v35, v35
v_mfma_f32_32x32x16_bf16 v[80:95], v[128:131], v[116:119], v[80:95]
s_waitcnt lgkmcnt(0)
buffer_load_dwordx4 v4, s[52:55], m0 offen sc1 lds
buffer_load_dwordx4 v4, s[52:55], m0 offen offset:1024 sc1 lds
v_exp_f32 v36, v36
v_exp_f32 v37, v37
v_cvt_pk_bf16_f32 v112, v32, v33
v_exp_f32 v38, v38
v_cvt_pk_bf16_f32 v113, v34, v35
v_exp_f32 v39, v39
v_mfma_f32_32x32x16_bf16 v[16:31], v[120:123], v[136:139], v[16:31]
buffer_load_dwordx4 v4, s[52:55], m0 offen offset:2048 sc1 lds
buffer_load_dwordx4 v4, s[52:55], m0 offen offset:3072 sc1 lds
s_add_u32 m0, m0, 16384
v_exp_f32 v40, v40
v_exp_f32 v41, v41
v_cvt_pk_bf16_f32 v114, v36, v37
v_exp_f32 v42, v42
v_cvt_pk_bf16_f32 v115, v38, v39
v_exp_f32 v43, v43
v_exp_f32 v44, v44
v_mfma_f32_32x32x16_bf16 v[96:111], v[124:127], v[112:115], v[96:111]
ds_read_b128 v[124:127], v7 offset:2432
v_exp_f32 v45, v45
v_exp_f32 v46, v46
v_cvt_pk_bf16_f32 v116, v40, v41
v_exp_f32 v47, v47
v_cvt_pk_bf16_f32 v117, v42, v43
v_cvt_pk_bf16_f32 v118, v44, v45
v_cvt_pk_bf16_f32 v119, v46, v47
v_mfma_f32_32x32x8_bf16 v[32:47], v[132:133], v[156:157], v[64:79]
v_exp_f32 v16, v16
v_exp_f32 v17, v17
v_exp_f32 v18, v18
v_exp_f32 v19, v19
v_mfma_f32_32x32x16_bf16 v[96:111], v[128:131], v[116:119], v[96:111]
ds_read_b128 v[128:131], v7 offset:3040
buffer_load_dwordx4 v4, s[52:55], m0 offen sc1 lds
buffer_load_dwordx4 v4, s[52:55], m0 offen offset:1024 sc1 lds
v_exp_f32 v20, v20
v_exp_f32 v21, v21
v_cvt_pk_bf16_f32 v112, v16, v17
v_exp_f32 v22, v22
v_cvt_pk_bf16_f32 v113, v18, v19
v_exp_f32 v23, v23
v_mfma_f32_32x32x16_bf16 v[32:47], v[120:123], v[140:143], v[32:47]
s_waitcnt lgkmcnt(1)
ds_read_b128 v[120:123], v4 offset:3072
v_exp_f32 v24, v24
v_exp_f32 v25, v25
v_cvt_pk_bf16_f32 v114, v20, v21
v_exp_f32 v26, v26
v_cvt_pk_bf16_f32 v115, v22, v23
v_exp_f32 v27, v27
v_exp_f32 v28, v28
v_mfma_f32_32x32x16_bf16 v[80:95], v[124:127], v[112:115], v[80:95]
s_waitcnt lgkmcnt(1)
v_exp_f32 v29, v29
v_exp_f32 v30, v30
v_cvt_pk_bf16_f32 v116, v24, v25
v_exp_f32 v31, v31
v_cvt_pk_bf16_f32 v117, v26, v27
v_cvt_pk_bf16_f32 v118, v28, v29
v_cvt_pk_bf16_f32 v119, v30, v31
v_mfma_f32_32x32x8_bf16 v[16:31], v[132:133], v[150:151], v[48:63]
v_exp_f32 v32, v32
v_exp_f32 v33, v33
v_exp_f32 v34, v34
v_exp_f32 v35, v35
v_mfma_f32_32x32x16_bf16 v[80:95], v[128:131], v[116:119], v[80:95]
s_waitcnt lgkmcnt(0)
buffer_load_dwordx4 v4, s[52:55], m0 offen offset:2048 sc1 lds
buffer_load_dwordx4 v4, s[52:55], m0 offen offset:3072 sc1 lds
s_add_u32 m0, m0, 16384
v_exp_f32 v36, v36
v_exp_f32 v37, v37
v_cvt_pk_bf16_f32 v112, v32, v33
v_exp_f32 v38, v38
v_cvt_pk_bf16_f32 v113, v34, v35
v_exp_f32 v39, v39
v_mfma_f32_32x32x16_bf16 v[16:31], v[120:123], v[136:139], v[16:31]
v_exp_f32 v40, v40
v_exp_f32 v41, v41
v_cvt_pk_bf16_f32 v114, v36, v37
v_exp_f32 v42, v42
v_cvt_pk_bf16_f32 v115, v38, v39
v_exp_f32 v43, v43
v_exp_f32 v44, v44
v_mfma_f32_32x32x16_bf16 v[96:111], v[124:127], v[112:115], v[96:111]
ds_read_b128 v[124:127], v7 offset:3648
v_exp_f32 v45, v45
v_exp_f32 v46, v46
v_cvt_pk_bf16_f32 v116, v40, v41
v_exp_f32 v47, v47
v_cvt_pk_bf16_f32 v117, v42, v43
v_cvt_pk_bf16_f32 v118, v44, v45
v_cvt_pk_bf16_f32 v119, v46, v47
v_mfma_f32_32x32x8_bf16 v[32:47], v[132:133], v[158:159], v[64:79]
v_exp_f32 v16, v16
v_exp_f32 v17, v17
v_exp_f32 v18, v18
v_exp_f32 v19, v19
v_mfma_f32_32x32x16_bf16 v[96:111], v[128:131], v[116:119], v[96:111]
ds_read_b128 v[128:131], v7 offset:4256
ds_read_b64 v[132:133], v10 offset:512
buffer_load_dwordx4 v4, s[52:55], m0 offen sc1 lds
buffer_load_dwordx4 v4, s[52:55], m0 offen offset:1024 sc1 lds
v_exp_f32 v20, v20
v_exp_f32 v21, v21
v_cvt_pk_bf16_f32 v112, v16, v17
v_exp_f32 v22, v22
v_cvt_pk_bf16_f32 v113, v18, v19
v_exp_f32 v23, v23
v_mfma_f32_32x32x16_bf16 v[32:47], v[120:123], v[140:143], v[32:47]
s_waitcnt lgkmcnt(2)
ds_read_b128 v[120:123], v4 offset:4096
buffer_load_dwordx4 v4, s[52:55], m0 offen offset:2048 sc1 lds
buffer_load_dwordx4 v4, s[52:55], m0 offen offset:3072 sc1 lds
s_add_u32 m0, m0, 16384
v_exp_f32 v24, v24
v_exp_f32 v25, v25
v_cvt_pk_bf16_f32 v114, v20, v21
v_exp_f32 v26, v26
v_cvt_pk_bf16_f32 v115, v22, v23
v_exp_f32 v27, v27
v_exp_f32 v28, v28
v_mfma_f32_32x32x16_bf16 v[80:95], v[124:127], v[112:115], v[80:95]
s_waitcnt lgkmcnt(1)
v_exp_f32 v29, v29
v_exp_f32 v30, v30
v_cvt_pk_bf16_f32 v116, v24, v25
v_exp_f32 v31, v31
v_cvt_pk_bf16_f32 v117, v26, v27
v_cvt_pk_bf16_f32 v118, v28, v29
v_cvt_pk_bf16_f32 v119, v30, v31
v_mfma_f32_32x32x8_bf16 v[16:31], v[132:133], v[144:145], v[48:63]
v_exp_f32 v32, v32
v_exp_f32 v33, v33
v_exp_f32 v34, v34
v_exp_f32 v35, v35
v_mfma_f32_32x32x16_bf16 v[80:95], v[128:131], v[116:119], v[80:95]
s_waitcnt lgkmcnt(0)
buffer_load_dwordx4 v4, s[52:55], m0 offen sc1 lds
buffer_load_dwordx4 v4, s[52:55], m0 offen offset:1024 sc1 lds
v_exp_f32 v36, v36
v_exp_f32 v37, v37
v_cvt_pk_bf16_f32 v112, v32, v33
v_exp_f32 v38, v38
v_cvt_pk_bf16_f32 v113, v34, v35
v_exp_f32 v39, v39
v_mfma_f32_32x32x16_bf16 v[16:31], v[120:123], v[136:139], v[16:31]
v_exp_f32 v40, v40
v_exp_f32 v41, v41
v_cvt_pk_bf16_f32 v114, v36, v37
v_exp_f32 v42, v42
v_cvt_pk_bf16_f32 v115, v38, v39
v_exp_f32 v43, v43
v_exp_f32 v44, v44
v_mfma_f32_32x32x16_bf16 v[96:111], v[124:127], v[112:115], v[96:111]
ds_read_b128 v[124:127], v7 offset:4864
v_exp_f32 v45, v45
v_exp_f32 v46, v46
v_cvt_pk_bf16_f32 v116, v40, v41
v_exp_f32 v47, v47
v_cvt_pk_bf16_f32 v117, v42, v43
v_cvt_pk_bf16_f32 v118, v44, v45
v_cvt_pk_bf16_f32 v119, v46, v47
v_mfma_f32_32x32x8_bf16 v[32:47], v[132:133], v[152:153], v[64:79]
v_exp_f32 v16, v16
v_exp_f32 v17, v17
v_exp_f32 v18, v18
v_exp_f32 v19, v19
v_mfma_f32_32x32x16_bf16 v[96:111], v[128:131], v[116:119], v[96:111]
ds_read_b128 v[128:131], v7 offset:5472
buffer_load_dwordx4 v4, s[52:55], m0 offen offset:2048 sc1 lds
buffer_load_dwordx4 v4, s[52:55], m0 offen offset:3072 sc1 lds
s_add_u32 m0, m0, 16384
v_exp_f32 v20, v20
v_exp_f32 v21, v21
v_cvt_pk_bf16_f32 v112, v16, v17
v_exp_f32 v22, v22
v_cvt_pk_bf16_f32 v113, v18, v19
v_exp_f32 v23, v23
)"
R"(v_mfma_f32_32x32x16_bf16 v[32:47], v[120:123], v[140:143], v[32:47]
s_waitcnt lgkmcnt(1)
ds_read_b128 v[120:123], v4 offset:5120
v_exp_f32 v24, v24
v_exp_f32 v25, v25
v_cvt_pk_bf16_f32 v114, v20, v21
v_exp_f32 v26, v26
v_cvt_pk_bf16_f32 v115, v22, v23
v_exp_f32 v27, v27
v_exp_f32 v28, v28
v_mfma_f32_32x32x16_bf16 v[80:95], v[124:127], v[112:115], v[80:95]
s_waitcnt lgkmcnt(1)
v_exp_f32 v29, v29
v_exp_f32 v30, v30
v_cvt_pk_bf16_f32 v116, v24, v25
v_exp_f32 v31, v31
v_cvt_pk_bf16_f32 v117, v26, v27
v_cvt_pk_bf16_f32 v118, v28, v29
v_cvt_pk_bf16_f32 v119, v30, v31
v_mfma_f32_32x32x8_bf16 v[16:31], v[132:133], v[146:147], v[48:63]
v_exp_f32 v32, v32
v_exp_f32 v33, v33
v_exp_f32 v34, v34
v_exp_f32 v35, v35
v_mfma_f32_32x32x16_bf16 v[80:95], v[128:131], v[116:119], v[80:95]
s_waitcnt lgkmcnt(0)
buffer_load_dwordx4 v4, s[52:55], m0 offen sc1 lds
buffer_load_dwordx4 v4, s[52:55], m0 offen offset:1024 sc1 lds
v_exp_f32 v36, v36
v_exp_f32 v37, v37
v_cvt_pk_bf16_f32 v112, v32, v33
v_exp_f32 v38, v38
v_cvt_pk_bf16_f32 v113, v34, v35
v_exp_f32 v39, v39
v_mfma_f32_32x32x16_bf16 v[16:31], v[120:123], v[136:139], v[16:31]
buffer_load_dwordx4 v4, s[52:55], m0 offen offset:2048 sc1 lds
buffer_load_dwordx4 v4, s[52:55], m0 offen offset:3072 sc1 lds
s_add_u32 m0, m0, 16384
v_exp_f32 v40, v40
v_exp_f32 v41, v41
v_cvt_pk_bf16_f32 v114, v36, v37
v_exp_f32 v42, v42
v_cvt_pk_bf16_f32 v115, v38, v39
v_exp_f32 v43, v43
v_exp_f32 v44, v44
v_mfma_f32_32x32x16_bf16 v[96:111], v[124:127], v[112:115], v[96:111]
ds_read_b128 v[124:127], v7 offset:6080
v_exp_f32 v45, v45
v_exp_f32 v46, v46
v_cvt_pk_bf16_f32 v116, v40, v41
v_exp_f32 v47, v47
v_cvt_pk_bf16_f32 v117, v42, v43
v_cvt_pk_bf16_f32 v118, v44, v45
v_cvt_pk_bf16_f32 v119, v46, v47
v_mfma_f32_32x32x8_bf16 v[32:47], v[132:133], v[154:155], v[64:79]
v_exp_f32 v16, v16
v_exp_f32 v17, v17
v_exp_f32 v18, v18
v_exp_f32 v19, v19
v_mfma_f32_32x32x16_bf16 v[96:111], v[128:131], v[116:119], v[96:111]
ds_read_b128 v[128:131], v7 offset:6688
buffer_load_dwordx4 v4, s[52:55], m0 offen sc1 lds
buffer_load_dwordx4 v4, s[52:55], m0 offen offset:1024 sc1 lds
v_exp_f32 v20, v20
v_exp_f32 v21, v21
v_cvt_pk_bf16_f32 v112, v16, v17
v_exp_f32 v22, v22
v_cvt_pk_bf16_f32 v113, v18, v19
v_exp_f32 v23, v23
v_mfma_f32_32x32x16_bf16 v[32:47], v[120:123], v[140:143], v[32:47]
s_waitcnt lgkmcnt(1)
ds_read_b128 v[120:123], v4 offset:6144
v_exp_f32 v24, v24
v_exp_f32 v25, v25
v_cvt_pk_bf16_f32 v114, v20, v21
v_exp_f32 v26, v26
v_cvt_pk_bf16_f32 v115, v22, v23
v_exp_f32 v27, v27
v_exp_f32 v28, v28
v_mfma_f32_32x32x16_bf16 v[80:95], v[124:127], v[112:115], v[80:95]
s_waitcnt lgkmcnt(1)
v_exp_f32 v29, v29
v_exp_f32 v30, v30
v_cvt_pk_bf16_f32 v116, v24, v25
v_exp_f32 v31, v31
v_cvt_pk_bf16_f32 v117, v26, v27
v_cvt_pk_bf16_f32 v118, v28, v29
v_cvt_pk_bf16_f32 v119, v30, v31
v_mfma_f32_32x32x8_bf16 v[16:31], v[132:133], v[148:149], v[48:63]
v_exp_f32 v32, v32
v_exp_f32 v33, v33
v_exp_f32 v34, v34
v_exp_f32 v35, v35
v_mfma_f32_32x32x16_bf16 v[80:95], v[128:131], v[116:119], v[80:95]
s_waitcnt lgkmcnt(0)
buffer_load_dwordx4 v4, s[52:55], m0 offen offset:2048 sc1 lds
buffer_load_dwordx4 v4, s[52:55], m0 offen offset:3072 sc1 lds
v_exp_f32 v36, v36
v_exp_f32 v37, v37
v_cvt_pk_bf16_f32 v112, v32, v33
v_exp_f32 v38, v38
v_cvt_pk_bf16_f32 v113, v34, v35
v_exp_f32 v39, v39
v_mfma_f32_32x32x16_bf16 v[16:31], v[120:123], v[136:139], v[16:31]
v_exp_f32 v40, v40
v_exp_f32 v41, v41
v_cvt_pk_bf16_f32 v114, v36, v37
v_exp_f32 v42, v42
v_cvt_pk_bf16_f32 v115, v38, v39
v_exp_f32 v43, v43
v_exp_f32 v44, v44
v_mfma_f32_32x32x16_bf16 v[96:111], v[124:127], v[112:115], v[96:111]
ds_read_b128 v[124:127], v7 offset:7296
v_exp_f32 v45, v45
v_exp_f32 v46, v46
v_cvt_pk_bf16_f32 v116, v40, v41
v_exp_f32 v47, v47
v_cvt_pk_bf16_f32 v117, v42, v43
v_cvt_pk_bf16_f32 v118, v44, v45
v_cvt_pk_bf16_f32 v119, v46, v47
v_mfma_f32_32x32x8_bf16 v[32:47], v[132:133], v[156:157], v[64:79]
v_exp_f32 v16, v16
v_exp_f32 v17, v17
v_exp_f32 v18, v18
v_exp_f32 v19, v19
v_mfma_f32_32x32x16_bf16 v[96:111], v[128:131], v[116:119], v[96:111]
ds_read_b128 v[128:131], v7 offset:7904
v_exp_f32 v20, v20
v_exp_f32 v21, v21
v_cvt_pk_bf16_f32 v112, v16, v17
v_exp_f32 v22, v22
v_cvt_pk_bf16_f32 v113, v18, v19
v_exp_f32 v23, v23
v_mfma_f32_32x32x16_bf16 v[32:47], v[120:123], v[140:143], v[32:47]
s_waitcnt lgkmcnt(1)
ds_read_b128 v[120:123], v4 offset:7168
v_exp_f32 v24, v24
v_exp_f32 v25, v25
v_cvt_pk_bf16_f32 v114, v20, v21
v_exp_f32 v26, v26
v_cvt_pk_bf16_f32 v115, v22, v23
v_exp_f32 v27, v27
v_exp_f32 v28, v28
v_mfma_f32_32x32x16_bf16 v[80:95], v[124:127], v[112:115], v[80:95]
s_waitcnt lgkmcnt(1)
v_exp_f32 v29, v29
v_exp_f32 v30, v30
v_cvt_pk_bf16_f32 v116, v24, v25
v_exp_f32 v31, v31
v_cvt_pk_bf16_f32 v117, v26, v27
v_cvt_pk_bf16_f32 v118, v28, v29
v_cvt_pk_bf16_f32 v119, v30, v31
s_waitcnt vmcnt(4)
s_barrier
v_mfma_f32_32x32x8_bf16 v[16:31], v[132:133], v[150:151], v[48:63]
v_exp_f32 v32, v32
v_exp_f32 v33, v33
v_exp_f32 v34, v34
v_exp_f32 v35, v35
v_mfma_f32_32x32x16_bf16 v[80:95], v[128:131], v[116:119], v[80:95]
s_waitcnt lgkmcnt(0)
v_exp_f32 v36, v36
v_exp_f32 v37, v37
v_cvt_pk_bf16_f32 v112, v32, v33
v_exp_f32 v38, v38
v_cvt_pk_bf16_f32 v113, v34, v35
v_exp_f32 v39, v39
v_mfma_f32_32x32x16_bf16 v[16:31], v[120:123], v[136:139], v[16:31]
v_exp_f32 v40, v40
v_exp_f32 v41, v41
v_cvt_pk_bf16_f32 v114, v36, v37
v_exp_f32 v42, v42
v_cvt_pk_bf16_f32 v115, v38, v39
v_exp_f32 v43, v43
v_exp_f32 v44, v44
v_mfma_f32_32x32x16_bf16 v[96:111], v[124:127], v[112:115], v[96:111]
ds_read_b128 v[124:127], v7 offset:8512
v_exp_f32 v45, v45
v_exp_f32 v46, v46
v_cvt_pk_bf16_f32 v116, v40, v41
v_exp_f32 v47, v47
v_cvt_pk_bf16_f32 v117, v42, v43
v_cvt_pk_bf16_f32 v118, v44, v45
v_cvt_pk_bf16_f32 v119, v46, v47
s_and_b32 s96, s33, 31
s_lshl_b32 s96, s96, 2
s_add_u32 s96, s96, s39
s_mul_i32 s96, s96, 5248
v_min_u32 v166, 40, v1
v_lshlrev_b32 v166, 7, v166
v_add_u32 v166, s96, v166
v_min_u32 v166, 663424, v166
v_mfma_f32_32x32x8_bf16 v[32:47], v[132:133], v[158:159], v[64:79]
v_exp_f32 v16, v16
v_exp_f32 v17, v17
v_exp_f32 v18, v18
v_exp_f32 v19, v19
v_mfma_f32_32x32x16_bf16 v[96:111], v[128:131], v[116:119], v[96:111]
ds_read_b128 v[128:131], v7 offset:9120
ds_read_b64 v[132:133], v10 offset:18944
global_load_dword v167, v166, s[20:21]
v_exp_f32 v20, v20
v_exp_f32 v21, v21
v_cvt_pk_bf16_f32 v112, v16, v17
v_exp_f32 v22, v22
v_cvt_pk_bf16_f32 v113, v18, v19
v_exp_f32 v23, v23
v_mfma_f32_32x32x16_bf16 v[32:47], v[120:123], v[140:143], v[32:47]
s_waitcnt lgkmcnt(2)
ds_read_b128 v[120:123], v4 offset:18944
v_exp_f32 v24, v24
v_exp_f32 v25, v25
v_cvt_pk_bf16_f32 v114, v20, v21
v_exp_f32 v26, v26
v_cvt_pk_bf16_f32 v115, v22, v23
v_exp_f32 v27, v27
v_exp_f32 v28, v28
v_mfma_f32_32x32x16_bf16 v[80:95], v[124:127], v[112:115], v[80:95]
s_waitcnt lgkmcnt(1)
v_exp_f32 v29, v29
v_exp_f32 v30, v30
v_cvt_pk_bf16_f32 v116, v24, v25
v_exp_f32 v31, v31
v_cvt_pk_bf16_f32 v117, v26, v27
v_cvt_pk_bf16_f32 v118, v28, v29
v_cvt_pk_bf16_f32 v119, v30, v31
v_mfma_f32_32x32x8_bf16 v[16:31], v[132:133], v[144:145], v[48:63]
v_exp_f32 v32, v32
v_exp_f32 v33, v33
v_exp_f32 v34, v34
v_exp_f32 v35, v35
v_mfma_f32_32x32x16_bf16 v[80:95], v[128:131], v[116:119], v[80:95]
s_waitcnt lgkmcnt(0)
v_exp_f32 v36, v36
v_exp_f32 v37, v37
v_cvt_pk_bf16_f32 v112, v32, v33
v_exp_f32 v38, v38
v_cvt_pk_bf16_f32 v113, v34, v35
v_exp_f32 v39, v39
v_mfma_f32_32x32x16_bf16 v[16:31], v[120:123], v[136:139], v[16:31]
v_exp_f32 v40, v40
v_exp_f32 v41, v41
v_cvt_pk_bf16_f32 v114, v36, v37
v_exp_f32 v42, v42
v_cvt_pk_bf16_f32 v115, v38, v39
v_exp_f32 v43, v43
v_exp_f32 v44, v44
v_mfma_f32_32x32x16_bf16 v[96:111], v[124:127], v[112:115], v[96:111]
ds_read_b128 v[124:127], v7 offset:18944
v_exp_f32 v45, v45
v_exp_f32 v46, v46
v_cvt_pk_bf16_f32 v116, v40, v41
v_exp_f32 v47, v47
v_cvt_pk_bf16_f32 v117, v42, v43
v_cvt_pk_bf16_f32 v118, v44, v45
v_cvt_pk_bf16_f32 v119, v46, v47
v_mfma_f32_32x32x8_bf16 v[32:47], v[132:133], v[152:153], v[64:79]
v_exp_f32 v16, v16
v_exp_f32 v17, v17
v_exp_f32 v18, v18
v_exp_f32 v19, v19
v_mfma_f32_32x32x16_bf16 v[96:111], v[128:131], v[116:119], v[96:111]
ds_read_b128 v[128:131], v7 offset:19552
global_load_dwordx4 a[0:3], v224, s[22:23]
global_load_dwordx4 a[4:7], v225, s[22:23]
v_exp_f32 v20, v20
v_exp_f32 v21, v21
v_cvt_pk_bf16_f32 v112, v16, v17
v_exp_f32 v22, v22
v_cvt_pk_bf16_f32 v113, v18, v19
v_exp_f32 v23, v23
v_mfma_f32_32x32x16_bf16 v[32:47], v[120:123], v[140:143], v[32:47]
s_waitcnt lgkmcnt(1)
ds_read_b128 v[120:123], v4 offset:19968
v_exp_f32 v24, v24
v_exp_f32 v25, v25
v_cvt_pk_bf16_f32 v114, v20, v21
v_exp_f32 v26, v26
v_cvt_pk_bf16_f32 v115, v22, v23
v_exp_f32 v27, v27
v_exp_f32 v28, v28
v_mfma_f32_32x32x16_bf16 v[80:95], v[124:127], v[112:115], v[80:95]
s_waitcnt lgkmcnt(1)
v_exp_f32 v29, v29
v_exp_f32 v30, v30
v_cvt_pk_bf16_f32 v116, v24, v25
v_exp_f32 v31, v31
v_cvt_pk_bf16_f32 v117, v26, v27
v_cvt_pk_bf16_f32 v118, v28, v29
v_cvt_pk_bf16_f32 v119, v30, v31
v_mfma_f32_32x32x8_bf16 v[16:31], v[132:133], v[146:147], v[48:63]
v_exp_f32 v32, v32
v_exp_f32 v33, v33
v_exp_f32 v34, v34
v_exp_f32 v35, v35
v_mfma_f32_32x32x16_bf16 v[80:95], v[128:131], v[116:119], v[80:95]
s_waitcnt lgkmcnt(0)
v_exp_f32 v36, v36
v_exp_f32 v37, v37
v_cvt_pk_bf16_f32 v112, v32, v33
v_exp_f32 v38, v38
v_cvt_pk_bf16_f32 v113, v34, v35
v_exp_f32 v39, v39
v_mfma_f32_32x32x16_bf16 v[16:31], v[120:123], v[136:139], v[16:31]
v_exp_f32 v40, v40
v_exp_f32 v41, v41
v_cvt_pk_bf16_f32 v114, v36, v37
v_exp_f32 v42, v42
v_cvt_pk_bf16_f32 v115, v38, v39
v_exp_f32 v43, v43
v_exp_f32 v44, v44
v_mfma_f32_32x32x16_bf16 v[96:111], v[124:127], v[112:115], v[96:111]
ds_read_b128 v[124:127], v7 offset:20160
v_exp_f32 v45, v45
v_exp_f32 v46, v46
v_cvt_pk_bf16_f32 v116, v40, v41
v_exp_f32 v47, v47
v_cvt_pk_bf16_f32 v117, v42, v43
v_cvt_pk_bf16_f32 v118, v44, v45
v_cvt_pk_bf16_f32 v119, v46, v47
v_mfma_f32_32x32x8_bf16 v[32:47], v[132:133], v[154:155], v[64:79]
v_exp_f32 v16, v16
v_exp_f32 v17, v17
v_exp_f32 v18, v18
v_exp_f32 v19, v19
v_mfma_f32_32x32x16_bf16 v[96:111], v[128:131], v[116:119], v[96:111]
ds_read_b128 v[128:131], v7 offset:20768
v_exp_f32 v20, v20
v_exp_f32 v21, v21
v_cvt_pk_bf16_f32 v112, v16, v17
v_exp_f32 v22, v22
v_cvt_pk_bf16_f32 v113, v18, v19
v_exp_f32 v23, v23
v_mfma_f32_32x32x16_bf16 v[32:47], v[120:123], v[140:143], v[32:47]
s_waitcnt lgkmcnt(1)
ds_read_b128 v[120:123], v4 offset:20992
v_exp_f32 v24, v24
v_exp_f32 v25, v25
v_cvt_pk_bf16_f32 v114, v20, v21
v_exp_f32 v26, v26
v_cvt_pk_bf16_f32 v115, v22, v23
v_exp_f32 v27, v27
v_exp_f32 v28, v28
v_mfma_f32_32x32x16_bf16 v[80:95], v[124:127], v[112:115], v[80:95]
s_waitcnt lgkmcnt(1)
v_exp_f32 v29, v29
v_exp_f32 v30, v30
v_cvt_pk_bf16_f32 v116, v24, v25
v_exp_f32 v31, v31
v_cvt_pk_bf16_f32 v117, v26, v27
v_cvt_pk_bf16_f32 v118, v28, v29
v_cvt_pk_bf16_f32 v119, v30, v31
v_mfma_f32_32x32x8_bf16 v[16:31], v[132:133], v[148:149], v[48:63]
v_exp_f32 v32, v32
v_exp_f32 v33, v33
v_exp_f32 v34, v34
v_exp_f32 v35, v35
v_mfma_f32_32x32x16_bf16 v[80:95], v[128:131], v[116:119], v[80:95]
LW0_1_%=:
s_cbranch_execz LW0_5_%=
s_waitcnt lgkmcnt(0)
global_load_dwordx4 a[8:11], v226, s[22:23]
global_load_dwordx4 a[12:15], v227, s[22:23]
v_exp_f32 v36, v36
v_exp_f32 v37, v37
v_cvt_pk_bf16_f32 v112, v32, v33
v_exp_f32 v38, v38
v_cvt_pk_bf16_f32 v113, v34, v35
v_exp_f32 v39, v39
v_mfma_f32_32x32x16_bf16 v[16:31], v[120:123], v[136:139], v[16:31]
v_exp_f32 v40, v40
v_exp_f32 v41, v41
v_cvt_pk_bf16_f32 v114, v36, v37
v_exp_f32 v42, v42
v_cvt_pk_bf16_f32 v115, v38, v39
v_exp_f32 v43, v43
v_exp_f32 v44, v44
v_mfma_f32_32x32x16_bf16 v[96:111], v[124:127], v[112:115], v[96:111]
ds_read_b128 v[124:127], v7 offset:21376
v_exp_f32 v45, v45
v_exp_f32 v46, v46
v_cvt_pk_bf16_f32 v116, v40, v41
v_exp_f32 v47, v47
v_cvt_pk_bf16_f32 v117, v42, v43
v_cvt_pk_bf16_f32 v118, v44, v45
v_cvt_pk_bf16_f32 v119, v46, v47
v_mfma_f32_32x32x8_bf16 v[32:47], v[132:133], v[156:157], v[64:79]
v_exp_f32 v16, v16
v_exp_f32 v17, v17
v_exp_f32 v18, v18
v_exp_f32 v19, v19
v_mfma_f32_32x32x16_bf16 v[96:111], v[128:131], v[116:119], v[96:111]
ds_read_b128 v[128:131], v7 offset:21984
v_exp_f32 v20, v20
v_exp_f32 v21, v21
v_cvt_pk_bf16_f32 v112, v16, v17
v_exp_f32 v22, v22
v_cvt_pk_bf16_f32 v113, v18, v19
v_exp_f32 v23, v23
v_mfma_f32_32x32x16_bf16 v[32:47], v[120:123], v[140:143], v[32:47]
)"
R"(s_waitcnt lgkmcnt(1)
ds_read_b128 v[120:123], v4 offset:22016
v_exp_f32 v24, v24
v_exp_f32 v25, v25
v_cvt_pk_bf16_f32 v114, v20, v21
v_exp_f32 v26, v26
v_cvt_pk_bf16_f32 v115, v22, v23
v_exp_f32 v27, v27
v_exp_f32 v28, v28
v_mfma_f32_32x32x16_bf16 v[80:95], v[124:127], v[112:115], v[80:95]
s_waitcnt lgkmcnt(1)
v_exp_f32 v29, v29
v_exp_f32 v30, v30
v_cvt_pk_bf16_f32 v116, v24, v25
v_exp_f32 v31, v31
v_cvt_pk_bf16_f32 v117, v26, v27
v_cvt_pk_bf16_f32 v118, v28, v29
v_cvt_pk_bf16_f32 v119, v30, v31
v_mfma_f32_32x32x8_bf16 v[16:31], v[132:133], v[150:151], v[48:63]
v_exp_f32 v32, v32
v_exp_f32 v33, v33
v_exp_f32 v34, v34
v_exp_f32 v35, v35
v_mfma_f32_32x32x16_bf16 v[80:95], v[128:131], v[116:119], v[80:95]
s_waitcnt lgkmcnt(0)
v_exp_f32 v36, v36
v_exp_f32 v37, v37
v_cvt_pk_bf16_f32 v112, v32, v33
v_exp_f32 v38, v38
v_cvt_pk_bf16_f32 v113, v34, v35
v_exp_f32 v39, v39
v_mfma_f32_32x32x16_bf16 v[16:31], v[120:123], v[136:139], v[16:31]
v_exp_f32 v40, v40
v_exp_f32 v41, v41
v_cvt_pk_bf16_f32 v114, v36, v37
v_exp_f32 v42, v42
v_cvt_pk_bf16_f32 v115, v38, v39
v_exp_f32 v43, v43
v_exp_f32 v44, v44
v_mfma_f32_32x32x16_bf16 v[96:111], v[124:127], v[112:115], v[96:111]
ds_read_b128 v[124:127], v7 offset:22592
v_exp_f32 v45, v45
v_exp_f32 v46, v46
v_cvt_pk_bf16_f32 v116, v40, v41
v_exp_f32 v47, v47
v_cvt_pk_bf16_f32 v117, v42, v43
v_cvt_pk_bf16_f32 v118, v44, v45
v_cvt_pk_bf16_f32 v119, v46, v47
v_mfma_f32_32x32x8_bf16 v[32:47], v[132:133], v[158:159], v[64:79]
v_exp_f32 v16, v16
v_exp_f32 v17, v17
v_exp_f32 v18, v18
v_exp_f32 v19, v19
v_mfma_f32_32x32x16_bf16 v[96:111], v[128:131], v[116:119], v[96:111]
ds_read_b128 v[128:131], v7 offset:23200
ds_read_b64 v[132:133], v10 offset:19456
global_load_dwordx4 a[16:19], v228, s[6:7]
global_load_dwordx4 a[20:23], v229, s[6:7]
v_exp_f32 v20, v20
v_exp_f32 v21, v21
v_cvt_pk_bf16_f32 v112, v16, v17
v_exp_f32 v22, v22
v_cvt_pk_bf16_f32 v113, v18, v19
v_exp_f32 v23, v23
v_mfma_f32_32x32x16_bf16 v[32:47], v[120:123], v[140:143], v[32:47]
s_waitcnt lgkmcnt(2)
ds_read_b128 v[120:123], v4 offset:23040
v_exp_f32 v24, v24
v_exp_f32 v25, v25
v_cvt_pk_bf16_f32 v114, v20, v21
v_exp_f32 v26, v26
v_cvt_pk_bf16_f32 v115, v22, v23
v_exp_f32 v27, v27
v_exp_f32 v28, v28
v_mfma_f32_32x32x16_bf16 v[80:95], v[124:127], v[112:115], v[80:95]
s_waitcnt lgkmcnt(1)
v_exp_f32 v29, v29
v_exp_f32 v30, v30
v_cvt_pk_bf16_f32 v116, v24, v25
v_exp_f32 v31, v31
v_cvt_pk_bf16_f32 v117, v26, v27
v_cvt_pk_bf16_f32 v118, v28, v29
v_cvt_pk_bf16_f32 v119, v30, v31
v_mfma_f32_32x32x8_bf16 v[16:31], v[132:133], v[144:145], v[48:63]
v_exp_f32 v32, v32
v_exp_f32 v33, v33
v_exp_f32 v34, v34
v_exp_f32 v35, v35
v_mfma_f32_32x32x16_bf16 v[80:95], v[128:131], v[116:119], v[80:95]
s_waitcnt lgkmcnt(0)
v_exp_f32 v36, v36
v_exp_f32 v37, v37
v_cvt_pk_bf16_f32 v112, v32, v33
v_exp_f32 v38, v38
v_cvt_pk_bf16_f32 v113, v34, v35
v_exp_f32 v39, v39
v_mfma_f32_32x32x16_bf16 v[16:31], v[120:123], v[136:139], v[16:31]
v_exp_f32 v40, v40
v_exp_f32 v41, v41
v_cvt_pk_bf16_f32 v114, v36, v37
v_exp_f32 v42, v42
v_cvt_pk_bf16_f32 v115, v38, v39
v_exp_f32 v43, v43
v_exp_f32 v44, v44
v_mfma_f32_32x32x16_bf16 v[96:111], v[124:127], v[112:115], v[96:111]
ds_read_b128 v[124:127], v7 offset:23808
v_exp_f32 v45, v45
v_exp_f32 v46, v46
v_cvt_pk_bf16_f32 v116, v40, v41
v_exp_f32 v47, v47
v_cvt_pk_bf16_f32 v117, v42, v43
v_cvt_pk_bf16_f32 v118, v44, v45
v_cvt_pk_bf16_f32 v119, v46, v47
v_mfma_f32_32x32x8_bf16 v[32:47], v[132:133], v[152:153], v[64:79]
v_exp_f32 v16, v16
v_exp_f32 v17, v17
v_exp_f32 v18, v18
v_exp_f32 v19, v19
v_mfma_f32_32x32x16_bf16 v[96:111], v[128:131], v[116:119], v[96:111]
ds_read_b128 v[128:131], v7 offset:24416
v_exp_f32 v20, v20
v_exp_f32 v21, v21
v_cvt_pk_bf16_f32 v112, v16, v17
v_exp_f32 v22, v22
v_cvt_pk_bf16_f32 v113, v18, v19
v_exp_f32 v23, v23
v_mfma_f32_32x32x16_bf16 v[32:47], v[120:123], v[140:143], v[32:47]
s_waitcnt lgkmcnt(1)
ds_read_b128 v[120:123], v4 offset:24064
v_exp_f32 v24, v24
v_exp_f32 v25, v25
v_cvt_pk_bf16_f32 v114, v20, v21
v_exp_f32 v26, v26
v_cvt_pk_bf16_f32 v115, v22, v23
v_exp_f32 v27, v27
v_exp_f32 v28, v28
v_mfma_f32_32x32x16_bf16 v[80:95], v[124:127], v[112:115], v[80:95]
s_waitcnt lgkmcnt(1)
v_exp_f32 v29, v29
v_exp_f32 v30, v30
v_cvt_pk_bf16_f32 v116, v24, v25
v_exp_f32 v31, v31
v_cvt_pk_bf16_f32 v117, v26, v27
v_cvt_pk_bf16_f32 v118, v28, v29
v_cvt_pk_bf16_f32 v119, v30, v31
v_mfma_f32_32x32x8_bf16 v[16:31], v[132:133], v[146:147], v[48:63]
v_exp_f32 v32, v32
v_exp_f32 v33, v33
v_exp_f32 v34, v34
v_exp_f32 v35, v35
v_mfma_f32_32x32x16_bf16 v[80:95], v[128:131], v[116:119], v[80:95]
s_waitcnt lgkmcnt(0)
global_load_dwordx4 a[24:27], v230, s[6:7]
global_load_dwordx4 a[28:31], v231, s[6:7]
v_exp_f32 v36, v36
v_exp_f32 v37, v37
v_cvt_pk_bf16_f32 v112, v32, v33
v_exp_f32 v38, v38
v_cvt_pk_bf16_f32 v113, v34, v35
v_exp_f32 v39, v39
v_mfma_f32_32x32x16_bf16 v[16:31], v[120:123], v[136:139], v[16:31]
v_exp_f32 v40, v40
v_exp_f32 v41, v41
v_cvt_pk_bf16_f32 v114, v36, v37
v_exp_f32 v42, v42
v_cvt_pk_bf16_f32 v115, v38, v39
v_exp_f32 v43, v43
v_exp_f32 v44, v44
v_mfma_f32_32x32x16_bf16 v[96:111], v[124:127], v[112:115], v[96:111]
ds_read_b128 v[124:127], v7 offset:25024
v_exp_f32 v45, v45
v_exp_f32 v46, v46
v_cvt_pk_bf16_f32 v116, v40, v41
v_exp_f32 v47, v47
v_cvt_pk_bf16_f32 v117, v42, v43
v_cvt_pk_bf16_f32 v118, v44, v45
v_cvt_pk_bf16_f32 v119, v46, v47
v_mfma_f32_32x32x8_bf16 v[32:47], v[132:133], v[154:155], v[64:79]
v_exp_f32 v16, v16
v_exp_f32 v17, v17
v_exp_f32 v18, v18
v_exp_f32 v19, v19
v_mfma_f32_32x32x16_bf16 v[96:111], v[128:131], v[116:119], v[96:111]
ds_read_b128 v[128:131], v7 offset:25632
v_exp_f32 v20, v20
v_exp_f32 v21, v21
v_cvt_pk_bf16_f32 v112, v16, v17
v_exp_f32 v22, v22
v_cvt_pk_bf16_f32 v113, v18, v19
v_exp_f32 v23, v23
v_mfma_f32_32x32x16_bf16 v[32:47], v[120:123], v[140:143], v[32:47]
s_waitcnt lgkmcnt(1)
ds_read_b128 v[120:123], v4 offset:25088
v_exp_f32 v24, v24
v_exp_f32 v25, v25
v_cvt_pk_bf16_f32 v114, v20, v21
v_exp_f32 v26, v26
v_cvt_pk_bf16_f32 v115, v22, v23
v_exp_f32 v27, v27
v_exp_f32 v28, v28
v_mfma_f32_32x32x16_bf16 v[80:95], v[124:127], v[112:115], v[80:95]
s_waitcnt lgkmcnt(1)
v_exp_f32 v29, v29
v_exp_f32 v30, v30
v_cvt_pk_bf16_f32 v116, v24, v25
v_exp_f32 v31, v31
v_cvt_pk_bf16_f32 v117, v26, v27
v_cvt_pk_bf16_f32 v118, v28, v29
v_cvt_pk_bf16_f32 v119, v30, v31
v_mfma_f32_32x32x8_bf16 v[16:31], v[132:133], v[148:149], v[48:63]
v_exp_f32 v32, v32
v_exp_f32 v33, v33
v_exp_f32 v34, v34
v_exp_f32 v35, v35
v_mfma_f32_32x32x16_bf16 v[80:95], v[128:131], v[116:119], v[80:95]
s_waitcnt lgkmcnt(0)
v_exp_f32 v36, v36
v_exp_f32 v37, v37
v_cvt_pk_bf16_f32 v112, v32, v33
v_exp_f32 v38, v38
v_cvt_pk_bf16_f32 v113, v34, v35
v_exp_f32 v39, v39
v_mfma_f32_32x32x16_bf16 v[16:31], v[120:123], v[136:139], v[16:31]
v_exp_f32 v40, v40
v_exp_f32 v41, v41
v_cvt_pk_bf16_f32 v114, v36, v37
v_exp_f32 v42, v42
v_cvt_pk_bf16_f32 v115, v38, v39
v_exp_f32 v43, v43
v_exp_f32 v44, v44
v_mfma_f32_32x32x16_bf16 v[96:111], v[124:127], v[112:115], v[96:111]
ds_read_b128 v[124:127], v7 offset:26240
v_exp_f32 v45, v45
v_exp_f32 v46, v46
v_cvt_pk_bf16_f32 v116, v40, v41
v_exp_f32 v47, v47
v_cvt_pk_bf16_f32 v117, v42, v43
v_cvt_pk_bf16_f32 v118, v44, v45
v_cvt_pk_bf16_f32 v119, v46, v47
v_mfma_f32_32x32x8_bf16 v[32:47], v[132:133], v[156:157], v[64:79]
v_exp_f32 v16, v16
v_exp_f32 v17, v17
v_exp_f32 v18, v18
v_exp_f32 v19, v19
v_mfma_f32_32x32x16_bf16 v[96:111], v[128:131], v[116:119], v[96:111]
ds_read_b128 v[128:131], v7 offset:26848
global_load_dwordx4 a[32:35], v232, s[26:27]
global_load_dwordx4 a[36:39], v233, s[26:27]
v_exp_f32 v20, v20
v_exp_f32 v21, v21
v_cvt_pk_bf16_f32 v112, v16, v17
v_exp_f32 v22, v22
v_cvt_pk_bf16_f32 v113, v18, v19
v_exp_f32 v23, v23
v_mfma_f32_32x32x16_bf16 v[32:47], v[120:123], v[140:143], v[32:47]
s_waitcnt lgkmcnt(1)
ds_read_b128 v[120:123], v4 offset:26112
v_exp_f32 v24, v24
v_exp_f32 v25, v25
v_cvt_pk_bf16_f32 v114, v20, v21
v_exp_f32 v26, v26
v_cvt_pk_bf16_f32 v115, v22, v23
v_exp_f32 v27, v27
v_exp_f32 v28, v28
v_mfma_f32_32x32x16_bf16 v[80:95], v[124:127], v[112:115], v[80:95]
s_waitcnt lgkmcnt(1)
v_exp_f32 v29, v29
v_exp_f32 v30, v30
v_cvt_pk_bf16_f32 v116, v24, v25
v_exp_f32 v31, v31
v_cvt_pk_bf16_f32 v117, v26, v27
v_cvt_pk_bf16_f32 v118, v28, v29
v_cvt_pk_bf16_f32 v119, v30, v31
v_mfma_f32_32x32x8_bf16 v[16:31], v[132:133], v[150:151], v[48:63]
v_exp_f32 v32, v32
v_exp_f32 v33, v33
v_exp_f32 v34, v34
v_exp_f32 v35, v35
v_mfma_f32_32x32x16_bf16 v[80:95], v[128:131], v[116:119], v[80:95]
s_waitcnt lgkmcnt(0)
v_exp_f32 v36, v36
v_exp_f32 v37, v37
v_cvt_pk_bf16_f32 v112, v32, v33
v_exp_f32 v38, v38
v_cvt_pk_bf16_f32 v113, v34, v35
v_exp_f32 v39, v39
v_mfma_f32_32x32x16_bf16 v[16:31], v[120:123], v[136:139], v[16:31]
v_exp_f32 v40, v40
v_exp_f32 v41, v41
v_cvt_pk_bf16_f32 v114, v36, v37
v_exp_f32 v42, v42
v_cvt_pk_bf16_f32 v115, v38, v39
v_exp_f32 v43, v43
v_exp_f32 v44, v44
v_mfma_f32_32x32x16_bf16 v[96:111], v[124:127], v[112:115], v[96:111]
ds_read_b128 v[124:127], v7 offset:27456
v_exp_f32 v45, v45
v_exp_f32 v46, v46
v_cvt_pk_bf16_f32 v116, v40, v41
v_exp_f32 v47, v47
v_cvt_pk_bf16_f32 v117, v42, v43
v_cvt_pk_bf16_f32 v118, v44, v45
v_cvt_pk_bf16_f32 v119, v46, v47
v_mfma_f32_32x32x8_bf16 v[32:47], v[132:133], v[158:159], v[64:79]
v_exp_f32 v16, v16
v_exp_f32 v17, v17
v_exp_f32 v18, v18
v_exp_f32 v19, v19
v_mfma_f32_32x32x16_bf16 v[96:111], v[128:131], v[116:119], v[96:111]
ds_read_b128 v[128:131], v7 offset:28064
ds_read_b64 v[132:133], v10 offset:37888
v_exp_f32 v20, v20
v_exp_f32 v21, v21
v_cvt_pk_bf16_f32 v112, v16, v17
v_exp_f32 v22, v22
v_cvt_pk_bf16_f32 v113, v18, v19
v_exp_f32 v23, v23
v_mfma_f32_32x32x16_bf16 v[32:47], v[120:123], v[140:143], v[32:47]
s_waitcnt lgkmcnt(2)
ds_read_b128 v[120:123], v4 offset:37888
v_exp_f32 v24, v24
v_exp_f32 v25, v25
v_cvt_pk_bf16_f32 v114, v20, v21
v_exp_f32 v26, v26
v_cvt_pk_bf16_f32 v115, v22, v23
v_exp_f32 v27, v27
v_exp_f32 v28, v28
v_mfma_f32_32x32x16_bf16 v[80:95], v[124:127], v[112:115], v[80:95]
s_waitcnt lgkmcnt(1)
v_exp_f32 v29, v29
v_exp_f32 v30, v30
v_cvt_pk_bf16_f32 v116, v24, v25
v_exp_f32 v31, v31
v_cvt_pk_bf16_f32 v117, v26, v27
v_cvt_pk_bf16_f32 v118, v28, v29
v_cvt_pk_bf16_f32 v119, v30, v31
v_mfma_f32_32x32x8_bf16 v[16:31], v[132:133], v[144:145], v[48:63]
v_exp_f32 v32, v32
v_exp_f32 v33, v33
v_exp_f32 v34, v34
v_exp_f32 v35, v35
v_mfma_f32_32x32x16_bf16 v[80:95], v[128:131], v[116:119], v[80:95]
s_waitcnt lgkmcnt(0)
global_load_dwordx4 a[40:43], v234, s[26:27]
global_load_dwordx4 a[44:47], v235, s[26:27]
v_exp_f32 v36, v36
v_exp_f32 v37, v37
v_cvt_pk_bf16_f32 v112, v32, v33
v_exp_f32 v38, v38
v_cvt_pk_bf16_f32 v113, v34, v35
v_exp_f32 v39, v39
v_mfma_f32_32x32x16_bf16 v[16:31], v[120:123], v[136:139], v[16:31]
v_exp_f32 v40, v40
v_exp_f32 v41, v41
v_cvt_pk_bf16_f32 v114, v36, v37
v_exp_f32 v42, v42
v_cvt_pk_bf16_f32 v115, v38, v39
v_exp_f32 v43, v43
v_exp_f32 v44, v44
v_mfma_f32_32x32x16_bf16 v[96:111], v[124:127], v[112:115], v[96:111]
ds_read_b128 v[124:127], v7 offset:37888
v_exp_f32 v45, v45
v_exp_f32 v46, v46
v_cvt_pk_bf16_f32 v116, v40, v41
v_exp_f32 v47, v47
v_cvt_pk_bf16_f32 v117, v42, v43
v_cvt_pk_bf16_f32 v118, v44, v45
v_cvt_pk_bf16_f32 v119, v46, v47
v_mfma_f32_32x32x8_bf16 v[32:47], v[132:133], v[152:153], v[64:79]
v_exp_f32 v16, v16
v_exp_f32 v17, v17
v_exp_f32 v18, v18
v_exp_f32 v19, v19
v_mfma_f32_32x32x16_bf16 v[96:111], v[128:131], v[116:119], v[96:111]
ds_read_b128 v[128:131], v7 offset:38496
v_exp_f32 v20, v20
v_exp_f32 v21, v21
v_cvt_pk_bf16_f32 v112, v16, v17
v_exp_f32 v22, v22
v_cvt_pk_bf16_f32 v113, v18, v19
v_exp_f32 v23, v23
v_mfma_f32_32x32x16_bf16 v[32:47], v[120:123], v[140:143], v[32:47]
s_waitcnt lgkmcnt(1)
ds_read_b128 v[120:123], v4 offset:38912
v_exp_f32 v24, v24
v_exp_f32 v25, v25
v_cvt_pk_bf16_f32 v114, v20, v21
v_exp_f32 v26, v26
v_cvt_pk_bf16_f32 v115, v22, v23
v_exp_f32 v27, v27
v_exp_f32 v28, v28
v_mfma_f32_32x32x16_bf16 v[80:95], v[124:127], v[112:115], v[80:95]
s_waitcnt lgkmcnt(1)
v_exp_f32 v29, v29
v_exp_f32 v30, v30
v_cvt_pk_bf16_f32 v116, v24, v25
v_exp_f32 v31, v31
v_cvt_pk_bf16_f32 v117, v26, v27
v_cvt_pk_bf16_f32 v118, v28, v29
v_cvt_pk_bf16_f32 v119, v30, v31
)"
R"(v_mfma_f32_32x32x8_bf16 v[16:31], v[132:133], v[146:147], v[48:63]
v_exp_f32 v32, v32
v_exp_f32 v33, v33
v_exp_f32 v34, v34
v_exp_f32 v35, v35
v_mfma_f32_32x32x16_bf16 v[80:95], v[128:131], v[116:119], v[80:95]
s_waitcnt lgkmcnt(0)
v_exp_f32 v36, v36
v_exp_f32 v37, v37
v_cvt_pk_bf16_f32 v112, v32, v33
v_exp_f32 v38, v38
v_cvt_pk_bf16_f32 v113, v34, v35
v_exp_f32 v39, v39
v_mfma_f32_32x32x16_bf16 v[16:31], v[120:123], v[136:139], v[16:31]
v_exp_f32 v40, v40
v_exp_f32 v41, v41
v_cvt_pk_bf16_f32 v114, v36, v37
v_exp_f32 v42, v42
v_cvt_pk_bf16_f32 v115, v38, v39
v_exp_f32 v43, v43
v_exp_f32 v44, v44
v_mfma_f32_32x32x16_bf16 v[96:111], v[124:127], v[112:115], v[96:111]
ds_read_b128 v[124:127], v7 offset:39104
v_exp_f32 v45, v45
v_exp_f32 v46, v46
v_cvt_pk_bf16_f32 v116, v40, v41
v_exp_f32 v47, v47
v_cvt_pk_bf16_f32 v117, v42, v43
v_cvt_pk_bf16_f32 v118, v44, v45
v_cvt_pk_bf16_f32 v119, v46, v47
v_mfma_f32_32x32x8_bf16 v[32:47], v[132:133], v[154:155], v[64:79]
v_exp_f32 v16, v16
v_exp_f32 v17, v17
v_exp_f32 v18, v18
v_exp_f32 v19, v19
v_mfma_f32_32x32x16_bf16 v[96:111], v[128:131], v[116:119], v[96:111]
ds_read_b128 v[128:131], v7 offset:39712
v_exp_f32 v20, v20
v_exp_f32 v21, v21
v_cvt_pk_bf16_f32 v112, v16, v17
v_exp_f32 v22, v22
v_cvt_pk_bf16_f32 v113, v18, v19
v_exp_f32 v23, v23
v_mfma_f32_32x32x16_bf16 v[32:47], v[120:123], v[140:143], v[32:47]
s_waitcnt lgkmcnt(1)
ds_read_b128 v[120:123], v4 offset:39936
v_exp_f32 v24, v24
v_exp_f32 v25, v25
v_cvt_pk_bf16_f32 v114, v20, v21
v_exp_f32 v26, v26
v_cvt_pk_bf16_f32 v115, v22, v23
v_exp_f32 v27, v27
v_exp_f32 v28, v28
v_mfma_f32_32x32x16_bf16 v[80:95], v[124:127], v[112:115], v[80:95]
s_waitcnt lgkmcnt(1)
v_exp_f32 v29, v29
v_exp_f32 v30, v30
v_cvt_pk_bf16_f32 v116, v24, v25
v_exp_f32 v31, v31
v_cvt_pk_bf16_f32 v117, v26, v27
v_cvt_pk_bf16_f32 v118, v28, v29
v_cvt_pk_bf16_f32 v119, v30, v31
v_mfma_f32_32x32x8_bf16 v[16:31], v[132:133], v[148:149], v[48:63]
v_exp_f32 v32, v32
v_exp_f32 v33, v33
v_exp_f32 v34, v34
v_exp_f32 v35, v35
v_mfma_f32_32x32x16_bf16 v[80:95], v[128:131], v[116:119], v[80:95]
s_waitcnt lgkmcnt(0)
v_exp_f32 v36, v36
v_exp_f32 v37, v37
v_cvt_pk_bf16_f32 v112, v32, v33
v_exp_f32 v38, v38
v_cvt_pk_bf16_f32 v113, v34, v35
v_exp_f32 v39, v39
v_mfma_f32_32x32x16_bf16 v[16:31], v[120:123], v[136:139], v[16:31]
v_exp_f32 v40, v40
v_exp_f32 v41, v41
v_cvt_pk_bf16_f32 v114, v36, v37
v_exp_f32 v42, v42
v_cvt_pk_bf16_f32 v115, v38, v39
v_exp_f32 v43, v43
v_exp_f32 v44, v44
v_mfma_f32_32x32x16_bf16 v[96:111], v[124:127], v[112:115], v[96:111]
ds_read_b128 v[124:127], v7 offset:40320
v_exp_f32 v45, v45
v_exp_f32 v46, v46
v_cvt_pk_bf16_f32 v116, v40, v41
v_exp_f32 v47, v47
v_cvt_pk_bf16_f32 v117, v42, v43
v_cvt_pk_bf16_f32 v118, v44, v45
v_cvt_pk_bf16_f32 v119, v46, v47
v_mfma_f32_32x32x8_bf16 v[32:47], v[132:133], v[156:157], v[64:79]
v_exp_f32 v16, v16
v_exp_f32 v17, v17
v_exp_f32 v18, v18
v_exp_f32 v19, v19
v_mfma_f32_32x32x16_bf16 v[96:111], v[128:131], v[116:119], v[96:111]
ds_read_b128 v[128:131], v7 offset:40928
v_exp_f32 v20, v20
v_exp_f32 v21, v21
v_cvt_pk_bf16_f32 v112, v16, v17
v_exp_f32 v22, v22
v_cvt_pk_bf16_f32 v113, v18, v19
v_exp_f32 v23, v23
v_mfma_f32_32x32x16_bf16 v[32:47], v[120:123], v[140:143], v[32:47]
s_waitcnt lgkmcnt(1)
ds_read_b128 v[120:123], v4 offset:40960
v_exp_f32 v24, v24
v_exp_f32 v25, v25
v_cvt_pk_bf16_f32 v114, v20, v21
v_exp_f32 v26, v26
v_cvt_pk_bf16_f32 v115, v22, v23
v_exp_f32 v27, v27
v_exp_f32 v28, v28
v_mfma_f32_32x32x16_bf16 v[80:95], v[124:127], v[112:115], v[80:95]
s_waitcnt lgkmcnt(1)
v_exp_f32 v29, v29
v_exp_f32 v30, v30
v_cvt_pk_bf16_f32 v116, v24, v25
v_exp_f32 v31, v31
v_cvt_pk_bf16_f32 v117, v26, v27
v_cvt_pk_bf16_f32 v118, v28, v29
v_cvt_pk_bf16_f32 v119, v30, v31
v_mfma_f32_32x32x8_bf16 v[16:31], v[132:133], v[150:151], v[48:63]
v_exp_f32 v32, v32
v_exp_f32 v33, v33
v_exp_f32 v34, v34
v_exp_f32 v35, v35
v_mfma_f32_32x32x16_bf16 v[80:95], v[128:131], v[116:119], v[80:95]
s_waitcnt lgkmcnt(0)
v_exp_f32 v36, v36
v_exp_f32 v37, v37
v_cvt_pk_bf16_f32 v112, v32, v33
v_exp_f32 v38, v38
v_cvt_pk_bf16_f32 v113, v34, v35
v_exp_f32 v39, v39
v_mfma_f32_32x32x16_bf16 v[16:31], v[120:123], v[136:139], v[16:31]
v_exp_f32 v40, v40
v_exp_f32 v41, v41
v_cvt_pk_bf16_f32 v114, v36, v37
v_exp_f32 v42, v42
v_cvt_pk_bf16_f32 v115, v38, v39
v_exp_f32 v43, v43
v_exp_f32 v44, v44
v_mfma_f32_32x32x16_bf16 v[96:111], v[124:127], v[112:115], v[96:111]
ds_read_b128 v[124:127], v7 offset:41536
v_exp_f32 v45, v45
v_exp_f32 v46, v46
v_cvt_pk_bf16_f32 v116, v40, v41
v_exp_f32 v47, v47
v_cvt_pk_bf16_f32 v117, v42, v43
v_cvt_pk_bf16_f32 v118, v44, v45
v_cvt_pk_bf16_f32 v119, v46, v47
v_mfma_f32_32x32x8_bf16 v[32:47], v[132:133], v[158:159], v[64:79]
v_exp_f32 v16, v16
v_exp_f32 v17, v17
v_exp_f32 v18, v18
v_exp_f32 v19, v19
v_mfma_f32_32x32x16_bf16 v[96:111], v[128:131], v[116:119], v[96:111]
ds_read_b128 v[128:131], v7 offset:42144
ds_read_b64 v[132:133], v10 offset:38400
v_exp_f32 v20, v20
v_exp_f32 v21, v21
v_cvt_pk_bf16_f32 v112, v16, v17
v_exp_f32 v22, v22
v_cvt_pk_bf16_f32 v113, v18, v19
v_exp_f32 v23, v23
v_mfma_f32_32x32x16_bf16 v[32:47], v[120:123], v[140:143], v[32:47]
s_waitcnt lgkmcnt(2)
ds_read_b128 v[120:123], v4 offset:41984
v_exp_f32 v24, v24
v_exp_f32 v25, v25
v_cvt_pk_bf16_f32 v114, v20, v21
v_exp_f32 v26, v26
v_cvt_pk_bf16_f32 v115, v22, v23
v_exp_f32 v27, v27
v_exp_f32 v28, v28
v_mfma_f32_32x32x16_bf16 v[80:95], v[124:127], v[112:115], v[80:95]
s_waitcnt lgkmcnt(1)
v_exp_f32 v29, v29
v_exp_f32 v30, v30
v_cvt_pk_bf16_f32 v116, v24, v25
v_exp_f32 v31, v31
v_cvt_pk_bf16_f32 v117, v26, v27
v_cvt_pk_bf16_f32 v118, v28, v29
v_cvt_pk_bf16_f32 v119, v30, v31
v_mfma_f32_32x32x8_bf16 v[16:31], v[132:133], v[144:145], v[48:63]
v_exp_f32 v32, v32
v_exp_f32 v33, v33
v_exp_f32 v34, v34
v_exp_f32 v35, v35
v_mfma_f32_32x32x16_bf16 v[80:95], v[128:131], v[116:119], v[80:95]
s_waitcnt lgkmcnt(0)
v_exp_f32 v36, v36
v_exp_f32 v37, v37
v_cvt_pk_bf16_f32 v112, v32, v33
v_exp_f32 v38, v38
v_cvt_pk_bf16_f32 v113, v34, v35
v_exp_f32 v39, v39
v_mfma_f32_32x32x16_bf16 v[16:31], v[120:123], v[136:139], v[16:31]
v_exp_f32 v40, v40
v_exp_f32 v41, v41
v_cvt_pk_bf16_f32 v114, v36, v37
v_exp_f32 v42, v42
v_cvt_pk_bf16_f32 v115, v38, v39
v_exp_f32 v43, v43
v_exp_f32 v44, v44
v_mfma_f32_32x32x16_bf16 v[96:111], v[124:127], v[112:115], v[96:111]
ds_read_b128 v[124:127], v7 offset:42752
v_exp_f32 v45, v45
v_exp_f32 v46, v46
v_cvt_pk_bf16_f32 v116, v40, v41
v_exp_f32 v47, v47
v_cvt_pk_bf16_f32 v117, v42, v43
v_cvt_pk_bf16_f32 v118, v44, v45
v_cvt_pk_bf16_f32 v119, v46, v47
v_mfma_f32_32x32x8_bf16 v[32:47], v[132:133], v[152:153], v[64:79]
v_exp_f32 v16, v16
v_exp_f32 v17, v17
v_exp_f32 v18, v18
v_exp_f32 v19, v19
v_mfma_f32_32x32x16_bf16 v[96:111], v[128:131], v[116:119], v[96:111]
ds_read_b128 v[128:131], v7 offset:43360
v_exp_f32 v20, v20
v_exp_f32 v21, v21
v_cvt_pk_bf16_f32 v112, v16, v17
v_exp_f32 v22, v22
v_cvt_pk_bf16_f32 v113, v18, v19
v_exp_f32 v23, v23
v_mfma_f32_32x32x16_bf16 v[32:47], v[120:123], v[140:143], v[32:47]
s_waitcnt lgkmcnt(1)
ds_read_b128 v[120:123], v4 offset:43008
v_exp_f32 v24, v24
v_exp_f32 v25, v25
v_cvt_pk_bf16_f32 v114, v20, v21
v_exp_f32 v26, v26
v_cvt_pk_bf16_f32 v115, v22, v23
v_exp_f32 v27, v27
v_exp_f32 v28, v28
v_mfma_f32_32x32x16_bf16 v[80:95], v[124:127], v[112:115], v[80:95]
s_waitcnt lgkmcnt(1)
v_exp_f32 v29, v29
v_exp_f32 v30, v30
v_cvt_pk_bf16_f32 v116, v24, v25
v_exp_f32 v31, v31
v_cvt_pk_bf16_f32 v117, v26, v27
v_cvt_pk_bf16_f32 v118, v28, v29
v_cvt_pk_bf16_f32 v119, v30, v31
v_mfma_f32_32x32x8_bf16 v[16:31], v[132:133], v[146:147], v[48:63]
v_exp_f32 v32, v32
v_exp_f32 v33, v33
v_exp_f32 v34, v34
v_exp_f32 v35, v35
v_mfma_f32_32x32x16_bf16 v[80:95], v[128:131], v[116:119], v[80:95]
s_waitcnt lgkmcnt(0)
v_exp_f32 v36, v36
v_exp_f32 v37, v37
v_cvt_pk_bf16_f32 v112, v32, v33
v_exp_f32 v38, v38
v_cvt_pk_bf16_f32 v113, v34, v35
v_exp_f32 v39, v39
v_mfma_f32_32x32x16_bf16 v[16:31], v[120:123], v[136:139], v[16:31]
v_exp_f32 v40, v40
v_exp_f32 v41, v41
v_cvt_pk_bf16_f32 v114, v36, v37
v_exp_f32 v42, v42
v_cvt_pk_bf16_f32 v115, v38, v39
v_exp_f32 v43, v43
v_exp_f32 v44, v44
v_mfma_f32_32x32x16_bf16 v[96:111], v[124:127], v[112:115], v[96:111]
LW0_2_%=:
s_cbranch_execz LW0_6_%=
ds_read_b128 v[124:127], v7 offset:43968
v_exp_f32 v45, v45
v_exp_f32 v46, v46
v_cvt_pk_bf16_f32 v116, v40, v41
v_exp_f32 v47, v47
v_cvt_pk_bf16_f32 v117, v42, v43
v_cvt_pk_bf16_f32 v118, v44, v45
v_cvt_pk_bf16_f32 v119, v46, v47
v_mfma_f32_32x32x8_bf16 v[32:47], v[132:133], v[154:155], v[64:79]
v_exp_f32 v16, v16
v_exp_f32 v17, v17
v_exp_f32 v18, v18
v_exp_f32 v19, v19
v_mfma_f32_32x32x16_bf16 v[96:111], v[128:131], v[116:119], v[96:111]
ds_read_b128 v[128:131], v7 offset:44576
v_exp_f32 v20, v20
v_exp_f32 v21, v21
v_cvt_pk_bf16_f32 v112, v16, v17
v_exp_f32 v22, v22
v_cvt_pk_bf16_f32 v113, v18, v19
v_exp_f32 v23, v23
v_mfma_f32_32x32x16_bf16 v[32:47], v[120:123], v[140:143], v[32:47]
s_waitcnt lgkmcnt(1)
ds_read_b128 v[120:123], v4 offset:44032
v_exp_f32 v24, v24
v_exp_f32 v25, v25
v_cvt_pk_bf16_f32 v114, v20, v21
v_exp_f32 v26, v26
v_cvt_pk_bf16_f32 v115, v22, v23
v_exp_f32 v27, v27
v_exp_f32 v28, v28
v_mfma_f32_32x32x16_bf16 v[80:95], v[124:127], v[112:115], v[80:95]
s_waitcnt lgkmcnt(1)
v_exp_f32 v29, v29
v_exp_f32 v30, v30
v_cvt_pk_bf16_f32 v116, v24, v25
v_exp_f32 v31, v31
v_cvt_pk_bf16_f32 v117, v26, v27
v_cvt_pk_bf16_f32 v118, v28, v29
v_cvt_pk_bf16_f32 v119, v30, v31
v_mfma_f32_32x32x8_bf16 v[16:31], v[132:133], v[148:149], v[48:63]
v_exp_f32 v32, v32
v_exp_f32 v33, v33
v_exp_f32 v34, v34
v_exp_f32 v35, v35
v_mfma_f32_32x32x16_bf16 v[80:95], v[128:131], v[116:119], v[80:95]
s_waitcnt lgkmcnt(0)
v_exp_f32 v36, v36
v_exp_f32 v37, v37
v_cvt_pk_bf16_f32 v112, v32, v33
v_exp_f32 v38, v38
v_cvt_pk_bf16_f32 v113, v34, v35
v_exp_f32 v39, v39
v_mfma_f32_32x32x16_bf16 v[16:31], v[120:123], v[136:139], v[16:31]
v_exp_f32 v40, v40
v_exp_f32 v41, v41
v_cvt_pk_bf16_f32 v114, v36, v37
v_exp_f32 v42, v42
v_cvt_pk_bf16_f32 v115, v38, v39
v_exp_f32 v43, v43
v_exp_f32 v44, v44
v_mfma_f32_32x32x16_bf16 v[96:111], v[124:127], v[112:115], v[96:111]
ds_read_b128 v[124:127], v7 offset:45184
v_exp_f32 v45, v45
v_exp_f32 v46, v46
v_cvt_pk_bf16_f32 v116, v40, v41
v_exp_f32 v47, v47
v_cvt_pk_bf16_f32 v117, v42, v43
v_cvt_pk_bf16_f32 v118, v44, v45
v_cvt_pk_bf16_f32 v119, v46, v47
v_mfma_f32_32x32x8_bf16 v[32:47], v[132:133], v[156:157], v[64:79]
v_exp_f32 v16, v16
v_exp_f32 v17, v17
v_exp_f32 v18, v18
v_exp_f32 v19, v19
v_mfma_f32_32x32x16_bf16 v[96:111], v[128:131], v[116:119], v[96:111]
ds_read_b128 v[128:131], v7 offset:45792
s_waitcnt vmcnt(11)
ds_write_b128 v242, a[0:3]
s_waitcnt vmcnt(10)
ds_write_b128 v242, a[4:7] offset:1024
v_exp_f32 v20, v20
v_exp_f32 v21, v21
v_cvt_pk_bf16_f32 v112, v16, v17
v_exp_f32 v22, v22
v_cvt_pk_bf16_f32 v113, v18, v19
v_exp_f32 v23, v23
v_mfma_f32_32x32x16_bf16 v[32:47], v[120:123], v[140:143], v[32:47]
s_waitcnt lgkmcnt(3)
ds_read_b128 v[120:123], v4 offset:45056
ds_read_b64_tr_b16 v[248:249], v243
ds_read_b64_tr_b16 v[250:251], v243 offset:512
ds_read_b64_tr_b16 v[252:253], v243 offset:64
ds_read_b64_tr_b16 v[254:255], v243 offset:576
v_exp_f32 v24, v24
v_exp_f32 v25, v25
v_cvt_pk_bf16_f32 v114, v20, v21
v_exp_f32 v26, v26
v_cvt_pk_bf16_f32 v115, v22, v23
v_exp_f32 v27, v27
v_exp_f32 v28, v28
v_mfma_f32_32x32x16_bf16 v[80:95], v[124:127], v[112:115], v[80:95]
s_waitcnt lgkmcnt(7)
v_exp_f32 v29, v29
v_exp_f32 v30, v30
v_cvt_pk_bf16_f32 v116, v24, v25
v_exp_f32 v31, v31
v_cvt_pk_bf16_f32 v117, v26, v27
v_cvt_pk_bf16_f32 v118, v28, v29
v_cvt_pk_bf16_f32 v119, v30, v31
v_mfma_f32_32x32x8_bf16 v[16:31], v[132:133], v[150:151], v[48:63]
v_exp_f32 v32, v32
v_exp_f32 v33, v33
v_exp_f32 v34, v34
v_exp_f32 v35, v35
v_mfma_f32_32x32x16_bf16 v[80:95], v[128:131], v[116:119], v[80:95]
s_waitcnt lgkmcnt(4)
s_waitcnt lgkmcnt(2)
global_store_dwordx4 v236, v[248:251], s[50:51] sc1
v_exp_f32 v36, v36
v_exp_f32 v37, v37
v_cvt_pk_bf16_f32 v112, v32, v33
v_exp_f32 v38, v38
v_cvt_pk_bf16_f32 v113, v34, v35
v_exp_f32 v39, v39
v_mfma_f32_32x32x16_bf16 v[16:31], v[120:123], v[136:139], v[16:31]
v_exp_f32 v40, v40
)"
R"(v_exp_f32 v41, v41
v_cvt_pk_bf16_f32 v114, v36, v37
v_exp_f32 v42, v42
v_cvt_pk_bf16_f32 v115, v38, v39
v_exp_f32 v43, v43
v_exp_f32 v44, v44
v_mfma_f32_32x32x16_bf16 v[96:111], v[124:127], v[112:115], v[96:111]
ds_read_b128 v[124:127], v7 offset:46400
v_exp_f32 v45, v45
v_exp_f32 v46, v46
v_cvt_pk_bf16_f32 v116, v40, v41
v_exp_f32 v47, v47
v_cvt_pk_bf16_f32 v117, v42, v43
v_cvt_pk_bf16_f32 v118, v44, v45
v_cvt_pk_bf16_f32 v119, v46, v47
v_mfma_f32_32x32x8_bf16 v[32:47], v[132:133], v[158:159], v[64:79]
v_exp_f32 v16, v16
v_exp_f32 v17, v17
v_exp_f32 v18, v18
v_exp_f32 v19, v19
v_mfma_f32_32x32x16_bf16 v[96:111], v[128:131], v[116:119], v[96:111]
ds_read_b128 v[128:131], v7 offset:47008
ds_read_b64 v[132:133], v11
s_waitcnt lgkmcnt(3)
global_store_dwordx4 v237, v[252:255], s[50:51] sc1
v_exp_f32 v20, v20
v_exp_f32 v21, v21
v_cvt_pk_bf16_f32 v112, v16, v17
v_exp_f32 v22, v22
v_cvt_pk_bf16_f32 v113, v18, v19
v_exp_f32 v23, v23
v_mfma_f32_32x32x16_bf16 v[32:47], v[120:123], v[140:143], v[32:47]
s_waitcnt lgkmcnt(2)
ds_read_b128 v[120:123], v5
s_waitcnt vmcnt(11)
ds_write_b128 v242, a[8:11]
s_waitcnt vmcnt(10)
ds_write_b128 v242, a[12:15] offset:1024
v_exp_f32 v24, v24
v_exp_f32 v25, v25
v_cvt_pk_bf16_f32 v114, v20, v21
v_exp_f32 v26, v26
v_cvt_pk_bf16_f32 v115, v22, v23
v_exp_f32 v27, v27
v_exp_f32 v28, v28
v_mfma_f32_32x32x16_bf16 v[80:95], v[124:127], v[112:115], v[80:95]
s_waitcnt lgkmcnt(3)
ds_read_b64_tr_b16 v[248:249], v243
ds_read_b64_tr_b16 v[250:251], v243 offset:512
ds_read_b64_tr_b16 v[252:253], v243 offset:64
ds_read_b64_tr_b16 v[254:255], v243 offset:576
v_exp_f32 v29, v29
v_exp_f32 v30, v30
v_cvt_pk_bf16_f32 v116, v24, v25
v_exp_f32 v31, v31
v_cvt_pk_bf16_f32 v117, v26, v27
v_cvt_pk_bf16_f32 v118, v28, v29
v_cvt_pk_bf16_f32 v119, v30, v31
v_mfma_f32_32x32x8_bf16 v[16:31], v[132:133], v[144:145], v[48:63]
v_exp_f32 v32, v32
v_exp_f32 v33, v33
v_exp_f32 v34, v34
v_exp_f32 v35, v35
v_mfma_f32_32x32x16_bf16 v[80:95], v[128:131], v[116:119], v[80:95]
s_waitcnt lgkmcnt(6)
s_waitcnt lgkmcnt(2)
global_store_dwordx4 v236, v[248:251], s[50:51] offset:512 sc1
v_exp_f32 v36, v36
v_exp_f32 v37, v37
v_cvt_pk_bf16_f32 v112, v32, v33
v_exp_f32 v38, v38
v_cvt_pk_bf16_f32 v113, v34, v35
v_exp_f32 v39, v39
v_mfma_f32_32x32x16_bf16 v[16:31], v[120:123], v[136:139], v[16:31]
v_exp_f32 v40, v40
v_exp_f32 v41, v41
v_cvt_pk_bf16_f32 v114, v36, v37
v_exp_f32 v42, v42
v_cvt_pk_bf16_f32 v115, v38, v39
v_exp_f32 v43, v43
v_exp_f32 v44, v44
v_mfma_f32_32x32x16_bf16 v[96:111], v[124:127], v[112:115], v[96:111]
ds_read_b128 v[124:127], v8
v_exp_f32 v45, v45
v_exp_f32 v46, v46
v_cvt_pk_bf16_f32 v116, v40, v41
v_exp_f32 v47, v47
v_cvt_pk_bf16_f32 v117, v42, v43
v_cvt_pk_bf16_f32 v118, v44, v45
v_cvt_pk_bf16_f32 v119, v46, v47
v_mfma_f32_32x32x8_bf16 v[32:47], v[132:133], v[152:153], v[64:79]
v_exp_f32 v16, v16
v_exp_f32 v17, v17
v_exp_f32 v18, v18
v_exp_f32 v19, v19
v_mfma_f32_32x32x16_bf16 v[96:111], v[128:131], v[116:119], v[96:111]
ds_read_b128 v[128:131], v8 offset:608
v_exp_f32 v20, v20
v_exp_f32 v21, v21
v_cvt_pk_bf16_f32 v112, v16, v17
v_exp_f32 v22, v22
v_cvt_pk_bf16_f32 v113, v18, v19
v_exp_f32 v23, v23
v_mfma_f32_32x32x16_bf16 v[32:47], v[120:123], v[140:143], v[32:47]
s_waitcnt lgkmcnt(1)
ds_read_b128 v[120:123], v5 offset:1024
v_exp_f32 v24, v24
v_exp_f32 v25, v25
v_cvt_pk_bf16_f32 v114, v20, v21
v_exp_f32 v26, v26
v_cvt_pk_bf16_f32 v115, v22, v23
v_exp_f32 v27, v27
v_exp_f32 v28, v28
v_mfma_f32_32x32x16_bf16 v[80:95], v[124:127], v[112:115], v[80:95]
s_waitcnt lgkmcnt(1)
v_exp_f32 v29, v29
v_exp_f32 v30, v30
v_cvt_pk_bf16_f32 v116, v24, v25
v_exp_f32 v31, v31
v_cvt_pk_bf16_f32 v117, v26, v27
v_cvt_pk_bf16_f32 v118, v28, v29
v_cvt_pk_bf16_f32 v119, v30, v31
v_mfma_f32_32x32x8_bf16 v[16:31], v[132:133], v[146:147], v[48:63]
v_exp_f32 v32, v32
v_exp_f32 v33, v33
v_exp_f32 v34, v34
v_exp_f32 v35, v35
v_mfma_f32_32x32x16_bf16 v[80:95], v[128:131], v[116:119], v[80:95]
s_waitcnt lgkmcnt(0)
global_store_dwordx4 v237, v[252:255], s[50:51] offset:512 sc1
v_exp_f32 v36, v36
v_exp_f32 v37, v37
v_cvt_pk_bf16_f32 v112, v32, v33
v_exp_f32 v38, v38
v_cvt_pk_bf16_f32 v113, v34, v35
v_exp_f32 v39, v39
v_mfma_f32_32x32x16_bf16 v[16:31], v[120:123], v[136:139], v[16:31]
s_waitcnt vmcnt(11)
ds_write_b128 v242, a[16:19]
s_waitcnt vmcnt(10)
ds_write_b128 v242, a[20:23] offset:1024
v_exp_f32 v40, v40
v_exp_f32 v41, v41
v_cvt_pk_bf16_f32 v114, v36, v37
v_exp_f32 v42, v42
v_cvt_pk_bf16_f32 v115, v38, v39
v_exp_f32 v43, v43
v_exp_f32 v44, v44
v_mfma_f32_32x32x16_bf16 v[96:111], v[124:127], v[112:115], v[96:111]
ds_read_b128 v[124:127], v8 offset:1216
ds_read_b64_tr_b16 v[248:249], v243
ds_read_b64_tr_b16 v[250:251], v243 offset:512
ds_read_b64_tr_b16 v[252:253], v243 offset:64
ds_read_b64_tr_b16 v[254:255], v243 offset:576
v_exp_f32 v45, v45
v_exp_f32 v46, v46
v_cvt_pk_bf16_f32 v116, v40, v41
v_exp_f32 v47, v47
v_cvt_pk_bf16_f32 v117, v42, v43
v_cvt_pk_bf16_f32 v118, v44, v45
v_cvt_pk_bf16_f32 v119, v46, v47
v_mfma_f32_32x32x8_bf16 v[32:47], v[132:133], v[154:155], v[64:79]
v_exp_f32 v16, v16
v_exp_f32 v17, v17
v_exp_f32 v18, v18
v_exp_f32 v19, v19
v_mfma_f32_32x32x16_bf16 v[96:111], v[128:131], v[116:119], v[96:111]
ds_read_b128 v[128:131], v8 offset:1824
s_waitcnt lgkmcnt(3)
global_store_dwordx4 v238, v[248:251], s[50:51] sc1
v_exp_f32 v20, v20
v_exp_f32 v21, v21
v_cvt_pk_bf16_f32 v112, v16, v17
v_exp_f32 v22, v22
v_cvt_pk_bf16_f32 v113, v18, v19
v_exp_f32 v23, v23
v_mfma_f32_32x32x16_bf16 v[32:47], v[120:123], v[140:143], v[32:47]
ds_read_b128 v[120:123], v5 offset:2048
v_exp_f32 v24, v24
v_exp_f32 v25, v25
v_cvt_pk_bf16_f32 v114, v20, v21
v_exp_f32 v26, v26
v_cvt_pk_bf16_f32 v115, v22, v23
v_exp_f32 v27, v27
v_exp_f32 v28, v28
v_mfma_f32_32x32x16_bf16 v[80:95], v[124:127], v[112:115], v[80:95]
s_waitcnt lgkmcnt(1)
v_exp_f32 v29, v29
v_exp_f32 v30, v30
v_cvt_pk_bf16_f32 v116, v24, v25
v_exp_f32 v31, v31
v_cvt_pk_bf16_f32 v117, v26, v27
v_cvt_pk_bf16_f32 v118, v28, v29
v_cvt_pk_bf16_f32 v119, v30, v31
v_mfma_f32_32x32x8_bf16 v[16:31], v[132:133], v[148:149], v[48:63]
v_exp_f32 v32, v32
v_exp_f32 v33, v33
v_exp_f32 v34, v34
v_exp_f32 v35, v35
v_mfma_f32_32x32x16_bf16 v[80:95], v[128:131], v[116:119], v[80:95]
s_waitcnt lgkmcnt(0)
v_exp_f32 v36, v36
v_exp_f32 v37, v37
v_cvt_pk_bf16_f32 v112, v32, v33
v_exp_f32 v38, v38
v_cvt_pk_bf16_f32 v113, v34, v35
v_exp_f32 v39, v39
v_mfma_f32_32x32x16_bf16 v[16:31], v[120:123], v[136:139], v[16:31]
v_exp_f32 v40, v40
v_exp_f32 v41, v41
v_cvt_pk_bf16_f32 v114, v36, v37
v_exp_f32 v42, v42
v_cvt_pk_bf16_f32 v115, v38, v39
v_exp_f32 v43, v43
v_exp_f32 v44, v44
v_mfma_f32_32x32x16_bf16 v[96:111], v[124:127], v[112:115], v[96:111]
ds_read_b128 v[124:127], v8 offset:2432
v_exp_f32 v45, v45
v_exp_f32 v46, v46
v_cvt_pk_bf16_f32 v116, v40, v41
v_exp_f32 v47, v47
v_cvt_pk_bf16_f32 v117, v42, v43
v_cvt_pk_bf16_f32 v118, v44, v45
v_cvt_pk_bf16_f32 v119, v46, v47
v_mfma_f32_32x32x8_bf16 v[32:47], v[132:133], v[156:157], v[64:79]
v_exp_f32 v16, v16
v_exp_f32 v17, v17
v_exp_f32 v18, v18
v_exp_f32 v19, v19
v_mfma_f32_32x32x16_bf16 v[96:111], v[128:131], v[116:119], v[96:111]
ds_read_b128 v[128:131], v8 offset:3040
global_store_dwordx4 v239, v[252:255], s[50:51] sc1
v_exp_f32 v20, v20
v_exp_f32 v21, v21
v_cvt_pk_bf16_f32 v112, v16, v17
v_exp_f32 v22, v22
v_cvt_pk_bf16_f32 v113, v18, v19
v_exp_f32 v23, v23
v_mfma_f32_32x32x16_bf16 v[32:47], v[120:123], v[140:143], v[32:47]
s_waitcnt lgkmcnt(1)
ds_read_b128 v[120:123], v5 offset:3072
s_waitcnt vmcnt(11)
ds_write_b128 v242, a[24:27]
s_waitcnt vmcnt(10)
ds_write_b128 v242, a[28:31] offset:1024
v_exp_f32 v24, v24
v_exp_f32 v25, v25
v_cvt_pk_bf16_f32 v114, v20, v21
v_exp_f32 v26, v26
v_cvt_pk_bf16_f32 v115, v22, v23
v_exp_f32 v27, v27
v_exp_f32 v28, v28
v_mfma_f32_32x32x16_bf16 v[80:95], v[124:127], v[112:115], v[80:95]
s_waitcnt lgkmcnt(3)
ds_read_b64_tr_b16 v[248:249], v243
ds_read_b64_tr_b16 v[250:251], v243 offset:512
ds_read_b64_tr_b16 v[252:253], v243 offset:64
ds_read_b64_tr_b16 v[254:255], v243 offset:576
v_exp_f32 v29, v29
v_exp_f32 v30, v30
v_cvt_pk_bf16_f32 v116, v24, v25
v_exp_f32 v31, v31
v_cvt_pk_bf16_f32 v117, v26, v27
v_cvt_pk_bf16_f32 v118, v28, v29
v_cvt_pk_bf16_f32 v119, v30, v31
v_mfma_f32_32x32x8_bf16 v[16:31], v[132:133], v[150:151], v[48:63]
v_exp_f32 v32, v32
v_exp_f32 v33, v33
v_exp_f32 v34, v34
v_exp_f32 v35, v35
v_mfma_f32_32x32x16_bf16 v[80:95], v[128:131], v[116:119], v[80:95]
s_waitcnt lgkmcnt(6)
s_waitcnt lgkmcnt(2)
global_store_dwordx4 v238, v[248:251], s[50:51] offset:512 sc1
v_exp_f32 v36, v36
v_exp_f32 v37, v37
v_cvt_pk_bf16_f32 v112, v32, v33
v_exp_f32 v38, v38
v_cvt_pk_bf16_f32 v113, v34, v35
v_exp_f32 v39, v39
v_mfma_f32_32x32x16_bf16 v[16:31], v[120:123], v[136:139], v[16:31]
v_exp_f32 v40, v40
v_exp_f32 v41, v41
v_cvt_pk_bf16_f32 v114, v36, v37
v_exp_f32 v42, v42
v_cvt_pk_bf16_f32 v115, v38, v39
v_exp_f32 v43, v43
v_exp_f32 v44, v44
v_mfma_f32_32x32x16_bf16 v[96:111], v[124:127], v[112:115], v[96:111]
ds_read_b128 v[124:127], v8 offset:3648
v_exp_f32 v45, v45
v_exp_f32 v46, v46
v_cvt_pk_bf16_f32 v116, v40, v41
v_exp_f32 v47, v47
v_cvt_pk_bf16_f32 v117, v42, v43
v_cvt_pk_bf16_f32 v118, v44, v45
v_cvt_pk_bf16_f32 v119, v46, v47
v_mfma_f32_32x32x8_bf16 v[32:47], v[132:133], v[158:159], v[64:79]
v_exp_f32 v16, v16
v_exp_f32 v17, v17
v_exp_f32 v18, v18
v_exp_f32 v19, v19
v_mfma_f32_32x32x16_bf16 v[96:111], v[128:131], v[116:119], v[96:111]
ds_read_b128 v[128:131], v8 offset:4256
ds_read_b64 v[132:133], v11 offset:512
s_waitcnt lgkmcnt(3)
global_store_dwordx4 v239, v[252:255], s[50:51] offset:512 sc1
v_exp_f32 v20, v20
v_exp_f32 v21, v21
v_cvt_pk_bf16_f32 v112, v16, v17
v_exp_f32 v22, v22
v_cvt_pk_bf16_f32 v113, v18, v19
v_exp_f32 v23, v23
v_mfma_f32_32x32x16_bf16 v[32:47], v[120:123], v[140:143], v[32:47]
s_waitcnt lgkmcnt(2)
ds_read_b128 v[120:123], v5 offset:4096
v_exp_f32 v24, v24
v_exp_f32 v25, v25
v_cvt_pk_bf16_f32 v114, v20, v21
v_exp_f32 v26, v26
v_cvt_pk_bf16_f32 v115, v22, v23
v_exp_f32 v27, v27
v_exp_f32 v28, v28
v_mfma_f32_32x32x16_bf16 v[80:95], v[124:127], v[112:115], v[80:95]
s_waitcnt lgkmcnt(1)
v_exp_f32 v29, v29
v_exp_f32 v30, v30
v_cvt_pk_bf16_f32 v116, v24, v25
v_exp_f32 v31, v31
v_cvt_pk_bf16_f32 v117, v26, v27
v_cvt_pk_bf16_f32 v118, v28, v29
v_cvt_pk_bf16_f32 v119, v30, v31
v_mfma_f32_32x32x8_bf16 v[16:31], v[132:133], v[144:145], v[48:63]
v_exp_f32 v32, v32
v_exp_f32 v33, v33
v_exp_f32 v34, v34
v_exp_f32 v35, v35
v_mfma_f32_32x32x16_bf16 v[80:95], v[128:131], v[116:119], v[80:95]
s_waitcnt lgkmcnt(0)
s_waitcnt vmcnt(11)
ds_write_b128 v242, a[32:35]
s_waitcnt vmcnt(10)
ds_write_b128 v242, a[36:39] offset:1024
v_exp_f32 v36, v36
v_exp_f32 v37, v37
v_cvt_pk_bf16_f32 v112, v32, v33
v_exp_f32 v38, v38
v_cvt_pk_bf16_f32 v113, v34, v35
v_exp_f32 v39, v39
v_mfma_f32_32x32x16_bf16 v[16:31], v[120:123], v[136:139], v[16:31]
ds_read_b64_tr_b16 v[248:249], v243
ds_read_b64_tr_b16 v[250:251], v243 offset:512
ds_read_b64_tr_b16 v[252:253], v243 offset:64
ds_read_b64_tr_b16 v[254:255], v243 offset:576
v_exp_f32 v40, v40
v_exp_f32 v41, v41
v_cvt_pk_bf16_f32 v114, v36, v37
v_exp_f32 v42, v42
v_cvt_pk_bf16_f32 v115, v38, v39
v_exp_f32 v43, v43
v_exp_f32 v44, v44
v_mfma_f32_32x32x16_bf16 v[96:111], v[124:127], v[112:115], v[96:111]
ds_read_b128 v[124:127], v8 offset:4864
v_exp_f32 v45, v45
v_exp_f32 v46, v46
v_cvt_pk_bf16_f32 v116, v40, v41
v_exp_f32 v47, v47
v_cvt_pk_bf16_f32 v117, v42, v43
v_cvt_pk_bf16_f32 v118, v44, v45
v_cvt_pk_bf16_f32 v119, v46, v47
v_mfma_f32_32x32x8_bf16 v[32:47], v[132:133], v[152:153], v[64:79]
v_exp_f32 v16, v16
v_exp_f32 v17, v17
v_exp_f32 v18, v18
v_exp_f32 v19, v19
v_mfma_f32_32x32x16_bf16 v[96:111], v[128:131], v[116:119], v[96:111]
ds_read_b128 v[128:131], v8 offset:5472
s_waitcnt lgkmcnt(4)
s_mov_b32 exec_lo, s45
s_mov_b32 exec_hi, s45
global_store_dwordx4 v240, v[248:251], s[50:51] sc1
s_mov_b64 exec, -1
v_exp_f32 v20, v20
v_exp_f32 v21, v21
v_cvt_pk_bf16_f32 v112, v16, v17
v_exp_f32 v22, v22
v_cvt_pk_bf16_f32 v113, v18, v19
v_exp_f32 v23, v23
v_mfma_f32_32x32x16_bf16 v[32:47], v[120:123], v[140:143], v[32:47]
s_waitcnt lgkmcnt(1)
ds_read_b128 v[120:123], v5 offset:5120
v_exp_f32 v24, v24
v_exp_f32 v25, v25
v_cvt_pk_bf16_f32 v114, v20, v21
v_exp_f32 v26, v26
v_cvt_pk_bf16_f32 v115, v22, v23
v_exp_f32 v27, v27
v_exp_f32 v28, v28
v_mfma_f32_32x32x16_bf16 v[80:95], v[124:127], v[112:115], v[80:95]
s_waitcnt lgkmcnt(1)
)"
R"(v_exp_f32 v29, v29
v_exp_f32 v30, v30
v_cvt_pk_bf16_f32 v116, v24, v25
v_exp_f32 v31, v31
v_cvt_pk_bf16_f32 v117, v26, v27
v_cvt_pk_bf16_f32 v118, v28, v29
v_cvt_pk_bf16_f32 v119, v30, v31
v_mfma_f32_32x32x8_bf16 v[16:31], v[132:133], v[146:147], v[48:63]
v_exp_f32 v32, v32
v_exp_f32 v33, v33
v_exp_f32 v34, v34
v_exp_f32 v35, v35
v_mfma_f32_32x32x16_bf16 v[80:95], v[128:131], v[116:119], v[80:95]
s_waitcnt lgkmcnt(0)
s_mov_b32 exec_lo, s45
s_mov_b32 exec_hi, s45
global_store_dwordx4 v241, v[252:255], s[50:51] sc1
s_mov_b64 exec, -1
v_exp_f32 v36, v36
v_exp_f32 v37, v37
v_cvt_pk_bf16_f32 v112, v32, v33
v_exp_f32 v38, v38
v_cvt_pk_bf16_f32 v113, v34, v35
v_exp_f32 v39, v39
v_mfma_f32_32x32x16_bf16 v[16:31], v[120:123], v[136:139], v[16:31]
v_exp_f32 v40, v40
v_exp_f32 v41, v41
v_cvt_pk_bf16_f32 v114, v36, v37
v_exp_f32 v42, v42
v_cvt_pk_bf16_f32 v115, v38, v39
v_exp_f32 v43, v43
v_exp_f32 v44, v44
v_mfma_f32_32x32x16_bf16 v[96:111], v[124:127], v[112:115], v[96:111]
ds_read_b128 v[124:127], v8 offset:6080
v_exp_f32 v45, v45
v_exp_f32 v46, v46
v_cvt_pk_bf16_f32 v116, v40, v41
v_exp_f32 v47, v47
v_cvt_pk_bf16_f32 v117, v42, v43
v_cvt_pk_bf16_f32 v118, v44, v45
v_cvt_pk_bf16_f32 v119, v46, v47
v_mfma_f32_32x32x8_bf16 v[32:47], v[132:133], v[154:155], v[64:79]
v_exp_f32 v16, v16
v_exp_f32 v17, v17
v_exp_f32 v18, v18
v_exp_f32 v19, v19
v_mfma_f32_32x32x16_bf16 v[96:111], v[128:131], v[116:119], v[96:111]
ds_read_b128 v[128:131], v8 offset:6688
s_waitcnt vmcnt(11)
ds_write_b128 v242, a[40:43]
s_waitcnt vmcnt(10)
ds_write_b128 v242, a[44:47] offset:1024
v_exp_f32 v20, v20
v_exp_f32 v21, v21
v_cvt_pk_bf16_f32 v112, v16, v17
v_exp_f32 v22, v22
v_cvt_pk_bf16_f32 v113, v18, v19
v_exp_f32 v23, v23
v_mfma_f32_32x32x16_bf16 v[32:47], v[120:123], v[140:143], v[32:47]
s_waitcnt lgkmcnt(3)
ds_read_b128 v[120:123], v5 offset:6144
ds_read_b64_tr_b16 v[248:249], v243
ds_read_b64_tr_b16 v[250:251], v243 offset:512
ds_read_b64_tr_b16 v[252:253], v243 offset:64
ds_read_b64_tr_b16 v[254:255], v243 offset:576
v_exp_f32 v24, v24
v_exp_f32 v25, v25
v_cvt_pk_bf16_f32 v114, v20, v21
v_exp_f32 v26, v26
v_cvt_pk_bf16_f32 v115, v22, v23
v_exp_f32 v27, v27
v_exp_f32 v28, v28
v_mfma_f32_32x32x16_bf16 v[80:95], v[124:127], v[112:115], v[80:95]
s_waitcnt lgkmcnt(7)
v_exp_f32 v29, v29
v_exp_f32 v30, v30
v_cvt_pk_bf16_f32 v116, v24, v25
v_exp_f32 v31, v31
v_cvt_pk_bf16_f32 v117, v26, v27
v_cvt_pk_bf16_f32 v118, v28, v29
v_cvt_pk_bf16_f32 v119, v30, v31
v_mfma_f32_32x32x8_bf16 v[16:31], v[132:133], v[148:149], v[48:63]
v_exp_f32 v32, v32
v_exp_f32 v33, v33
v_exp_f32 v34, v34
v_exp_f32 v35, v35
v_mfma_f32_32x32x16_bf16 v[80:95], v[128:131], v[116:119], v[80:95]
s_waitcnt lgkmcnt(4)
s_waitcnt lgkmcnt(2)
s_mov_b32 exec_lo, s45
s_mov_b32 exec_hi, s45
global_store_dwordx4 v240, v[248:251], s[50:51] offset:512 sc1
s_mov_b64 exec, -1
v_exp_f32 v36, v36
v_exp_f32 v37, v37
v_cvt_pk_bf16_f32 v112, v32, v33
v_exp_f32 v38, v38
v_cvt_pk_bf16_f32 v113, v34, v35
v_exp_f32 v39, v39
v_mfma_f32_32x32x16_bf16 v[16:31], v[120:123], v[136:139], v[16:31]
v_exp_f32 v40, v40
v_exp_f32 v41, v41
v_cvt_pk_bf16_f32 v114, v36, v37
v_exp_f32 v42, v42
v_cvt_pk_bf16_f32 v115, v38, v39
v_exp_f32 v43, v43
v_exp_f32 v44, v44
v_mfma_f32_32x32x16_bf16 v[96:111], v[124:127], v[112:115], v[96:111]
ds_read_b128 v[124:127], v8 offset:7296
v_exp_f32 v45, v45
v_exp_f32 v46, v46
v_cvt_pk_bf16_f32 v116, v40, v41
v_exp_f32 v47, v47
v_cvt_pk_bf16_f32 v117, v42, v43
v_cvt_pk_bf16_f32 v118, v44, v45
v_cvt_pk_bf16_f32 v119, v46, v47
v_mfma_f32_32x32x8_bf16 v[32:47], v[132:133], v[156:157], v[64:79]
v_exp_f32 v16, v16
v_exp_f32 v17, v17
v_exp_f32 v18, v18
v_exp_f32 v19, v19
v_mfma_f32_32x32x16_bf16 v[96:111], v[128:131], v[116:119], v[96:111]
ds_read_b128 v[128:131], v8 offset:7904
s_waitcnt lgkmcnt(2)
s_mov_b32 exec_lo, s45
s_mov_b32 exec_hi, s45
global_store_dwordx4 v241, v[252:255], s[50:51] offset:512 sc1
s_mov_b64 exec, -1
v_exp_f32 v20, v20
v_exp_f32 v21, v21
v_cvt_pk_bf16_f32 v112, v16, v17
v_exp_f32 v22, v22
v_cvt_pk_bf16_f32 v113, v18, v19
v_exp_f32 v23, v23
v_mfma_f32_32x32x16_bf16 v[32:47], v[120:123], v[140:143], v[32:47]
s_waitcnt lgkmcnt(1)
ds_read_b128 v[120:123], v5 offset:7168
v_exp_f32 v24, v24
v_exp_f32 v25, v25
v_cvt_pk_bf16_f32 v114, v20, v21
v_exp_f32 v26, v26
v_cvt_pk_bf16_f32 v115, v22, v23
v_exp_f32 v27, v27
v_exp_f32 v28, v28
v_mfma_f32_32x32x16_bf16 v[80:95], v[124:127], v[112:115], v[80:95]
s_waitcnt lgkmcnt(1)
v_exp_f32 v29, v29
v_exp_f32 v30, v30
v_cvt_pk_bf16_f32 v116, v24, v25
v_exp_f32 v31, v31
v_cvt_pk_bf16_f32 v117, v26, v27
v_cvt_pk_bf16_f32 v118, v28, v29
v_cvt_pk_bf16_f32 v119, v30, v31
v_mfma_f32_32x32x8_bf16 v[16:31], v[132:133], v[150:151], v[48:63]
v_exp_f32 v32, v32
v_exp_f32 v33, v33
v_exp_f32 v34, v34
v_exp_f32 v35, v35
v_mfma_f32_32x32x16_bf16 v[80:95], v[128:131], v[116:119], v[80:95]
s_waitcnt lgkmcnt(0)
v_exp_f32 v36, v36
v_exp_f32 v37, v37
v_cvt_pk_bf16_f32 v112, v32, v33
v_exp_f32 v38, v38
v_cvt_pk_bf16_f32 v113, v34, v35
v_exp_f32 v39, v39
v_mfma_f32_32x32x16_bf16 v[16:31], v[120:123], v[136:139], v[16:31]
v_exp_f32 v40, v40
v_exp_f32 v41, v41
v_cvt_pk_bf16_f32 v114, v36, v37
v_exp_f32 v42, v42
v_cvt_pk_bf16_f32 v115, v38, v39
v_exp_f32 v43, v43
v_exp_f32 v44, v44
v_mfma_f32_32x32x16_bf16 v[96:111], v[124:127], v[112:115], v[96:111]
LW0_3_%=:
s_cbranch_execz LWEND0_%=
ds_read_b128 v[124:127], v8 offset:8512
v_exp_f32 v45, v45
v_exp_f32 v46, v46
v_cvt_pk_bf16_f32 v116, v40, v41
v_exp_f32 v47, v47
v_cvt_pk_bf16_f32 v117, v42, v43
v_cvt_pk_bf16_f32 v118, v44, v45
v_cvt_pk_bf16_f32 v119, v46, v47
v_mfma_f32_32x32x8_bf16 v[32:47], v[132:133], v[158:159], v[64:79]
v_exp_f32 v16, v16
v_exp_f32 v17, v17
v_exp_f32 v18, v18
v_exp_f32 v19, v19
v_mfma_f32_32x32x16_bf16 v[96:111], v[128:131], v[116:119], v[96:111]
ds_read_b128 v[128:131], v8 offset:9120
ds_read_b64 v[132:133], v11 offset:18944
v_exp_f32 v20, v20
v_exp_f32 v21, v21
v_cvt_pk_bf16_f32 v112, v16, v17
v_exp_f32 v22, v22
v_cvt_pk_bf16_f32 v113, v18, v19
v_exp_f32 v23, v23
v_mfma_f32_32x32x16_bf16 v[32:47], v[120:123], v[140:143], v[32:47]
s_waitcnt lgkmcnt(2)
ds_read_b128 v[120:123], v5 offset:18944
v_exp_f32 v24, v24
v_exp_f32 v25, v25
v_cvt_pk_bf16_f32 v114, v20, v21
v_exp_f32 v26, v26
v_cvt_pk_bf16_f32 v115, v22, v23
v_exp_f32 v27, v27
v_exp_f32 v28, v28
v_mfma_f32_32x32x16_bf16 v[80:95], v[124:127], v[112:115], v[80:95]
s_waitcnt lgkmcnt(1)
v_exp_f32 v29, v29
v_exp_f32 v30, v30
v_cvt_pk_bf16_f32 v116, v24, v25
v_exp_f32 v31, v31
v_cvt_pk_bf16_f32 v117, v26, v27
v_cvt_pk_bf16_f32 v118, v28, v29
v_cvt_pk_bf16_f32 v119, v30, v31
v_mfma_f32_32x32x8_bf16 v[16:31], v[132:133], v[144:145], v[48:63]
v_exp_f32 v32, v32
v_exp_f32 v33, v33
v_exp_f32 v34, v34
v_exp_f32 v35, v35
v_mfma_f32_32x32x16_bf16 v[80:95], v[128:131], v[116:119], v[80:95]
s_waitcnt lgkmcnt(0)
global_store_dwordx4 v4, v[244:247], s[8:9] sc1
v_exp_f32 v36, v36
v_exp_f32 v37, v37
v_cvt_pk_bf16_f32 v112, v32, v33
v_exp_f32 v38, v38
v_cvt_pk_bf16_f32 v113, v34, v35
v_exp_f32 v39, v39
v_mfma_f32_32x32x16_bf16 v[16:31], v[120:123], v[136:139], v[16:31]
v_exp_f32 v40, v40
v_exp_f32 v41, v41
v_cvt_pk_bf16_f32 v114, v36, v37
v_exp_f32 v42, v42
v_cvt_pk_bf16_f32 v115, v38, v39
v_exp_f32 v43, v43
v_exp_f32 v44, v44
v_mfma_f32_32x32x16_bf16 v[96:111], v[124:127], v[112:115], v[96:111]
ds_read_b128 v[124:127], v8 offset:18944
v_exp_f32 v45, v45
v_exp_f32 v46, v46
v_cvt_pk_bf16_f32 v116, v40, v41
v_exp_f32 v47, v47
v_cvt_pk_bf16_f32 v117, v42, v43
v_cvt_pk_bf16_f32 v118, v44, v45
v_cvt_pk_bf16_f32 v119, v46, v47
v_mfma_f32_32x32x8_bf16 v[32:47], v[132:133], v[152:153], v[64:79]
v_exp_f32 v16, v16
v_exp_f32 v17, v17
v_exp_f32 v18, v18
v_exp_f32 v19, v19
v_mfma_f32_32x32x16_bf16 v[96:111], v[128:131], v[116:119], v[96:111]
ds_read_b128 v[128:131], v8 offset:19552
global_store_dwordx4 v4, v[244:247], s[8:9] offset:1024 sc1
v_exp_f32 v20, v20
v_exp_f32 v21, v21
v_cvt_pk_bf16_f32 v112, v16, v17
v_exp_f32 v22, v22
v_cvt_pk_bf16_f32 v113, v18, v19
v_exp_f32 v23, v23
v_mfma_f32_32x32x16_bf16 v[32:47], v[120:123], v[140:143], v[32:47]
s_waitcnt lgkmcnt(1)
ds_read_b128 v[120:123], v5 offset:19968
v_exp_f32 v24, v24
v_exp_f32 v25, v25
v_cvt_pk_bf16_f32 v114, v20, v21
v_exp_f32 v26, v26
v_cvt_pk_bf16_f32 v115, v22, v23
v_exp_f32 v27, v27
v_exp_f32 v28, v28
v_mfma_f32_32x32x16_bf16 v[80:95], v[124:127], v[112:115], v[80:95]
s_waitcnt lgkmcnt(1)
v_exp_f32 v29, v29
v_exp_f32 v30, v30
v_cvt_pk_bf16_f32 v116, v24, v25
v_exp_f32 v31, v31
v_cvt_pk_bf16_f32 v117, v26, v27
v_cvt_pk_bf16_f32 v118, v28, v29
v_cvt_pk_bf16_f32 v119, v30, v31
v_mfma_f32_32x32x8_bf16 v[16:31], v[132:133], v[146:147], v[48:63]
v_exp_f32 v32, v32
v_exp_f32 v33, v33
v_exp_f32 v34, v34
v_exp_f32 v35, v35
v_mfma_f32_32x32x16_bf16 v[80:95], v[128:131], v[116:119], v[80:95]
s_waitcnt lgkmcnt(0)
global_store_dwordx4 v4, v[244:247], s[8:9] offset:2048 sc1
v_exp_f32 v36, v36
v_exp_f32 v37, v37
v_cvt_pk_bf16_f32 v112, v32, v33
v_exp_f32 v38, v38
v_cvt_pk_bf16_f32 v113, v34, v35
v_exp_f32 v39, v39
v_mfma_f32_32x32x16_bf16 v[16:31], v[120:123], v[136:139], v[16:31]
v_exp_f32 v40, v40
v_exp_f32 v41, v41
v_cvt_pk_bf16_f32 v114, v36, v37
v_exp_f32 v42, v42
v_cvt_pk_bf16_f32 v115, v38, v39
v_exp_f32 v43, v43
v_exp_f32 v44, v44
v_mfma_f32_32x32x16_bf16 v[96:111], v[124:127], v[112:115], v[96:111]
ds_read_b128 v[124:127], v8 offset:20160
v_exp_f32 v45, v45
v_exp_f32 v46, v46
v_cvt_pk_bf16_f32 v116, v40, v41
v_exp_f32 v47, v47
v_cvt_pk_bf16_f32 v117, v42, v43
v_cvt_pk_bf16_f32 v118, v44, v45
v_cvt_pk_bf16_f32 v119, v46, v47
v_mfma_f32_32x32x8_bf16 v[32:47], v[132:133], v[154:155], v[64:79]
v_exp_f32 v16, v16
v_exp_f32 v17, v17
v_exp_f32 v18, v18
v_exp_f32 v19, v19
v_mfma_f32_32x32x16_bf16 v[96:111], v[128:131], v[116:119], v[96:111]
ds_read_b128 v[128:131], v8 offset:20768
global_store_dwordx4 v4, v[244:247], s[8:9] offset:3072 sc1
v_exp_f32 v20, v20
v_exp_f32 v21, v21
v_cvt_pk_bf16_f32 v112, v16, v17
v_exp_f32 v22, v22
v_cvt_pk_bf16_f32 v113, v18, v19
v_exp_f32 v23, v23
v_mfma_f32_32x32x16_bf16 v[32:47], v[120:123], v[140:143], v[32:47]
s_waitcnt lgkmcnt(1)
ds_read_b128 v[120:123], v5 offset:20992
v_exp_f32 v24, v24
v_exp_f32 v25, v25
v_cvt_pk_bf16_f32 v114, v20, v21
v_exp_f32 v26, v26
v_cvt_pk_bf16_f32 v115, v22, v23
v_exp_f32 v27, v27
v_exp_f32 v28, v28
v_mfma_f32_32x32x16_bf16 v[80:95], v[124:127], v[112:115], v[80:95]
s_waitcnt lgkmcnt(1)
v_exp_f32 v29, v29
v_exp_f32 v30, v30
v_cvt_pk_bf16_f32 v116, v24, v25
v_exp_f32 v31, v31
v_cvt_pk_bf16_f32 v117, v26, v27
v_cvt_pk_bf16_f32 v118, v28, v29
v_cvt_pk_bf16_f32 v119, v30, v31
v_mfma_f32_32x32x8_bf16 v[16:31], v[132:133], v[148:149], v[48:63]
v_exp_f32 v32, v32
v_exp_f32 v33, v33
v_exp_f32 v34, v34
v_exp_f32 v35, v35
v_mfma_f32_32x32x16_bf16 v[80:95], v[128:131], v[116:119], v[80:95]
s_waitcnt lgkmcnt(0)
global_store_dwordx4 v4, v[244:247], s[8:9] offset:3712 sc1
v_exp_f32 v36, v36
v_exp_f32 v37, v37
v_cvt_pk_bf16_f32 v112, v32, v33
v_exp_f32 v38, v38
v_cvt_pk_bf16_f32 v113, v34, v35
v_exp_f32 v39, v39
v_mfma_f32_32x32x16_bf16 v[16:31], v[120:123], v[136:139], v[16:31]
v_exp_f32 v40, v40
v_exp_f32 v41, v41
v_cvt_pk_bf16_f32 v114, v36, v37
v_exp_f32 v42, v42
v_cvt_pk_bf16_f32 v115, v38, v39
v_exp_f32 v43, v43
v_exp_f32 v44, v44
v_mfma_f32_32x32x16_bf16 v[96:111], v[124:127], v[112:115], v[96:111]
ds_read_b128 v[124:127], v8 offset:21376
v_exp_f32 v45, v45
v_exp_f32 v46, v46
v_cvt_pk_bf16_f32 v116, v40, v41
v_exp_f32 v47, v47
v_cvt_pk_bf16_f32 v117, v42, v43
v_cvt_pk_bf16_f32 v118, v44, v45
v_cvt_pk_bf16_f32 v119, v46, v47
v_mfma_f32_32x32x8_bf16 v[32:47], v[132:133], v[156:157], v[64:79]
v_exp_f32 v16, v16
v_exp_f32 v17, v17
v_exp_f32 v18, v18
v_exp_f32 v19, v19
v_mfma_f32_32x32x16_bf16 v[96:111], v[128:131], v[116:119], v[96:111]
ds_read_b128 v[128:131], v8 offset:21984
v_exp_f32 v20, v20
v_exp_f32 v21, v21
v_cvt_pk_bf16_f32 v112, v16, v17
v_exp_f32 v22, v22
v_cvt_pk_bf16_f32 v113, v18, v19
v_exp_f32 v23, v23
v_mfma_f32_32x32x16_bf16 v[32:47], v[120:123], v[140:143], v[32:47]
s_waitcnt lgkmcnt(1)
ds_read_b128 v[120:123], v5 offset:22016
v_exp_f32 v24, v24
v_exp_f32 v25, v25
v_cvt_pk_bf16_f32 v114, v20, v21
v_exp_f32 v26, v26
v_cvt_pk_bf16_f32 v115, v22, v23
v_exp_f32 v27, v27
v_exp_f32 v28, v28
)"
R"(v_mfma_f32_32x32x16_bf16 v[80:95], v[124:127], v[112:115], v[80:95]
s_waitcnt lgkmcnt(1)
v_exp_f32 v29, v29
v_exp_f32 v30, v30
v_cvt_pk_bf16_f32 v116, v24, v25
v_exp_f32 v31, v31
v_cvt_pk_bf16_f32 v117, v26, v27
v_cvt_pk_bf16_f32 v118, v28, v29
v_cvt_pk_bf16_f32 v119, v30, v31
v_mfma_f32_32x32x8_bf16 v[16:31], v[132:133], v[150:151], v[48:63]
v_exp_f32 v32, v32
v_exp_f32 v33, v33
v_exp_f32 v34, v34
v_exp_f32 v35, v35
v_mfma_f32_32x32x16_bf16 v[80:95], v[128:131], v[116:119], v[80:95]
s_waitcnt lgkmcnt(0)
global_store_dwordx4 v4, v[244:247], s[10:11] sc1
v_exp_f32 v36, v36
v_exp_f32 v37, v37
v_cvt_pk_bf16_f32 v112, v32, v33
v_exp_f32 v38, v38
v_cvt_pk_bf16_f32 v113, v34, v35
v_exp_f32 v39, v39
v_mfma_f32_32x32x16_bf16 v[16:31], v[120:123], v[136:139], v[16:31]
v_exp_f32 v40, v40
v_exp_f32 v41, v41
v_cvt_pk_bf16_f32 v114, v36, v37
v_exp_f32 v42, v42
v_cvt_pk_bf16_f32 v115, v38, v39
v_exp_f32 v43, v43
v_exp_f32 v44, v44
v_mfma_f32_32x32x16_bf16 v[96:111], v[124:127], v[112:115], v[96:111]
ds_read_b128 v[124:127], v8 offset:22592
v_exp_f32 v45, v45
v_exp_f32 v46, v46
v_cvt_pk_bf16_f32 v116, v40, v41
v_exp_f32 v47, v47
v_cvt_pk_bf16_f32 v117, v42, v43
v_cvt_pk_bf16_f32 v118, v44, v45
v_cvt_pk_bf16_f32 v119, v46, v47
v_mfma_f32_32x32x8_bf16 v[32:47], v[132:133], v[158:159], v[64:79]
v_exp_f32 v16, v16
v_exp_f32 v17, v17
v_exp_f32 v18, v18
v_exp_f32 v19, v19
v_mfma_f32_32x32x16_bf16 v[96:111], v[128:131], v[116:119], v[96:111]
ds_read_b128 v[128:131], v8 offset:23200
ds_read_b64 v[132:133], v11 offset:19456
global_store_dwordx4 v4, v[244:247], s[10:11] offset:1024 sc1
s_add_u32 s10, s10, 2097152
s_addc_u32 s11, s11, 0
v_exp_f32 v20, v20
v_exp_f32 v21, v21
v_cvt_pk_bf16_f32 v112, v16, v17
v_exp_f32 v22, v22
v_cvt_pk_bf16_f32 v113, v18, v19
v_exp_f32 v23, v23
v_mfma_f32_32x32x16_bf16 v[32:47], v[120:123], v[140:143], v[32:47]
s_waitcnt lgkmcnt(2)
ds_read_b128 v[120:123], v5 offset:23040
v_exp_f32 v24, v24
v_exp_f32 v25, v25
v_cvt_pk_bf16_f32 v114, v20, v21
v_exp_f32 v26, v26
v_cvt_pk_bf16_f32 v115, v22, v23
v_exp_f32 v27, v27
v_exp_f32 v28, v28
v_mfma_f32_32x32x16_bf16 v[80:95], v[124:127], v[112:115], v[80:95]
s_waitcnt lgkmcnt(1)
v_exp_f32 v29, v29
v_exp_f32 v30, v30
v_cvt_pk_bf16_f32 v116, v24, v25
v_exp_f32 v31, v31
v_cvt_pk_bf16_f32 v117, v26, v27
v_cvt_pk_bf16_f32 v118, v28, v29
v_cvt_pk_bf16_f32 v119, v30, v31
v_mfma_f32_32x32x8_bf16 v[16:31], v[132:133], v[144:145], v[48:63]
v_exp_f32 v32, v32
v_exp_f32 v33, v33
v_exp_f32 v34, v34
v_exp_f32 v35, v35
v_mfma_f32_32x32x16_bf16 v[80:95], v[128:131], v[116:119], v[80:95]
s_waitcnt lgkmcnt(0)
global_store_dwordx4 v4, v[244:247], s[10:11] sc1
v_exp_f32 v36, v36
v_exp_f32 v37, v37
v_cvt_pk_bf16_f32 v112, v32, v33
v_exp_f32 v38, v38
v_cvt_pk_bf16_f32 v113, v34, v35
v_exp_f32 v39, v39
v_mfma_f32_32x32x16_bf16 v[16:31], v[120:123], v[136:139], v[16:31]
v_exp_f32 v40, v40
v_exp_f32 v41, v41
v_cvt_pk_bf16_f32 v114, v36, v37
v_exp_f32 v42, v42
v_cvt_pk_bf16_f32 v115, v38, v39
v_exp_f32 v43, v43
v_exp_f32 v44, v44
v_mfma_f32_32x32x16_bf16 v[96:111], v[124:127], v[112:115], v[96:111]
ds_read_b128 v[124:127], v8 offset:23808
v_exp_f32 v45, v45
v_exp_f32 v46, v46
v_cvt_pk_bf16_f32 v116, v40, v41
v_exp_f32 v47, v47
v_cvt_pk_bf16_f32 v117, v42, v43
v_cvt_pk_bf16_f32 v118, v44, v45
v_cvt_pk_bf16_f32 v119, v46, v47
v_mfma_f32_32x32x8_bf16 v[32:47], v[132:133], v[152:153], v[64:79]
v_exp_f32 v16, v16
v_exp_f32 v17, v17
v_exp_f32 v18, v18
v_exp_f32 v19, v19
v_mfma_f32_32x32x16_bf16 v[96:111], v[128:131], v[116:119], v[96:111]
ds_read_b128 v[128:131], v8 offset:24416
global_store_dwordx4 v4, v[244:247], s[10:11] offset:1024 sc1
s_add_u32 s10, s10, 2097152
s_addc_u32 s11, s11, 0
v_exp_f32 v20, v20
v_exp_f32 v21, v21
v_cvt_pk_bf16_f32 v112, v16, v17
v_exp_f32 v22, v22
v_cvt_pk_bf16_f32 v113, v18, v19
v_exp_f32 v23, v23
v_mfma_f32_32x32x16_bf16 v[32:47], v[120:123], v[140:143], v[32:47]
s_waitcnt lgkmcnt(1)
ds_read_b128 v[120:123], v5 offset:24064
v_exp_f32 v24, v24
v_exp_f32 v25, v25
v_cvt_pk_bf16_f32 v114, v20, v21
v_exp_f32 v26, v26
v_cvt_pk_bf16_f32 v115, v22, v23
v_exp_f32 v27, v27
v_exp_f32 v28, v28
v_mfma_f32_32x32x16_bf16 v[80:95], v[124:127], v[112:115], v[80:95]
s_waitcnt lgkmcnt(1)
v_exp_f32 v29, v29
v_exp_f32 v30, v30
v_cvt_pk_bf16_f32 v116, v24, v25
v_exp_f32 v31, v31
v_cvt_pk_bf16_f32 v117, v26, v27
v_cvt_pk_bf16_f32 v118, v28, v29
v_cvt_pk_bf16_f32 v119, v30, v31
v_mfma_f32_32x32x8_bf16 v[16:31], v[132:133], v[146:147], v[48:63]
v_exp_f32 v32, v32
v_exp_f32 v33, v33
v_exp_f32 v34, v34
v_exp_f32 v35, v35
v_mfma_f32_32x32x16_bf16 v[80:95], v[128:131], v[116:119], v[80:95]
s_waitcnt lgkmcnt(0)
v_exp_f32 v36, v36
v_exp_f32 v37, v37
v_cvt_pk_bf16_f32 v112, v32, v33
v_exp_f32 v38, v38
v_cvt_pk_bf16_f32 v113, v34, v35
v_exp_f32 v39, v39
v_mfma_f32_32x32x16_bf16 v[16:31], v[120:123], v[136:139], v[16:31]
v_exp_f32 v40, v40
v_exp_f32 v41, v41
v_cvt_pk_bf16_f32 v114, v36, v37
v_exp_f32 v42, v42
v_cvt_pk_bf16_f32 v115, v38, v39
v_exp_f32 v43, v43
v_exp_f32 v44, v44
v_mfma_f32_32x32x16_bf16 v[96:111], v[124:127], v[112:115], v[96:111]
ds_read_b128 v[124:127], v8 offset:25024
v_exp_f32 v45, v45
v_exp_f32 v46, v46
v_cvt_pk_bf16_f32 v116, v40, v41
v_exp_f32 v47, v47
v_cvt_pk_bf16_f32 v117, v42, v43
v_cvt_pk_bf16_f32 v118, v44, v45
v_cvt_pk_bf16_f32 v119, v46, v47
v_mfma_f32_32x32x8_bf16 v[32:47], v[132:133], v[154:155], v[64:79]
v_exp_f32 v16, v16
v_exp_f32 v17, v17
v_exp_f32 v18, v18
v_exp_f32 v19, v19
v_mfma_f32_32x32x16_bf16 v[96:111], v[128:131], v[116:119], v[96:111]
ds_read_b128 v[128:131], v8 offset:25632
global_store_dwordx4 v4, v[244:247], s[10:11] sc1
v_exp_f32 v20, v20
v_exp_f32 v21, v21
v_cvt_pk_bf16_f32 v112, v16, v17
v_exp_f32 v22, v22
v_cvt_pk_bf16_f32 v113, v18, v19
v_exp_f32 v23, v23
v_mfma_f32_32x32x16_bf16 v[32:47], v[120:123], v[140:143], v[32:47]
s_waitcnt lgkmcnt(1)
ds_read_b128 v[120:123], v5 offset:25088
v_exp_f32 v24, v24
v_exp_f32 v25, v25
v_cvt_pk_bf16_f32 v114, v20, v21
v_exp_f32 v26, v26
v_cvt_pk_bf16_f32 v115, v22, v23
v_exp_f32 v27, v27
v_exp_f32 v28, v28
v_mfma_f32_32x32x16_bf16 v[80:95], v[124:127], v[112:115], v[80:95]
s_waitcnt lgkmcnt(1)
v_exp_f32 v29, v29
v_exp_f32 v30, v30
v_cvt_pk_bf16_f32 v116, v24, v25
v_exp_f32 v31, v31
v_cvt_pk_bf16_f32 v117, v26, v27
v_cvt_pk_bf16_f32 v118, v28, v29
v_cvt_pk_bf16_f32 v119, v30, v31
v_mfma_f32_32x32x8_bf16 v[16:31], v[132:133], v[148:149], v[48:63]
v_exp_f32 v32, v32
v_exp_f32 v33, v33
v_exp_f32 v34, v34
v_exp_f32 v35, v35
v_mfma_f32_32x32x16_bf16 v[80:95], v[128:131], v[116:119], v[80:95]
s_waitcnt lgkmcnt(0)
global_store_dwordx4 v4, v[244:247], s[10:11] offset:1024 sc1
s_add_u32 s10, s10, 2097152
s_addc_u32 s11, s11, 0
v_exp_f32 v36, v36
v_exp_f32 v37, v37
v_cvt_pk_bf16_f32 v112, v32, v33
v_exp_f32 v38, v38
v_cvt_pk_bf16_f32 v113, v34, v35
v_exp_f32 v39, v39
v_mfma_f32_32x32x16_bf16 v[16:31], v[120:123], v[136:139], v[16:31]
v_exp_f32 v40, v40
v_exp_f32 v41, v41
v_cvt_pk_bf16_f32 v114, v36, v37
v_exp_f32 v42, v42
v_cvt_pk_bf16_f32 v115, v38, v39
v_exp_f32 v43, v43
v_exp_f32 v44, v44
v_mfma_f32_32x32x16_bf16 v[96:111], v[124:127], v[112:115], v[96:111]
ds_read_b128 v[124:127], v8 offset:26240
v_exp_f32 v45, v45
v_exp_f32 v46, v46
v_cvt_pk_bf16_f32 v116, v40, v41
v_exp_f32 v47, v47
v_cvt_pk_bf16_f32 v117, v42, v43
v_cvt_pk_bf16_f32 v118, v44, v45
v_cvt_pk_bf16_f32 v119, v46, v47
v_mfma_f32_32x32x8_bf16 v[32:47], v[132:133], v[156:157], v[64:79]
v_exp_f32 v16, v16
v_exp_f32 v17, v17
v_exp_f32 v18, v18
v_exp_f32 v19, v19
v_mfma_f32_32x32x16_bf16 v[96:111], v[128:131], v[116:119], v[96:111]
ds_read_b128 v[128:131], v8 offset:26848
global_store_dwordx4 v4, v[244:247], s[10:11] sc1
v_exp_f32 v20, v20
v_exp_f32 v21, v21
v_cvt_pk_bf16_f32 v112, v16, v17
v_exp_f32 v22, v22
v_cvt_pk_bf16_f32 v113, v18, v19
v_exp_f32 v23, v23
v_mfma_f32_32x32x16_bf16 v[32:47], v[120:123], v[140:143], v[32:47]
s_waitcnt lgkmcnt(1)
ds_read_b128 v[120:123], v5 offset:26112
v_exp_f32 v24, v24
v_exp_f32 v25, v25
v_cvt_pk_bf16_f32 v114, v20, v21
v_exp_f32 v26, v26
v_cvt_pk_bf16_f32 v115, v22, v23
v_exp_f32 v27, v27
v_exp_f32 v28, v28
v_mfma_f32_32x32x16_bf16 v[80:95], v[124:127], v[112:115], v[80:95]
s_waitcnt lgkmcnt(1)
v_exp_f32 v29, v29
v_exp_f32 v30, v30
v_cvt_pk_bf16_f32 v116, v24, v25
v_exp_f32 v31, v31
v_cvt_pk_bf16_f32 v117, v26, v27
v_cvt_pk_bf16_f32 v118, v28, v29
v_cvt_pk_bf16_f32 v119, v30, v31
v_mfma_f32_32x32x8_bf16 v[16:31], v[132:133], v[150:151], v[48:63]
v_exp_f32 v32, v32
v_exp_f32 v33, v33
v_exp_f32 v34, v34
v_exp_f32 v35, v35
v_mfma_f32_32x32x16_bf16 v[80:95], v[128:131], v[116:119], v[80:95]
s_waitcnt lgkmcnt(0)
global_store_dwordx4 v4, v[244:247], s[10:11] offset:1024 sc1
s_add_u32 s10, s10, 2097152
s_addc_u32 s11, s11, 0
v_exp_f32 v36, v36
v_exp_f32 v37, v37
v_cvt_pk_bf16_f32 v112, v32, v33
v_exp_f32 v38, v38
v_cvt_pk_bf16_f32 v113, v34, v35
v_exp_f32 v39, v39
v_mfma_f32_32x32x16_bf16 v[16:31], v[120:123], v[136:139], v[16:31]
v_exp_f32 v40, v40
v_exp_f32 v41, v41
v_cvt_pk_bf16_f32 v114, v36, v37
v_exp_f32 v42, v42
v_cvt_pk_bf16_f32 v115, v38, v39
v_exp_f32 v43, v43
v_exp_f32 v44, v44
v_mfma_f32_32x32x16_bf16 v[96:111], v[124:127], v[112:115], v[96:111]
ds_read_b128 v[124:127], v8 offset:27456
v_exp_f32 v45, v45
v_exp_f32 v46, v46
v_cvt_pk_bf16_f32 v116, v40, v41
v_exp_f32 v47, v47
v_cvt_pk_bf16_f32 v117, v42, v43
v_cvt_pk_bf16_f32 v118, v44, v45
v_cvt_pk_bf16_f32 v119, v46, v47
v_mfma_f32_32x32x8_bf16 v[32:47], v[132:133], v[158:159], v[64:79]
v_exp_f32 v16, v16
v_exp_f32 v17, v17
v_exp_f32 v18, v18
v_exp_f32 v19, v19
v_mfma_f32_32x32x16_bf16 v[96:111], v[128:131], v[116:119], v[96:111]
ds_read_b128 v[128:131], v8 offset:28064
ds_read_b64 v[132:133], v11 offset:37888
v_exp_f32 v20, v20
v_exp_f32 v21, v21
v_cvt_pk_bf16_f32 v112, v16, v17
v_exp_f32 v22, v22
v_cvt_pk_bf16_f32 v113, v18, v19
v_exp_f32 v23, v23
v_mfma_f32_32x32x16_bf16 v[32:47], v[120:123], v[140:143], v[32:47]
s_waitcnt lgkmcnt(2)
ds_read_b128 v[120:123], v5 offset:37888
v_exp_f32 v24, v24
v_exp_f32 v25, v25
v_cvt_pk_bf16_f32 v114, v20, v21
v_exp_f32 v26, v26
v_cvt_pk_bf16_f32 v115, v22, v23
v_exp_f32 v27, v27
v_exp_f32 v28, v28
v_mfma_f32_32x32x16_bf16 v[80:95], v[124:127], v[112:115], v[80:95]
s_waitcnt lgkmcnt(1)
v_exp_f32 v29, v29
v_exp_f32 v30, v30
v_cvt_pk_bf16_f32 v116, v24, v25
v_exp_f32 v31, v31
v_cvt_pk_bf16_f32 v117, v26, v27
v_cvt_pk_bf16_f32 v118, v28, v29
v_cvt_pk_bf16_f32 v119, v30, v31
v_mfma_f32_32x32x8_bf16 v[16:31], v[132:133], v[144:145], v[48:63]
v_exp_f32 v32, v32
v_exp_f32 v33, v33
v_exp_f32 v34, v34
v_exp_f32 v35, v35
v_mfma_f32_32x32x16_bf16 v[80:95], v[128:131], v[116:119], v[80:95]
s_waitcnt lgkmcnt(0)
global_store_dwordx4 v4, v[244:247], s[10:11] sc1
v_exp_f32 v36, v36
v_exp_f32 v37, v37
v_cvt_pk_bf16_f32 v112, v32, v33
v_exp_f32 v38, v38
v_cvt_pk_bf16_f32 v113, v34, v35
v_exp_f32 v39, v39
v_mfma_f32_32x32x16_bf16 v[16:31], v[120:123], v[136:139], v[16:31]
v_exp_f32 v40, v40
v_exp_f32 v41, v41
v_cvt_pk_bf16_f32 v114, v36, v37
v_exp_f32 v42, v42
v_cvt_pk_bf16_f32 v115, v38, v39
v_exp_f32 v43, v43
v_exp_f32 v44, v44
v_mfma_f32_32x32x16_bf16 v[96:111], v[124:127], v[112:115], v[96:111]
ds_read_b128 v[124:127], v8 offset:37888
v_exp_f32 v45, v45
v_exp_f32 v46, v46
v_cvt_pk_bf16_f32 v116, v40, v41
v_exp_f32 v47, v47
v_cvt_pk_bf16_f32 v117, v42, v43
v_cvt_pk_bf16_f32 v118, v44, v45
v_cvt_pk_bf16_f32 v119, v46, v47
v_mfma_f32_32x32x8_bf16 v[32:47], v[132:133], v[152:153], v[64:79]
v_exp_f32 v16, v16
v_exp_f32 v17, v17
v_exp_f32 v18, v18
v_exp_f32 v19, v19
v_mfma_f32_32x32x16_bf16 v[96:111], v[128:131], v[116:119], v[96:111]
ds_read_b128 v[128:131], v8 offset:38496
global_store_dwordx4 v4, v[244:247], s[10:11] offset:1024 sc1
s_add_u32 s10, s10, 2097152
s_addc_u32 s11, s11, 0
v_exp_f32 v20, v20
v_exp_f32 v21, v21
v_cvt_pk_bf16_f32 v112, v16, v17
v_exp_f32 v22, v22
v_cvt_pk_bf16_f32 v113, v18, v19
v_exp_f32 v23, v23
v_mfma_f32_32x32x16_bf16 v[32:47], v[120:123], v[140:143], v[32:47]
s_waitcnt lgkmcnt(1)
ds_read_b128 v[120:123], v5 offset:38912
v_exp_f32 v24, v24
v_exp_f32 v25, v25
v_cvt_pk_bf16_f32 v114, v20, v21
v_exp_f32 v26, v26
v_cvt_pk_bf16_f32 v115, v22, v23
v_exp_f32 v27, v27
v_exp_f32 v28, v28
v_mfma_f32_32x32x16_bf16 v[80:95], v[124:127], v[112:115], v[80:95]
s_waitcnt lgkmcnt(1)
v_exp_f32 v29, v29
v_exp_f32 v30, v30
v_cvt_pk_bf16_f32 v116, v24, v25
v_exp_f32 v31, v31
)"
R"(v_cvt_pk_bf16_f32 v117, v26, v27
v_cvt_pk_bf16_f32 v118, v28, v29
v_cvt_pk_bf16_f32 v119, v30, v31
v_mfma_f32_32x32x8_bf16 v[16:31], v[132:133], v[146:147], v[48:63]
v_exp_f32 v32, v32
v_exp_f32 v33, v33
v_exp_f32 v34, v34
v_exp_f32 v35, v35
v_mfma_f32_32x32x16_bf16 v[80:95], v[128:131], v[116:119], v[80:95]
s_waitcnt lgkmcnt(0)
global_store_dwordx4 v4, v[244:247], s[10:11] sc1
v_exp_f32 v36, v36
v_exp_f32 v37, v37
v_cvt_pk_bf16_f32 v112, v32, v33
v_exp_f32 v38, v38
v_cvt_pk_bf16_f32 v113, v34, v35
v_exp_f32 v39, v39
v_mfma_f32_32x32x16_bf16 v[16:31], v[120:123], v[136:139], v[16:31]
v_exp_f32 v40, v40
v_exp_f32 v41, v41
v_cvt_pk_bf16_f32 v114, v36, v37
v_exp_f32 v42, v42
v_cvt_pk_bf16_f32 v115, v38, v39
v_exp_f32 v43, v43
v_exp_f32 v44, v44
v_mfma_f32_32x32x16_bf16 v[96:111], v[124:127], v[112:115], v[96:111]
ds_read_b128 v[124:127], v8 offset:39104
v_exp_f32 v45, v45
v_exp_f32 v46, v46
v_cvt_pk_bf16_f32 v116, v40, v41
v_exp_f32 v47, v47
v_cvt_pk_bf16_f32 v117, v42, v43
v_cvt_pk_bf16_f32 v118, v44, v45
v_cvt_pk_bf16_f32 v119, v46, v47
v_mfma_f32_32x32x8_bf16 v[32:47], v[132:133], v[154:155], v[64:79]
v_exp_f32 v16, v16
v_exp_f32 v17, v17
v_exp_f32 v18, v18
v_exp_f32 v19, v19
v_mfma_f32_32x32x16_bf16 v[96:111], v[128:131], v[116:119], v[96:111]
ds_read_b128 v[128:131], v8 offset:39712
global_store_dwordx4 v4, v[244:247], s[10:11] offset:1024 sc1
s_add_u32 s10, s10, 2097152
s_addc_u32 s11, s11, 0
v_exp_f32 v20, v20
v_exp_f32 v21, v21
v_cvt_pk_bf16_f32 v112, v16, v17
v_exp_f32 v22, v22
v_cvt_pk_bf16_f32 v113, v18, v19
v_exp_f32 v23, v23
v_mfma_f32_32x32x16_bf16 v[32:47], v[120:123], v[140:143], v[32:47]
s_waitcnt lgkmcnt(1)
ds_read_b128 v[120:123], v5 offset:39936
v_exp_f32 v24, v24
v_exp_f32 v25, v25
v_cvt_pk_bf16_f32 v114, v20, v21
v_exp_f32 v26, v26
v_cvt_pk_bf16_f32 v115, v22, v23
v_exp_f32 v27, v27
v_exp_f32 v28, v28
v_mfma_f32_32x32x16_bf16 v[80:95], v[124:127], v[112:115], v[80:95]
s_waitcnt lgkmcnt(1)
v_exp_f32 v29, v29
v_exp_f32 v30, v30
v_cvt_pk_bf16_f32 v116, v24, v25
v_exp_f32 v31, v31
v_cvt_pk_bf16_f32 v117, v26, v27
v_cvt_pk_bf16_f32 v118, v28, v29
v_cvt_pk_bf16_f32 v119, v30, v31
v_mfma_f32_32x32x8_bf16 v[16:31], v[132:133], v[148:149], v[48:63]
v_exp_f32 v32, v32
v_exp_f32 v33, v33
v_exp_f32 v34, v34
v_exp_f32 v35, v35
v_mfma_f32_32x32x16_bf16 v[80:95], v[128:131], v[116:119], v[80:95]
s_waitcnt lgkmcnt(0)
v_exp_f32 v36, v36
v_exp_f32 v37, v37
v_cvt_pk_bf16_f32 v112, v32, v33
v_exp_f32 v38, v38
v_cvt_pk_bf16_f32 v113, v34, v35
LW0_4_%=:
s_cbranch_execz LWEND0_%=
v_exp_f32 v39, v39
v_mfma_f32_32x32x16_bf16 v[16:31], v[120:123], v[136:139], v[16:31]
v_exp_f32 v40, v40
v_exp_f32 v41, v41
v_cvt_pk_bf16_f32 v114, v36, v37
v_exp_f32 v42, v42
v_cvt_pk_bf16_f32 v115, v38, v39
v_exp_f32 v43, v43
v_exp_f32 v44, v44
v_mfma_f32_32x32x16_bf16 v[96:111], v[124:127], v[112:115], v[96:111]
ds_read_b128 v[124:127], v8 offset:40320
v_exp_f32 v45, v45
v_exp_f32 v46, v46
v_cvt_pk_bf16_f32 v116, v40, v41
v_exp_f32 v47, v47
v_cvt_pk_bf16_f32 v117, v42, v43
v_cvt_pk_bf16_f32 v118, v44, v45
v_cvt_pk_bf16_f32 v119, v46, v47
v_mfma_f32_32x32x8_bf16 v[32:47], v[132:133], v[156:157], v[64:79]
v_exp_f32 v16, v16
v_exp_f32 v17, v17
v_exp_f32 v18, v18
v_exp_f32 v19, v19
v_mfma_f32_32x32x16_bf16 v[96:111], v[128:131], v[116:119], v[96:111]
ds_read_b128 v[128:131], v8 offset:40928
global_store_dwordx4 v4, v[244:247], s[10:11] sc1
v_exp_f32 v20, v20
v_exp_f32 v21, v21
v_cvt_pk_bf16_f32 v112, v16, v17
v_exp_f32 v22, v22
v_cvt_pk_bf16_f32 v113, v18, v19
v_exp_f32 v23, v23
v_mfma_f32_32x32x16_bf16 v[32:47], v[120:123], v[140:143], v[32:47]
s_waitcnt lgkmcnt(1)
ds_read_b128 v[120:123], v5 offset:40960
v_exp_f32 v24, v24
v_exp_f32 v25, v25
v_cvt_pk_bf16_f32 v114, v20, v21
v_exp_f32 v26, v26
v_cvt_pk_bf16_f32 v115, v22, v23
v_exp_f32 v27, v27
v_exp_f32 v28, v28
v_mfma_f32_32x32x16_bf16 v[80:95], v[124:127], v[112:115], v[80:95]
s_waitcnt lgkmcnt(1)
v_exp_f32 v29, v29
v_exp_f32 v30, v30
v_cvt_pk_bf16_f32 v116, v24, v25
v_exp_f32 v31, v31
v_cvt_pk_bf16_f32 v117, v26, v27
v_cvt_pk_bf16_f32 v118, v28, v29
v_cvt_pk_bf16_f32 v119, v30, v31
v_mfma_f32_32x32x8_bf16 v[16:31], v[132:133], v[150:151], v[48:63]
v_exp_f32 v32, v32
v_exp_f32 v33, v33
v_exp_f32 v34, v34
v_exp_f32 v35, v35
v_mfma_f32_32x32x16_bf16 v[80:95], v[128:131], v[116:119], v[80:95]
s_waitcnt lgkmcnt(0)
global_store_dwordx4 v4, v[244:247], s[10:11] offset:1024 sc1
s_add_u32 s10, s10, 2097152
s_addc_u32 s11, s11, 0
v_exp_f32 v36, v36
v_exp_f32 v37, v37
v_cvt_pk_bf16_f32 v112, v32, v33
v_exp_f32 v38, v38
v_cvt_pk_bf16_f32 v113, v34, v35
v_exp_f32 v39, v39
v_mfma_f32_32x32x16_bf16 v[16:31], v[120:123], v[136:139], v[16:31]
v_exp_f32 v40, v40
v_exp_f32 v41, v41
v_cvt_pk_bf16_f32 v114, v36, v37
v_exp_f32 v42, v42
v_cvt_pk_bf16_f32 v115, v38, v39
v_exp_f32 v43, v43
v_exp_f32 v44, v44
v_mfma_f32_32x32x16_bf16 v[96:111], v[124:127], v[112:115], v[96:111]
ds_read_b128 v[124:127], v8 offset:41536
v_exp_f32 v45, v45
v_exp_f32 v46, v46
v_cvt_pk_bf16_f32 v116, v40, v41
v_exp_f32 v47, v47
v_cvt_pk_bf16_f32 v117, v42, v43
v_cvt_pk_bf16_f32 v118, v44, v45
v_cvt_pk_bf16_f32 v119, v46, v47
v_mfma_f32_32x32x8_bf16 v[32:47], v[132:133], v[158:159], v[64:79]
v_exp_f32 v16, v16
v_exp_f32 v17, v17
v_exp_f32 v18, v18
v_exp_f32 v19, v19
v_mfma_f32_32x32x16_bf16 v[96:111], v[128:131], v[116:119], v[96:111]
ds_read_b128 v[128:131], v8 offset:42144
ds_read_b64 v[132:133], v11 offset:38400
global_store_dwordx4 v4, v[244:247], s[10:11] sc1
v_exp_f32 v20, v20
v_exp_f32 v21, v21
v_cvt_pk_bf16_f32 v112, v16, v17
v_exp_f32 v22, v22
v_cvt_pk_bf16_f32 v113, v18, v19
v_exp_f32 v23, v23
v_mfma_f32_32x32x16_bf16 v[32:47], v[120:123], v[140:143], v[32:47]
s_waitcnt lgkmcnt(2)
ds_read_b128 v[120:123], v5 offset:41984
v_exp_f32 v24, v24
v_exp_f32 v25, v25
v_cvt_pk_bf16_f32 v114, v20, v21
v_exp_f32 v26, v26
v_cvt_pk_bf16_f32 v115, v22, v23
v_exp_f32 v27, v27
v_exp_f32 v28, v28
v_mfma_f32_32x32x16_bf16 v[80:95], v[124:127], v[112:115], v[80:95]
s_waitcnt lgkmcnt(1)
v_exp_f32 v29, v29
v_exp_f32 v30, v30
v_cvt_pk_bf16_f32 v116, v24, v25
v_exp_f32 v31, v31
v_cvt_pk_bf16_f32 v117, v26, v27
v_cvt_pk_bf16_f32 v118, v28, v29
v_cvt_pk_bf16_f32 v119, v30, v31
v_mfma_f32_32x32x8_bf16 v[16:31], v[132:133], v[144:145], v[48:63]
v_exp_f32 v32, v32
v_exp_f32 v33, v33
v_exp_f32 v34, v34
v_exp_f32 v35, v35
v_mfma_f32_32x32x16_bf16 v[80:95], v[128:131], v[116:119], v[80:95]
s_waitcnt lgkmcnt(0)
global_store_dwordx4 v4, v[244:247], s[10:11] offset:1024 sc1
s_add_u32 s10, s10, 2097152
s_addc_u32 s11, s11, 0
v_exp_f32 v36, v36
v_exp_f32 v37, v37
v_cvt_pk_bf16_f32 v112, v32, v33
v_exp_f32 v38, v38
v_cvt_pk_bf16_f32 v113, v34, v35
v_exp_f32 v39, v39
v_mfma_f32_32x32x16_bf16 v[16:31], v[120:123], v[136:139], v[16:31]
v_exp_f32 v40, v40
v_exp_f32 v41, v41
v_cvt_pk_bf16_f32 v114, v36, v37
v_exp_f32 v42, v42
v_cvt_pk_bf16_f32 v115, v38, v39
v_exp_f32 v43, v43
v_exp_f32 v44, v44
v_mfma_f32_32x32x16_bf16 v[96:111], v[124:127], v[112:115], v[96:111]
ds_read_b128 v[124:127], v8 offset:42752
v_exp_f32 v45, v45
v_exp_f32 v46, v46
v_cvt_pk_bf16_f32 v116, v40, v41
v_exp_f32 v47, v47
v_cvt_pk_bf16_f32 v117, v42, v43
v_cvt_pk_bf16_f32 v118, v44, v45
v_cvt_pk_bf16_f32 v119, v46, v47
v_mfma_f32_32x32x8_bf16 v[32:47], v[132:133], v[152:153], v[64:79]
v_exp_f32 v16, v16
v_exp_f32 v17, v17
v_exp_f32 v18, v18
v_exp_f32 v19, v19
v_mfma_f32_32x32x16_bf16 v[96:111], v[128:131], v[116:119], v[96:111]
ds_read_b128 v[128:131], v8 offset:43360
v_exp_f32 v20, v20
v_exp_f32 v21, v21
v_cvt_pk_bf16_f32 v112, v16, v17
v_exp_f32 v22, v22
v_cvt_pk_bf16_f32 v113, v18, v19
v_exp_f32 v23, v23
v_mfma_f32_32x32x16_bf16 v[32:47], v[120:123], v[140:143], v[32:47]
s_waitcnt lgkmcnt(1)
ds_read_b128 v[120:123], v5 offset:43008
v_exp_f32 v24, v24
v_exp_f32 v25, v25
v_cvt_pk_bf16_f32 v114, v20, v21
v_exp_f32 v26, v26
v_cvt_pk_bf16_f32 v115, v22, v23
v_exp_f32 v27, v27
v_exp_f32 v28, v28
v_mfma_f32_32x32x16_bf16 v[80:95], v[124:127], v[112:115], v[80:95]
s_waitcnt lgkmcnt(1)
v_exp_f32 v29, v29
v_exp_f32 v30, v30
v_cvt_pk_bf16_f32 v116, v24, v25
v_exp_f32 v31, v31
v_cvt_pk_bf16_f32 v117, v26, v27
v_cvt_pk_bf16_f32 v118, v28, v29
v_cvt_pk_bf16_f32 v119, v30, v31
v_mfma_f32_32x32x8_bf16 v[16:31], v[132:133], v[146:147], v[48:63]
v_exp_f32 v32, v32
v_exp_f32 v33, v33
v_exp_f32 v34, v34
v_exp_f32 v35, v35
v_mfma_f32_32x32x16_bf16 v[80:95], v[128:131], v[116:119], v[80:95]
s_waitcnt lgkmcnt(0)
v_exp_f32 v36, v36
v_exp_f32 v37, v37
v_cvt_pk_bf16_f32 v112, v32, v33
v_exp_f32 v38, v38
v_cvt_pk_bf16_f32 v113, v34, v35
v_exp_f32 v39, v39
v_mfma_f32_32x32x16_bf16 v[16:31], v[120:123], v[136:139], v[16:31]
v_exp_f32 v40, v40
v_exp_f32 v41, v41
v_cvt_pk_bf16_f32 v114, v36, v37
v_exp_f32 v42, v42
v_cvt_pk_bf16_f32 v115, v38, v39
v_exp_f32 v43, v43
v_exp_f32 v44, v44
v_mfma_f32_32x32x16_bf16 v[96:111], v[124:127], v[112:115], v[96:111]
ds_read_b128 v[124:127], v8 offset:43968
v_exp_f32 v45, v45
v_exp_f32 v46, v46
v_cvt_pk_bf16_f32 v116, v40, v41
v_exp_f32 v47, v47
v_cvt_pk_bf16_f32 v117, v42, v43
v_cvt_pk_bf16_f32 v118, v44, v45
v_cvt_pk_bf16_f32 v119, v46, v47
v_mfma_f32_32x32x8_bf16 v[32:47], v[132:133], v[154:155], v[64:79]
v_exp_f32 v16, v16
v_exp_f32 v17, v17
v_exp_f32 v18, v18
v_exp_f32 v19, v19
v_mfma_f32_32x32x16_bf16 v[96:111], v[128:131], v[116:119], v[96:111]
ds_read_b128 v[128:131], v8 offset:44576
v_exp_f32 v20, v20
v_exp_f32 v21, v21
v_cvt_pk_bf16_f32 v112, v16, v17
v_exp_f32 v22, v22
v_cvt_pk_bf16_f32 v113, v18, v19
v_exp_f32 v23, v23
v_mfma_f32_32x32x16_bf16 v[32:47], v[120:123], v[140:143], v[32:47]
s_waitcnt lgkmcnt(1)
ds_read_b128 v[120:123], v5 offset:44032
v_exp_f32 v24, v24
v_exp_f32 v25, v25
v_cvt_pk_bf16_f32 v114, v20, v21
v_exp_f32 v26, v26
v_cvt_pk_bf16_f32 v115, v22, v23
v_exp_f32 v27, v27
v_exp_f32 v28, v28
v_mfma_f32_32x32x16_bf16 v[80:95], v[124:127], v[112:115], v[80:95]
s_waitcnt lgkmcnt(1)
v_exp_f32 v29, v29
v_exp_f32 v30, v30
v_cvt_pk_bf16_f32 v116, v24, v25
v_exp_f32 v31, v31
v_cvt_pk_bf16_f32 v117, v26, v27
v_cvt_pk_bf16_f32 v118, v28, v29
v_cvt_pk_bf16_f32 v119, v30, v31
v_mfma_f32_32x32x8_bf16 v[16:31], v[132:133], v[148:149], v[48:63]
v_exp_f32 v32, v32
v_exp_f32 v33, v33
v_exp_f32 v34, v34
v_exp_f32 v35, v35
v_mfma_f32_32x32x16_bf16 v[80:95], v[128:131], v[116:119], v[80:95]
s_waitcnt lgkmcnt(0)
v_exp_f32 v36, v36
v_exp_f32 v37, v37
v_cvt_pk_bf16_f32 v112, v32, v33
v_exp_f32 v38, v38
v_cvt_pk_bf16_f32 v113, v34, v35
v_exp_f32 v39, v39
v_mfma_f32_32x32x16_bf16 v[16:31], v[120:123], v[136:139], v[16:31]
v_exp_f32 v40, v40
v_exp_f32 v41, v41
v_cvt_pk_bf16_f32 v114, v36, v37
v_exp_f32 v42, v42
v_cvt_pk_bf16_f32 v115, v38, v39
v_exp_f32 v43, v43
v_exp_f32 v44, v44
v_mfma_f32_32x32x16_bf16 v[96:111], v[124:127], v[112:115], v[96:111]
ds_read_b128 v[124:127], v8 offset:45184
v_exp_f32 v45, v45
v_exp_f32 v46, v46
v_cvt_pk_bf16_f32 v116, v40, v41
v_exp_f32 v47, v47
v_cvt_pk_bf16_f32 v117, v42, v43
v_cvt_pk_bf16_f32 v118, v44, v45
v_cvt_pk_bf16_f32 v119, v46, v47
v_mfma_f32_32x32x8_bf16 v[32:47], v[132:133], v[156:157], v[64:79]
v_exp_f32 v16, v16
v_exp_f32 v17, v17
v_exp_f32 v18, v18
v_exp_f32 v19, v19
v_mfma_f32_32x32x16_bf16 v[96:111], v[128:131], v[116:119], v[96:111]
ds_read_b128 v[128:131], v8 offset:45792
v_exp_f32 v20, v20
v_exp_f32 v21, v21
v_cvt_pk_bf16_f32 v112, v16, v17
v_exp_f32 v22, v22
v_cvt_pk_bf16_f32 v113, v18, v19
v_exp_f32 v23, v23
v_mfma_f32_32x32x16_bf16 v[32:47], v[120:123], v[140:143], v[32:47]
s_waitcnt lgkmcnt(1)
ds_read_b128 v[120:123], v5 offset:45056
v_exp_f32 v24, v24
v_exp_f32 v25, v25
v_cvt_pk_bf16_f32 v114, v20, v21
v_exp_f32 v26, v26
v_cvt_pk_bf16_f32 v115, v22, v23
v_exp_f32 v27, v27
v_exp_f32 v28, v28
v_mfma_f32_32x32x16_bf16 v[80:95], v[124:127], v[112:115], v[80:95]
s_waitcnt lgkmcnt(1)
v_exp_f32 v29, v29
v_exp_f32 v30, v30
v_cvt_pk_bf16_f32 v116, v24, v25
v_exp_f32 v31, v31
v_cvt_pk_bf16_f32 v117, v26, v27
v_cvt_pk_bf16_f32 v118, v28, v29
v_cvt_pk_bf16_f32 v119, v30, v31
v_mfma_f32_32x32x8_bf16 v[16:31], v[132:133], v[150:151], v[48:63]
v_exp_f32 v32, v32
v_exp_f32 v33, v33
v_exp_f32 v34, v34
v_exp_f32 v35, v35
v_mfma_f32_32x32x16_bf16 v[80:95], v[128:131], v[116:119], v[80:95]
s_waitcnt lgkmcnt(0)
v_exp_f32 v36, v36
v_exp_f32 v37, v37
v_cvt_pk_bf16_f32 v112, v32, v33
)"
R"(v_exp_f32 v38, v38
v_cvt_pk_bf16_f32 v113, v34, v35
v_exp_f32 v39, v39
v_mfma_f32_32x32x16_bf16 v[16:31], v[120:123], v[136:139], v[16:31]
v_exp_f32 v40, v40
v_exp_f32 v41, v41
v_cvt_pk_bf16_f32 v114, v36, v37
v_exp_f32 v42, v42
v_cvt_pk_bf16_f32 v115, v38, v39
v_exp_f32 v43, v43
v_exp_f32 v44, v44
v_mfma_f32_32x32x16_bf16 v[96:111], v[124:127], v[112:115], v[96:111]
ds_read_b128 v[124:127], v8 offset:46400
v_exp_f32 v45, v45
v_exp_f32 v46, v46
v_cvt_pk_bf16_f32 v116, v40, v41
v_exp_f32 v47, v47
v_cvt_pk_bf16_f32 v117, v42, v43
v_cvt_pk_bf16_f32 v118, v44, v45
v_cvt_pk_bf16_f32 v119, v46, v47
v_mfma_f32_32x32x8_bf16 v[32:47], v[132:133], v[158:159], v[64:79]
v_exp_f32 v16, v16
v_exp_f32 v17, v17
v_exp_f32 v18, v18
v_exp_f32 v19, v19
v_mfma_f32_32x32x16_bf16 v[96:111], v[128:131], v[116:119], v[96:111]
ds_read_b128 v[128:131], v8 offset:47008
ds_read_b64 v[132:133], v12
v_exp_f32 v20, v20
v_exp_f32 v21, v21
v_cvt_pk_bf16_f32 v112, v16, v17
v_exp_f32 v22, v22
v_cvt_pk_bf16_f32 v113, v18, v19
v_exp_f32 v23, v23
v_mfma_f32_32x32x16_bf16 v[32:47], v[120:123], v[140:143], v[32:47]
s_waitcnt lgkmcnt(2)
ds_read_b128 v[120:123], v6
v_exp_f32 v24, v24
v_exp_f32 v25, v25
v_cvt_pk_bf16_f32 v114, v20, v21
v_exp_f32 v26, v26
v_cvt_pk_bf16_f32 v115, v22, v23
v_exp_f32 v27, v27
v_exp_f32 v28, v28
v_mfma_f32_32x32x16_bf16 v[80:95], v[124:127], v[112:115], v[80:95]
s_waitcnt lgkmcnt(1)
v_exp_f32 v29, v29
v_exp_f32 v30, v30
v_cvt_pk_bf16_f32 v116, v24, v25
v_exp_f32 v31, v31
v_cvt_pk_bf16_f32 v117, v26, v27
v_cvt_pk_bf16_f32 v118, v28, v29
v_cvt_pk_bf16_f32 v119, v30, v31
v_mfma_f32_32x32x8_bf16 v[16:31], v[132:133], v[144:145], v[48:63]
v_exp_f32 v32, v32
v_exp_f32 v33, v33
v_exp_f32 v34, v34
v_exp_f32 v35, v35
v_mfma_f32_32x32x16_bf16 v[80:95], v[128:131], v[116:119], v[80:95]
s_waitcnt lgkmcnt(0)
v_exp_f32 v36, v36
v_exp_f32 v37, v37
v_cvt_pk_bf16_f32 v112, v32, v33
v_exp_f32 v38, v38
v_cvt_pk_bf16_f32 v113, v34, v35
v_exp_f32 v39, v39
v_mfma_f32_32x32x16_bf16 v[16:31], v[120:123], v[136:139], v[16:31]
v_exp_f32 v40, v40
v_exp_f32 v41, v41
v_cvt_pk_bf16_f32 v114, v36, v37
v_exp_f32 v42, v42
v_cvt_pk_bf16_f32 v115, v38, v39
v_exp_f32 v43, v43
v_exp_f32 v44, v44
v_mfma_f32_32x32x16_bf16 v[96:111], v[124:127], v[112:115], v[96:111]
ds_read_b128 v[124:127], v9
v_exp_f32 v45, v45
v_exp_f32 v46, v46
v_cvt_pk_bf16_f32 v116, v40, v41
v_exp_f32 v47, v47
v_cvt_pk_bf16_f32 v117, v42, v43
v_cvt_pk_bf16_f32 v118, v44, v45
v_cvt_pk_bf16_f32 v119, v46, v47
v_mfma_f32_32x32x8_bf16 v[32:47], v[132:133], v[152:153], v[64:79]
v_exp_f32 v16, v16
v_exp_f32 v17, v17
v_exp_f32 v18, v18
v_exp_f32 v19, v19
v_mfma_f32_32x32x16_bf16 v[96:111], v[128:131], v[116:119], v[96:111]
ds_read_b128 v[128:131], v9 offset:608
v_exp_f32 v20, v20
v_exp_f32 v21, v21
v_cvt_pk_bf16_f32 v112, v16, v17
v_exp_f32 v22, v22
v_cvt_pk_bf16_f32 v113, v18, v19
v_exp_f32 v23, v23
v_mfma_f32_32x32x16_bf16 v[32:47], v[120:123], v[140:143], v[32:47]
s_waitcnt lgkmcnt(1)
ds_read_b128 v[120:123], v6 offset:1024
v_exp_f32 v24, v24
v_exp_f32 v25, v25
v_cvt_pk_bf16_f32 v114, v20, v21
v_exp_f32 v26, v26
v_cvt_pk_bf16_f32 v115, v22, v23
v_exp_f32 v27, v27
v_exp_f32 v28, v28
v_mfma_f32_32x32x16_bf16 v[80:95], v[124:127], v[112:115], v[80:95]
s_waitcnt lgkmcnt(1)
v_exp_f32 v29, v29
v_exp_f32 v30, v30
v_cvt_pk_bf16_f32 v116, v24, v25
v_exp_f32 v31, v31
v_cvt_pk_bf16_f32 v117, v26, v27
v_cvt_pk_bf16_f32 v118, v28, v29
v_cvt_pk_bf16_f32 v119, v30, v31
v_mfma_f32_32x32x8_bf16 v[16:31], v[132:133], v[146:147], v[48:63]
v_exp_f32 v32, v32
v_exp_f32 v33, v33
v_exp_f32 v34, v34
v_exp_f32 v35, v35
v_mfma_f32_32x32x16_bf16 v[80:95], v[128:131], v[116:119], v[80:95]
s_waitcnt lgkmcnt(0)
v_exp_f32 v36, v36
v_exp_f32 v37, v37
v_cvt_pk_bf16_f32 v112, v32, v33
v_exp_f32 v38, v38
v_cvt_pk_bf16_f32 v113, v34, v35
v_exp_f32 v39, v39
v_mfma_f32_32x32x16_bf16 v[16:31], v[120:123], v[136:139], v[16:31]
v_exp_f32 v40, v40
v_exp_f32 v41, v41
v_cvt_pk_bf16_f32 v114, v36, v37
v_exp_f32 v42, v42
v_cvt_pk_bf16_f32 v115, v38, v39
v_exp_f32 v43, v43
v_exp_f32 v44, v44
v_mfma_f32_32x32x16_bf16 v[96:111], v[124:127], v[112:115], v[96:111]
ds_read_b128 v[124:127], v9 offset:1216
v_exp_f32 v45, v45
v_exp_f32 v46, v46
v_cvt_pk_bf16_f32 v116, v40, v41
v_exp_f32 v47, v47
v_cvt_pk_bf16_f32 v117, v42, v43
v_cvt_pk_bf16_f32 v118, v44, v45
v_cvt_pk_bf16_f32 v119, v46, v47
s_waitcnt vmcnt(0)
s_barrier
s_cmp_eq_u32 s39, 3
s_cbranch_scc0 Lc_15_%=
s_lshl_b32 s96, s36, 3
s_add_u32 s96, s96, 4096
v_mov_b32 v176, s40
v_mov_b32 v177, s41
v_mov_b32 v162, s96
s_mov_b32 exec_lo, 1
s_mov_b32 exec_hi, 0
global_store_dwordx2 v162, v[176:177], s[50:51] sc1
s_mov_b64 exec, -1
Lc_15_%=:
s_mov_b32 s56, s20
s_and_b32 s57, s21, 0xffff
s_sub_u32 s60, s60, 52096
v_mfma_f32_32x32x8_bf16 v[32:47], v[132:133], v[154:155], v[64:79]
v_exp_f32 v16, v16
v_exp_f32 v17, v17
v_exp_f32 v18, v18
v_exp_f32 v19, v19
v_mfma_f32_32x32x16_bf16 v[96:111], v[128:131], v[116:119], v[96:111]
ds_read_b128 v[128:131], v9 offset:1824
s_mul_i32 s96, s93, 1152
s_add_u32 s4, s24, s96
s_addc_u32 s5, s25, 0
s_and_b32 s5, s5, 0xffff
s_mov_b32 s6, 1152
s_mov_b32 s7, 131072
s_and_b32 s96, s39, 1
s_lshl_b32 s96, s96, 10
s_add_u32 m0, s96, 161792
s_nop 0
buffer_load_dwordx4 v4, s[4:7], s96 offen lds
v_exp_f32 v20, v20
v_exp_f32 v21, v21
v_cvt_pk_bf16_f32 v112, v16, v17
v_exp_f32 v22, v22
v_cvt_pk_bf16_f32 v113, v18, v19
v_exp_f32 v23, v23
v_mfma_f32_32x32x16_bf16 v[32:47], v[120:123], v[140:143], v[32:47]
s_waitcnt lgkmcnt(1)
ds_read_b128 v[120:123], v6 offset:2048
v_exp_f32 v24, v24
v_exp_f32 v25, v25
v_cvt_pk_bf16_f32 v114, v20, v21
v_exp_f32 v26, v26
v_cvt_pk_bf16_f32 v115, v22, v23
v_exp_f32 v27, v27
v_exp_f32 v28, v28
v_mfma_f32_32x32x16_bf16 v[80:95], v[124:127], v[112:115], v[80:95]
s_waitcnt lgkmcnt(1)
v_exp_f32 v29, v29
v_exp_f32 v30, v30
v_cvt_pk_bf16_f32 v116, v24, v25
v_exp_f32 v31, v31
v_cvt_pk_bf16_f32 v117, v26, v27
v_cvt_pk_bf16_f32 v118, v28, v29
v_cvt_pk_bf16_f32 v119, v30, v31
v_mfma_f32_32x32x8_bf16 v[16:31], v[132:133], v[148:149], v[48:63]
v_exp_f32 v32, v32
v_exp_f32 v33, v33
v_exp_f32 v34, v34
v_exp_f32 v35, v35
v_mfma_f32_32x32x16_bf16 v[80:95], v[128:131], v[116:119], v[80:95]
s_waitcnt lgkmcnt(0)
s_add_u32 m0, s60, 0
s_add_u32 s62, s61, 0
buffer_load_dwordx4 v214, s[56:59], s62 offen lds
buffer_load_dwordx4 v215, s[56:59], s62 offen offset:1024 lds
buffer_load_dwordx4 v216, s[56:59], s62 offen offset:2048 lds
v_exp_f32 v36, v36
v_exp_f32 v37, v37
v_cvt_pk_bf16_f32 v112, v32, v33
v_exp_f32 v38, v38
v_cvt_pk_bf16_f32 v113, v34, v35
v_exp_f32 v39, v39
v_mfma_f32_32x32x16_bf16 v[16:31], v[120:123], v[136:139], v[16:31]
v_exp_f32 v40, v40
v_exp_f32 v41, v41
v_cvt_pk_bf16_f32 v114, v36, v37
v_exp_f32 v42, v42
v_cvt_pk_bf16_f32 v115, v38, v39
v_exp_f32 v43, v43
v_exp_f32 v44, v44
v_mfma_f32_32x32x16_bf16 v[96:111], v[124:127], v[112:115], v[96:111]
ds_read_b128 v[124:127], v9 offset:2432
v_exp_f32 v45, v45
v_exp_f32 v46, v46
v_cvt_pk_bf16_f32 v116, v40, v41
v_exp_f32 v47, v47
v_cvt_pk_bf16_f32 v117, v42, v43
v_cvt_pk_bf16_f32 v118, v44, v45
v_cvt_pk_bf16_f32 v119, v46, v47
v_mfma_f32_32x32x8_bf16 v[32:47], v[132:133], v[156:157], v[64:79]
v_exp_f32 v16, v16
v_exp_f32 v17, v17
v_exp_f32 v18, v18
v_exp_f32 v19, v19
v_mfma_f32_32x32x16_bf16 v[96:111], v[128:131], v[116:119], v[96:111]
ds_read_b128 v[128:131], v9 offset:3040
s_add_u32 m0, s60, 12416
s_add_u32 s62, s61, 73728
buffer_load_dwordx4 v214, s[56:59], s62 offen lds
buffer_load_dwordx4 v215, s[56:59], s62 offen offset:1024 lds
buffer_load_dwordx4 v216, s[56:59], s62 offen offset:2048 lds
v_exp_f32 v20, v20
v_exp_f32 v21, v21
v_cvt_pk_bf16_f32 v112, v16, v17
v_exp_f32 v22, v22
v_cvt_pk_bf16_f32 v113, v18, v19
v_exp_f32 v23, v23
v_mfma_f32_32x32x16_bf16 v[32:47], v[120:123], v[140:143], v[32:47]
s_waitcnt lgkmcnt(1)
ds_read_b128 v[120:123], v6 offset:3072
v_exp_f32 v24, v24
v_exp_f32 v25, v25
v_cvt_pk_bf16_f32 v114, v20, v21
v_exp_f32 v26, v26
v_cvt_pk_bf16_f32 v115, v22, v23
v_exp_f32 v27, v27
v_exp_f32 v28, v28
v_mfma_f32_32x32x16_bf16 v[80:95], v[124:127], v[112:115], v[80:95]
s_waitcnt lgkmcnt(1)
v_exp_f32 v29, v29
v_exp_f32 v30, v30
v_cvt_pk_bf16_f32 v116, v24, v25
v_exp_f32 v31, v31
v_cvt_pk_bf16_f32 v117, v26, v27
v_cvt_pk_bf16_f32 v118, v28, v29
v_cvt_pk_bf16_f32 v119, v30, v31
v_mfma_f32_32x32x8_bf16 v[16:31], v[132:133], v[150:151], v[48:63]
v_exp_f32 v32, v32
v_exp_f32 v33, v33
v_exp_f32 v34, v34
v_exp_f32 v35, v35
v_mfma_f32_32x32x16_bf16 v[80:95], v[128:131], v[116:119], v[80:95]
s_waitcnt lgkmcnt(0)
s_add_u32 m0, s60, 24832
s_add_u32 s62, s61, 147456
buffer_load_dwordx4 v214, s[56:59], s62 offen lds
buffer_load_dwordx4 v215, s[56:59], s62 offen offset:1024 lds
buffer_load_dwordx4 v216, s[56:59], s62 offen offset:2048 lds
v_exp_f32 v36, v36
v_exp_f32 v37, v37
v_cvt_pk_bf16_f32 v112, v32, v33
v_exp_f32 v38, v38
v_cvt_pk_bf16_f32 v113, v34, v35
v_exp_f32 v39, v39
v_mfma_f32_32x32x16_bf16 v[16:31], v[120:123], v[136:139], v[16:31]
v_exp_f32 v40, v40
v_exp_f32 v41, v41
v_cvt_pk_bf16_f32 v114, v36, v37
v_exp_f32 v42, v42
v_cvt_pk_bf16_f32 v115, v38, v39
v_exp_f32 v43, v43
v_exp_f32 v44, v44
v_mfma_f32_32x32x16_bf16 v[96:111], v[124:127], v[112:115], v[96:111]
ds_read_b128 v[124:127], v9 offset:3648
v_exp_f32 v45, v45
v_exp_f32 v46, v46
v_cvt_pk_bf16_f32 v116, v40, v41
v_exp_f32 v47, v47
v_cvt_pk_bf16_f32 v117, v42, v43
v_cvt_pk_bf16_f32 v118, v44, v45
v_cvt_pk_bf16_f32 v119, v46, v47
v_mfma_f32_32x32x8_bf16 v[32:47], v[132:133], v[158:159], v[64:79]
v_exp_f32 v16, v16
v_exp_f32 v17, v17
v_exp_f32 v18, v18
v_exp_f32 v19, v19
v_mfma_f32_32x32x16_bf16 v[96:111], v[128:131], v[116:119], v[96:111]
ds_read_b128 v[128:131], v9 offset:4256
ds_read_b64 v[132:133], v12 offset:512
v_exp_f32 v20, v20
v_exp_f32 v21, v21
v_cvt_pk_bf16_f32 v112, v16, v17
v_exp_f32 v22, v22
v_cvt_pk_bf16_f32 v113, v18, v19
v_exp_f32 v23, v23
v_mfma_f32_32x32x16_bf16 v[32:47], v[120:123], v[140:143], v[32:47]
s_waitcnt lgkmcnt(2)
ds_read_b128 v[120:123], v6 offset:4096
v_exp_f32 v24, v24
v_exp_f32 v25, v25
v_cvt_pk_bf16_f32 v114, v20, v21
v_exp_f32 v26, v26
v_cvt_pk_bf16_f32 v115, v22, v23
v_exp_f32 v27, v27
v_exp_f32 v28, v28
v_mfma_f32_32x32x16_bf16 v[80:95], v[124:127], v[112:115], v[80:95]
s_waitcnt lgkmcnt(1)
v_exp_f32 v29, v29
v_exp_f32 v30, v30
v_cvt_pk_bf16_f32 v116, v24, v25
v_exp_f32 v31, v31
v_cvt_pk_bf16_f32 v117, v26, v27
v_cvt_pk_bf16_f32 v118, v28, v29
v_cvt_pk_bf16_f32 v119, v30, v31
v_mfma_f32_32x32x8_bf16 v[16:31], v[132:133], v[144:145], v[48:63]
v_exp_f32 v32, v32
v_exp_f32 v33, v33
v_exp_f32 v34, v34
v_exp_f32 v35, v35
v_mfma_f32_32x32x16_bf16 v[80:95], v[128:131], v[116:119], v[80:95]
s_waitcnt lgkmcnt(0)
s_add_u32 m0, s60, 37248
s_add_u32 s62, s61, 221184
buffer_load_dwordx4 v214, s[56:59], s62 offen lds
buffer_load_dwordx4 v215, s[56:59], s62 offen offset:1024 lds
buffer_load_dwordx4 v216, s[56:59], s62 offen offset:2048 lds
v_exp_f32 v36, v36
v_exp_f32 v37, v37
v_cvt_pk_bf16_f32 v112, v32, v33
v_exp_f32 v38, v38
v_cvt_pk_bf16_f32 v113, v34, v35
v_exp_f32 v39, v39
v_mfma_f32_32x32x16_bf16 v[16:31], v[120:123], v[136:139], v[16:31]
v_exp_f32 v40, v40
v_exp_f32 v41, v41
v_cvt_pk_bf16_f32 v114, v36, v37
v_exp_f32 v42, v42
v_cvt_pk_bf16_f32 v115, v38, v39
v_exp_f32 v43, v43
v_exp_f32 v44, v44
v_mfma_f32_32x32x16_bf16 v[96:111], v[124:127], v[112:115], v[96:111]
ds_read_b128 v[124:127], v9 offset:4864
v_exp_f32 v45, v45
v_exp_f32 v46, v46
v_cvt_pk_bf16_f32 v116, v40, v41
v_exp_f32 v47, v47
v_cvt_pk_bf16_f32 v117, v42, v43
v_cvt_pk_bf16_f32 v118, v44, v45
v_cvt_pk_bf16_f32 v119, v46, v47
v_mfma_f32_32x32x8_bf16 v[32:47], v[132:133], v[152:153], v[64:79]
v_exp_f32 v16, v16
v_exp_f32 v17, v17
v_exp_f32 v18, v18
v_exp_f32 v19, v19
v_mfma_f32_32x32x16_bf16 v[96:111], v[128:131], v[116:119], v[96:111]
LW0_5_%=:
s_cbranch_execz LWEND0_%=
ds_read_b128 v[128:131], v9 offset:5472
s_add_u32 m0, s60, 49664
s_add_u32 s62, s61, 294912
buffer_load_dwordx4 v214, s[56:59], s62 offen lds
buffer_load_dwordx4 v215, s[56:59], s62 offen offset:1024 lds
buffer_load_dwordx4 v216, s[56:59], s62 offen offset:2048 lds
v_exp_f32 v20, v20
v_exp_f32 v21, v21
v_cvt_pk_bf16_f32 v112, v16, v17
v_exp_f32 v22, v22
v_cvt_pk_bf16_f32 v113, v18, v19
v_exp_f32 v23, v23
)"
R"(v_mfma_f32_32x32x16_bf16 v[32:47], v[120:123], v[140:143], v[32:47]
s_waitcnt lgkmcnt(1)
ds_read_b128 v[120:123], v6 offset:5120
v_exp_f32 v24, v24
v_exp_f32 v25, v25
v_cvt_pk_bf16_f32 v114, v20, v21
v_exp_f32 v26, v26
v_cvt_pk_bf16_f32 v115, v22, v23
v_exp_f32 v27, v27
v_exp_f32 v28, v28
v_mfma_f32_32x32x16_bf16 v[80:95], v[124:127], v[112:115], v[80:95]
s_waitcnt lgkmcnt(1)
v_exp_f32 v29, v29
v_exp_f32 v30, v30
v_cvt_pk_bf16_f32 v116, v24, v25
v_exp_f32 v31, v31
v_cvt_pk_bf16_f32 v117, v26, v27
v_cvt_pk_bf16_f32 v118, v28, v29
v_cvt_pk_bf16_f32 v119, v30, v31
v_mfma_f32_32x32x8_bf16 v[16:31], v[132:133], v[146:147], v[48:63]
v_exp_f32 v32, v32
v_exp_f32 v33, v33
v_exp_f32 v34, v34
v_exp_f32 v35, v35
v_mfma_f32_32x32x16_bf16 v[80:95], v[128:131], v[116:119], v[80:95]
s_waitcnt lgkmcnt(0)
s_add_u32 m0, s60, 62080
s_add_u32 s62, s61, 368640
buffer_load_dwordx4 v214, s[56:59], s62 offen lds
buffer_load_dwordx4 v215, s[56:59], s62 offen offset:1024 lds
buffer_load_dwordx4 v216, s[56:59], s62 offen offset:2048 lds
v_exp_f32 v36, v36
v_exp_f32 v37, v37
v_cvt_pk_bf16_f32 v112, v32, v33
v_exp_f32 v38, v38
v_cvt_pk_bf16_f32 v113, v34, v35
v_exp_f32 v39, v39
v_mfma_f32_32x32x16_bf16 v[16:31], v[120:123], v[136:139], v[16:31]
v_exp_f32 v40, v40
v_exp_f32 v41, v41
v_cvt_pk_bf16_f32 v114, v36, v37
v_exp_f32 v42, v42
v_cvt_pk_bf16_f32 v115, v38, v39
v_exp_f32 v43, v43
v_exp_f32 v44, v44
v_mfma_f32_32x32x16_bf16 v[96:111], v[124:127], v[112:115], v[96:111]
ds_read_b128 v[124:127], v9 offset:6080
v_exp_f32 v45, v45
v_exp_f32 v46, v46
v_cvt_pk_bf16_f32 v116, v40, v41
v_exp_f32 v47, v47
v_cvt_pk_bf16_f32 v117, v42, v43
v_cvt_pk_bf16_f32 v118, v44, v45
v_cvt_pk_bf16_f32 v119, v46, v47
v_mfma_f32_32x32x8_bf16 v[32:47], v[132:133], v[154:155], v[64:79]
v_exp_f32 v16, v16
v_exp_f32 v17, v17
v_exp_f32 v18, v18
v_exp_f32 v19, v19
v_mfma_f32_32x32x16_bf16 v[96:111], v[128:131], v[116:119], v[96:111]
ds_read_b128 v[128:131], v9 offset:6688
v_exp_f32 v20, v20
v_exp_f32 v21, v21
v_cvt_pk_bf16_f32 v112, v16, v17
v_exp_f32 v22, v22
v_cvt_pk_bf16_f32 v113, v18, v19
v_exp_f32 v23, v23
v_mfma_f32_32x32x16_bf16 v[32:47], v[120:123], v[140:143], v[32:47]
s_waitcnt lgkmcnt(1)
ds_read_b128 v[120:123], v6 offset:6144
v_exp_f32 v24, v24
v_exp_f32 v25, v25
v_cvt_pk_bf16_f32 v114, v20, v21
v_exp_f32 v26, v26
v_cvt_pk_bf16_f32 v115, v22, v23
v_exp_f32 v27, v27
v_exp_f32 v28, v28
v_mfma_f32_32x32x16_bf16 v[80:95], v[124:127], v[112:115], v[80:95]
s_waitcnt lgkmcnt(1)
v_exp_f32 v29, v29
v_exp_f32 v30, v30
v_cvt_pk_bf16_f32 v116, v24, v25
v_exp_f32 v31, v31
v_cvt_pk_bf16_f32 v117, v26, v27
v_cvt_pk_bf16_f32 v118, v28, v29
v_cvt_pk_bf16_f32 v119, v30, v31
v_mfma_f32_32x32x8_bf16 v[16:31], v[132:133], v[148:149], v[48:63]
v_exp_f32 v32, v32
v_exp_f32 v33, v33
v_exp_f32 v34, v34
v_exp_f32 v35, v35
v_mfma_f32_32x32x16_bf16 v[80:95], v[128:131], v[116:119], v[80:95]
s_waitcnt lgkmcnt(0)
s_add_u32 m0, s60, 74496
s_add_u32 s62, s61, 442368
buffer_load_dwordx4 v214, s[56:59], s62 offen lds
buffer_load_dwordx4 v215, s[56:59], s62 offen offset:1024 lds
buffer_load_dwordx4 v216, s[56:59], s62 offen offset:2048 lds
v_exp_f32 v36, v36
v_exp_f32 v37, v37
v_cvt_pk_bf16_f32 v112, v32, v33
v_exp_f32 v38, v38
v_cvt_pk_bf16_f32 v113, v34, v35
v_exp_f32 v39, v39
v_mfma_f32_32x32x16_bf16 v[16:31], v[120:123], v[136:139], v[16:31]
v_exp_f32 v40, v40
v_exp_f32 v41, v41
v_cvt_pk_bf16_f32 v114, v36, v37
v_exp_f32 v42, v42
v_cvt_pk_bf16_f32 v115, v38, v39
v_exp_f32 v43, v43
v_exp_f32 v44, v44
v_mfma_f32_32x32x16_bf16 v[96:111], v[124:127], v[112:115], v[96:111]
ds_read_b128 v[124:127], v9 offset:7296
v_exp_f32 v45, v45
v_exp_f32 v46, v46
v_cvt_pk_bf16_f32 v116, v40, v41
v_exp_f32 v47, v47
v_cvt_pk_bf16_f32 v117, v42, v43
v_cvt_pk_bf16_f32 v118, v44, v45
v_cvt_pk_bf16_f32 v119, v46, v47
v_mfma_f32_32x32x8_bf16 v[32:47], v[132:133], v[156:157], v[64:79]
v_exp_f32 v16, v16
v_exp_f32 v17, v17
v_exp_f32 v18, v18
v_exp_f32 v19, v19
v_mfma_f32_32x32x16_bf16 v[96:111], v[128:131], v[116:119], v[96:111]
ds_read_b128 v[128:131], v9 offset:7904
v_exp_f32 v20, v20
v_exp_f32 v21, v21
v_cvt_pk_bf16_f32 v112, v16, v17
v_exp_f32 v22, v22
v_cvt_pk_bf16_f32 v113, v18, v19
v_exp_f32 v23, v23
v_mfma_f32_32x32x16_bf16 v[32:47], v[120:123], v[140:143], v[32:47]
s_waitcnt lgkmcnt(1)
ds_read_b128 v[120:123], v6 offset:7168
v_exp_f32 v24, v24
v_exp_f32 v25, v25
v_cvt_pk_bf16_f32 v114, v20, v21
v_exp_f32 v26, v26
v_cvt_pk_bf16_f32 v115, v22, v23
v_exp_f32 v27, v27
v_exp_f32 v28, v28
v_mfma_f32_32x32x16_bf16 v[80:95], v[124:127], v[112:115], v[80:95]
s_waitcnt lgkmcnt(1)
v_exp_f32 v29, v29
v_exp_f32 v30, v30
v_cvt_pk_bf16_f32 v116, v24, v25
v_exp_f32 v31, v31
v_cvt_pk_bf16_f32 v117, v26, v27
v_cvt_pk_bf16_f32 v118, v28, v29
v_cvt_pk_bf16_f32 v119, v30, v31
v_mfma_f32_32x32x8_bf16 v[16:31], v[132:133], v[150:151], v[48:63]
v_exp_f32 v32, v32
v_exp_f32 v33, v33
v_exp_f32 v34, v34
v_exp_f32 v35, v35
v_mfma_f32_32x32x16_bf16 v[80:95], v[128:131], v[116:119], v[80:95]
s_waitcnt lgkmcnt(0)
v_exp_f32 v36, v36
v_exp_f32 v37, v37
v_cvt_pk_bf16_f32 v112, v32, v33
v_exp_f32 v38, v38
v_cvt_pk_bf16_f32 v113, v34, v35
v_exp_f32 v39, v39
v_mfma_f32_32x32x16_bf16 v[16:31], v[120:123], v[136:139], v[16:31]
v_exp_f32 v40, v40
v_exp_f32 v41, v41
v_cvt_pk_bf16_f32 v114, v36, v37
v_exp_f32 v42, v42
v_cvt_pk_bf16_f32 v115, v38, v39
v_exp_f32 v43, v43
v_exp_f32 v44, v44
v_mfma_f32_32x32x16_bf16 v[96:111], v[124:127], v[112:115], v[96:111]
ds_read_b128 v[124:127], v9 offset:8512
v_exp_f32 v45, v45
v_exp_f32 v46, v46
v_cvt_pk_bf16_f32 v116, v40, v41
v_exp_f32 v47, v47
v_cvt_pk_bf16_f32 v117, v42, v43
v_cvt_pk_bf16_f32 v118, v44, v45
v_cvt_pk_bf16_f32 v119, v46, v47
v_mfma_f32_32x32x8_bf16 v[32:47], v[132:133], v[158:159], v[64:79]
v_exp_f32 v16, v16
v_exp_f32 v17, v17
v_exp_f32 v18, v18
v_exp_f32 v19, v19
v_mfma_f32_32x32x16_bf16 v[96:111], v[128:131], v[116:119], v[96:111]
ds_read_b128 v[128:131], v9 offset:9120
ds_read_b64 v[132:133], v12 offset:18944
v_exp_f32 v20, v20
v_exp_f32 v21, v21
v_cvt_pk_bf16_f32 v112, v16, v17
v_exp_f32 v22, v22
v_cvt_pk_bf16_f32 v113, v18, v19
v_exp_f32 v23, v23
v_mfma_f32_32x32x16_bf16 v[32:47], v[120:123], v[140:143], v[32:47]
s_waitcnt lgkmcnt(2)
ds_read_b128 v[120:123], v6 offset:18944
v_exp_f32 v24, v24
v_exp_f32 v25, v25
v_cvt_pk_bf16_f32 v114, v20, v21
v_exp_f32 v26, v26
v_cvt_pk_bf16_f32 v115, v22, v23
v_exp_f32 v27, v27
v_exp_f32 v28, v28
v_mfma_f32_32x32x16_bf16 v[80:95], v[124:127], v[112:115], v[80:95]
s_waitcnt lgkmcnt(1)
v_exp_f32 v29, v29
v_exp_f32 v30, v30
v_cvt_pk_bf16_f32 v116, v24, v25
v_exp_f32 v31, v31
v_cvt_pk_bf16_f32 v117, v26, v27
v_cvt_pk_bf16_f32 v118, v28, v29
v_cvt_pk_bf16_f32 v119, v30, v31
v_mfma_f32_32x32x8_bf16 v[16:31], v[132:133], v[144:145], v[48:63]
v_exp_f32 v32, v32
v_exp_f32 v33, v33
v_exp_f32 v34, v34
v_exp_f32 v35, v35
v_mfma_f32_32x32x16_bf16 v[80:95], v[128:131], v[116:119], v[80:95]
s_waitcnt lgkmcnt(0)
global_load_dwordx4 v[176:179], v13, s[50:51] sc0 sc1
global_load_dwordx4 v[180:183], v13, s[50:51] offset:16 sc0 sc1
v_exp_f32 v36, v36
v_exp_f32 v37, v37
v_cvt_pk_bf16_f32 v112, v32, v33
v_exp_f32 v38, v38
v_cvt_pk_bf16_f32 v113, v34, v35
v_exp_f32 v39, v39
v_mfma_f32_32x32x16_bf16 v[16:31], v[120:123], v[136:139], v[16:31]
v_exp_f32 v40, v40
v_exp_f32 v41, v41
v_cvt_pk_bf16_f32 v114, v36, v37
v_exp_f32 v42, v42
v_cvt_pk_bf16_f32 v115, v38, v39
v_exp_f32 v43, v43
v_exp_f32 v44, v44
v_mfma_f32_32x32x16_bf16 v[96:111], v[124:127], v[112:115], v[96:111]
ds_read_b128 v[124:127], v9 offset:18944
v_exp_f32 v45, v45
v_exp_f32 v46, v46
v_cvt_pk_bf16_f32 v116, v40, v41
v_exp_f32 v47, v47
v_cvt_pk_bf16_f32 v117, v42, v43
v_cvt_pk_bf16_f32 v118, v44, v45
v_cvt_pk_bf16_f32 v119, v46, v47
v_mfma_f32_32x32x8_bf16 v[32:47], v[132:133], v[152:153], v[64:79]
v_exp_f32 v16, v16
v_exp_f32 v17, v17
v_exp_f32 v18, v18
v_exp_f32 v19, v19
v_mfma_f32_32x32x16_bf16 v[96:111], v[128:131], v[116:119], v[96:111]
ds_read_b128 v[128:131], v9 offset:19552
s_add_u32 m0, s60, 86912
s_add_u32 s62, s61, 516096
buffer_load_dwordx4 v214, s[56:59], s62 offen lds
buffer_load_dwordx4 v215, s[56:59], s62 offen offset:1024 lds
buffer_load_dwordx4 v216, s[56:59], s62 offen offset:2048 lds
v_exp_f32 v20, v20
v_exp_f32 v21, v21
v_cvt_pk_bf16_f32 v112, v16, v17
v_exp_f32 v22, v22
v_cvt_pk_bf16_f32 v113, v18, v19
v_exp_f32 v23, v23
v_mfma_f32_32x32x16_bf16 v[32:47], v[120:123], v[140:143], v[32:47]
s_waitcnt lgkmcnt(1)
ds_read_b128 v[120:123], v6 offset:19968
v_exp_f32 v24, v24
v_exp_f32 v25, v25
v_cvt_pk_bf16_f32 v114, v20, v21
v_exp_f32 v26, v26
v_cvt_pk_bf16_f32 v115, v22, v23
v_exp_f32 v27, v27
v_exp_f32 v28, v28
v_mfma_f32_32x32x16_bf16 v[80:95], v[124:127], v[112:115], v[80:95]
s_waitcnt lgkmcnt(1)
v_exp_f32 v29, v29
v_exp_f32 v30, v30
v_cvt_pk_bf16_f32 v116, v24, v25
v_exp_f32 v31, v31
v_cvt_pk_bf16_f32 v117, v26, v27
v_cvt_pk_bf16_f32 v118, v28, v29
v_cvt_pk_bf16_f32 v119, v30, v31
v_mfma_f32_32x32x8_bf16 v[16:31], v[132:133], v[146:147], v[48:63]
v_exp_f32 v32, v32
v_exp_f32 v33, v33
v_exp_f32 v34, v34
v_exp_f32 v35, v35
v_mfma_f32_32x32x16_bf16 v[80:95], v[128:131], v[116:119], v[80:95]
s_waitcnt lgkmcnt(0)
s_add_u32 m0, s60, 99328
s_add_u32 s62, s61, 589824
buffer_load_dwordx4 v214, s[56:59], s62 offen lds
buffer_load_dwordx4 v215, s[56:59], s62 offen offset:1024 lds
buffer_load_dwordx4 v216, s[56:59], s62 offen offset:2048 lds
v_exp_f32 v36, v36
v_exp_f32 v37, v37
v_cvt_pk_bf16_f32 v112, v32, v33
v_exp_f32 v38, v38
v_cvt_pk_bf16_f32 v113, v34, v35
v_exp_f32 v39, v39
v_mfma_f32_32x32x16_bf16 v[16:31], v[120:123], v[136:139], v[16:31]
v_exp_f32 v40, v40
v_exp_f32 v41, v41
v_cvt_pk_bf16_f32 v114, v36, v37
v_exp_f32 v42, v42
v_cvt_pk_bf16_f32 v115, v38, v39
v_exp_f32 v43, v43
v_exp_f32 v44, v44
v_mfma_f32_32x32x16_bf16 v[96:111], v[124:127], v[112:115], v[96:111]
ds_read_b128 v[124:127], v9 offset:20160
v_exp_f32 v45, v45
v_exp_f32 v46, v46
v_cvt_pk_bf16_f32 v116, v40, v41
v_exp_f32 v47, v47
v_cvt_pk_bf16_f32 v117, v42, v43
v_cvt_pk_bf16_f32 v118, v44, v45
v_cvt_pk_bf16_f32 v119, v46, v47
v_mfma_f32_32x32x8_bf16 v[32:47], v[132:133], v[154:155], v[64:79]
v_exp_f32 v16, v16
v_exp_f32 v17, v17
v_exp_f32 v18, v18
v_exp_f32 v19, v19
v_mfma_f32_32x32x16_bf16 v[96:111], v[128:131], v[116:119], v[96:111]
ds_read_b128 v[128:131], v9 offset:20768
v_exp_f32 v20, v20
v_exp_f32 v21, v21
v_cvt_pk_bf16_f32 v112, v16, v17
v_exp_f32 v22, v22
v_cvt_pk_bf16_f32 v113, v18, v19
v_exp_f32 v23, v23
v_mfma_f32_32x32x16_bf16 v[32:47], v[120:123], v[140:143], v[32:47]
s_waitcnt lgkmcnt(1)
ds_read_b128 v[120:123], v6 offset:20992
v_exp_f32 v24, v24
v_exp_f32 v25, v25
v_cvt_pk_bf16_f32 v114, v20, v21
v_exp_f32 v26, v26
v_cvt_pk_bf16_f32 v115, v22, v23
v_exp_f32 v27, v27
v_exp_f32 v28, v28
v_mfma_f32_32x32x16_bf16 v[80:95], v[124:127], v[112:115], v[80:95]
s_waitcnt lgkmcnt(1)
v_exp_f32 v29, v29
v_exp_f32 v30, v30
v_cvt_pk_bf16_f32 v116, v24, v25
v_exp_f32 v31, v31
v_cvt_pk_bf16_f32 v117, v26, v27
v_cvt_pk_bf16_f32 v118, v28, v29
v_cvt_pk_bf16_f32 v119, v30, v31
s_waitcnt vmcnt(7)
v_xor_b32 v176, s40, v176
v_xor_b32 v177, s41, v177
v_xor_b32 v178, s40, v178
v_xor_b32 v179, s41, v179
s_waitcnt vmcnt(6)
v_xor_b32 v180, s40, v180
v_xor_b32 v181, s41, v181
v_xor_b32 v182, s40, v182
v_xor_b32 v183, s41, v183
v_or_b32 v163, v176, v177
v_or3_b32 v163, v163, v178, v179
v_or3_b32 v163, v163, v180, v181
v_or3_b32 v163, v163, v182, v183
v_cmp_ne_u32 vcc, 0, v163
s_nop 0
s_cmp_eq_u64 vcc, 0
s_cbranch_scc0 Lx_20_%=
Lr_21_%=:
s_add_u32 s8, s50, s83
s_addc_u32 s9, s51, 0
s_and_b32 s9, s9, 0xffff
s_mov_b32 s10, 331776
s_mov_b32 s11, 131072
v_mfma_f32_32x32x8_bf16 v[16:31], v[132:133], v[148:149], v[48:63]
v_exp_f32 v32, v32
v_exp_f32 v33, v33
v_exp_f32 v34, v34
v_exp_f32 v35, v35
v_mfma_f32_32x32x16_bf16 v[80:95], v[128:131], v[116:119], v[80:95]
s_waitcnt lgkmcnt(0)
s_mov_b32 s63, 0
buffer_load_dwordx4 a[0:3], v4, s[8:11], s63 offen sc1
buffer_load_dwordx4 a[4:7], v4, s[8:11], s63 offen offset:1024 sc1
buffer_load_dwordx4 a[8:11], v4, s[8:11], s63 offen offset:2048 sc1
v_exp_f32 v36, v36
v_exp_f32 v37, v37
v_cvt_pk_bf16_f32 v112, v32, v33
v_exp_f32 v38, v38
v_cvt_pk_bf16_f32 v113, v34, v35
v_exp_f32 v39, v39
v_mfma_f32_32x32x16_bf16 v[16:31], v[120:123], v[136:139], v[16:31]
v_exp_f32 v40, v40
v_exp_f32 v41, v41
v_cvt_pk_bf16_f32 v114, v36, v37
v_exp_f32 v42, v42
v_cvt_pk_bf16_f32 v115, v38, v39
v_exp_f32 v43, v43
)"
R"(v_exp_f32 v44, v44
v_mfma_f32_32x32x16_bf16 v[96:111], v[124:127], v[112:115], v[96:111]
ds_read_b128 v[124:127], v9 offset:21376
v_exp_f32 v45, v45
v_exp_f32 v46, v46
v_cvt_pk_bf16_f32 v116, v40, v41
v_exp_f32 v47, v47
v_cvt_pk_bf16_f32 v117, v42, v43
v_cvt_pk_bf16_f32 v118, v44, v45
v_cvt_pk_bf16_f32 v119, v46, v47
v_mfma_f32_32x32x8_bf16 v[32:47], v[132:133], v[156:157], v[64:79]
v_exp_f32 v16, v16
v_exp_f32 v17, v17
v_exp_f32 v18, v18
v_exp_f32 v19, v19
v_mfma_f32_32x32x16_bf16 v[96:111], v[128:131], v[116:119], v[96:111]
ds_read_b128 v[128:131], v9 offset:21984
buffer_load_dwordx4 a[12:15], v4, s[8:11], s63 offen offset:3072 sc1
s_mov_b32 s63, 4096
buffer_load_dwordx4 a[16:19], v4, s[8:11], s63 offen sc1
buffer_load_dwordx4 a[20:23], v4, s[8:11], s63 offen offset:1024 sc1
v_exp_f32 v20, v20
v_exp_f32 v21, v21
v_cvt_pk_bf16_f32 v112, v16, v17
v_exp_f32 v22, v22
v_cvt_pk_bf16_f32 v113, v18, v19
v_exp_f32 v23, v23
v_mfma_f32_32x32x16_bf16 v[32:47], v[120:123], v[140:143], v[32:47]
s_waitcnt lgkmcnt(1)
ds_read_b128 v[120:123], v6 offset:22016
v_exp_f32 v24, v24
v_exp_f32 v25, v25
v_cvt_pk_bf16_f32 v114, v20, v21
v_exp_f32 v26, v26
v_cvt_pk_bf16_f32 v115, v22, v23
v_exp_f32 v27, v27
v_exp_f32 v28, v28
v_mfma_f32_32x32x16_bf16 v[80:95], v[124:127], v[112:115], v[80:95]
s_waitcnt lgkmcnt(1)
v_exp_f32 v29, v29
v_exp_f32 v30, v30
v_cvt_pk_bf16_f32 v116, v24, v25
v_exp_f32 v31, v31
v_cvt_pk_bf16_f32 v117, v26, v27
v_cvt_pk_bf16_f32 v118, v28, v29
v_cvt_pk_bf16_f32 v119, v30, v31
v_mfma_f32_32x32x8_bf16 v[16:31], v[132:133], v[150:151], v[48:63]
v_exp_f32 v32, v32
v_exp_f32 v33, v33
v_exp_f32 v34, v34
v_exp_f32 v35, v35
v_mfma_f32_32x32x16_bf16 v[80:95], v[128:131], v[116:119], v[80:95]
s_waitcnt lgkmcnt(0)
buffer_load_dwordx4 a[24:27], v4, s[8:11], s63 offen offset:2048 sc1
buffer_load_dwordx4 a[28:31], v4, s[8:11], s63 offen offset:3072 sc1
s_mov_b32 s63, 8192
buffer_load_dwordx4 a[32:35], v4, s[8:11], s63 offen sc1
v_exp_f32 v36, v36
v_exp_f32 v37, v37
v_cvt_pk_bf16_f32 v112, v32, v33
v_exp_f32 v38, v38
v_cvt_pk_bf16_f32 v113, v34, v35
v_exp_f32 v39, v39
v_mfma_f32_32x32x16_bf16 v[16:31], v[120:123], v[136:139], v[16:31]
v_exp_f32 v40, v40
v_exp_f32 v41, v41
v_cvt_pk_bf16_f32 v114, v36, v37
v_exp_f32 v42, v42
v_cvt_pk_bf16_f32 v115, v38, v39
v_exp_f32 v43, v43
v_exp_f32 v44, v44
v_mfma_f32_32x32x16_bf16 v[96:111], v[124:127], v[112:115], v[96:111]
ds_read_b128 v[124:127], v9 offset:22592
v_exp_f32 v45, v45
v_exp_f32 v46, v46
v_cvt_pk_bf16_f32 v116, v40, v41
v_exp_f32 v47, v47
v_cvt_pk_bf16_f32 v117, v42, v43
v_cvt_pk_bf16_f32 v118, v44, v45
v_cvt_pk_bf16_f32 v119, v46, v47
v_mfma_f32_32x32x8_bf16 v[32:47], v[132:133], v[158:159], v[64:79]
v_exp_f32 v16, v16
v_exp_f32 v17, v17
v_exp_f32 v18, v18
v_exp_f32 v19, v19
v_mfma_f32_32x32x16_bf16 v[96:111], v[128:131], v[116:119], v[96:111]
ds_read_b128 v[128:131], v9 offset:23200
ds_read_b64 v[132:133], v12 offset:19456
buffer_load_dwordx4 a[36:39], v4, s[8:11], s63 offen offset:1024 sc1
buffer_load_dwordx4 a[40:43], v4, s[8:11], s63 offen offset:2048 sc1
buffer_load_dwordx4 a[44:47], v4, s[8:11], s63 offen offset:3072 sc1
v_exp_f32 v20, v20
v_exp_f32 v21, v21
v_cvt_pk_bf16_f32 v112, v16, v17
v_exp_f32 v22, v22
v_cvt_pk_bf16_f32 v113, v18, v19
v_exp_f32 v23, v23
v_mfma_f32_32x32x16_bf16 v[32:47], v[120:123], v[140:143], v[32:47]
s_waitcnt lgkmcnt(2)
ds_read_b128 v[120:123], v6 offset:23040
v_exp_f32 v24, v24
v_exp_f32 v25, v25
v_cvt_pk_bf16_f32 v114, v20, v21
v_exp_f32 v26, v26
v_cvt_pk_bf16_f32 v115, v22, v23
v_exp_f32 v27, v27
v_exp_f32 v28, v28
v_mfma_f32_32x32x16_bf16 v[80:95], v[124:127], v[112:115], v[80:95]
s_waitcnt lgkmcnt(1)
v_exp_f32 v29, v29
v_exp_f32 v30, v30
v_cvt_pk_bf16_f32 v116, v24, v25
v_exp_f32 v31, v31
v_cvt_pk_bf16_f32 v117, v26, v27
v_cvt_pk_bf16_f32 v118, v28, v29
v_cvt_pk_bf16_f32 v119, v30, v31
v_mfma_f32_32x32x8_bf16 v[16:31], v[132:133], v[144:145], v[48:63]
v_exp_f32 v32, v32
v_exp_f32 v33, v33
v_exp_f32 v34, v34
v_exp_f32 v35, v35
v_mfma_f32_32x32x16_bf16 v[80:95], v[128:131], v[116:119], v[80:95]
s_waitcnt lgkmcnt(0)
s_mov_b32 s63, 12288
buffer_load_dwordx4 a[48:51], v4, s[8:11], s63 offen sc1
buffer_load_dwordx4 a[52:55], v4, s[8:11], s63 offen offset:1024 sc1
buffer_load_dwordx4 a[56:59], v4, s[8:11], s63 offen offset:2048 sc1
v_exp_f32 v36, v36
v_exp_f32 v37, v37
v_cvt_pk_bf16_f32 v112, v32, v33
v_exp_f32 v38, v38
v_cvt_pk_bf16_f32 v113, v34, v35
v_exp_f32 v39, v39
v_mfma_f32_32x32x16_bf16 v[16:31], v[120:123], v[136:139], v[16:31]
v_exp_f32 v40, v40
v_exp_f32 v41, v41
v_cvt_pk_bf16_f32 v114, v36, v37
v_exp_f32 v42, v42
v_cvt_pk_bf16_f32 v115, v38, v39
v_exp_f32 v43, v43
v_exp_f32 v44, v44
v_mfma_f32_32x32x16_bf16 v[96:111], v[124:127], v[112:115], v[96:111]
ds_read_b128 v[124:127], v9 offset:23808
v_exp_f32 v45, v45
v_exp_f32 v46, v46
v_cvt_pk_bf16_f32 v116, v40, v41
v_exp_f32 v47, v47
v_cvt_pk_bf16_f32 v117, v42, v43
v_cvt_pk_bf16_f32 v118, v44, v45
v_cvt_pk_bf16_f32 v119, v46, v47
v_mfma_f32_32x32x8_bf16 v[32:47], v[132:133], v[152:153], v[64:79]
v_exp_f32 v16, v16
v_exp_f32 v17, v17
v_exp_f32 v18, v18
v_exp_f32 v19, v19
v_mfma_f32_32x32x16_bf16 v[96:111], v[128:131], v[116:119], v[96:111]
ds_read_b128 v[128:131], v9 offset:24416
buffer_load_dwordx4 a[60:63], v4, s[8:11], s63 offen offset:3072 sc1
s_mov_b32 s63, 16384
buffer_load_dwordx4 a[64:67], v4, s[8:11], s63 offen sc1
buffer_load_dwordx4 a[68:71], v4, s[8:11], s63 offen offset:1024 sc1
v_exp_f32 v20, v20
v_exp_f32 v21, v21
v_cvt_pk_bf16_f32 v112, v16, v17
v_exp_f32 v22, v22
v_cvt_pk_bf16_f32 v113, v18, v19
v_exp_f32 v23, v23
v_mfma_f32_32x32x16_bf16 v[32:47], v[120:123], v[140:143], v[32:47]
s_waitcnt lgkmcnt(1)
ds_read_b128 v[120:123], v6 offset:24064
v_exp_f32 v24, v24
v_exp_f32 v25, v25
v_cvt_pk_bf16_f32 v114, v20, v21
v_exp_f32 v26, v26
v_cvt_pk_bf16_f32 v115, v22, v23
v_exp_f32 v27, v27
v_exp_f32 v28, v28
v_mfma_f32_32x32x16_bf16 v[80:95], v[124:127], v[112:115], v[80:95]
s_waitcnt lgkmcnt(1)
v_exp_f32 v29, v29
v_exp_f32 v30, v30
v_cvt_pk_bf16_f32 v116, v24, v25
v_exp_f32 v31, v31
v_cvt_pk_bf16_f32 v117, v26, v27
v_cvt_pk_bf16_f32 v118, v28, v29
v_cvt_pk_bf16_f32 v119, v30, v31
v_mfma_f32_32x32x8_bf16 v[16:31], v[132:133], v[146:147], v[48:63]
v_exp_f32 v32, v32
v_exp_f32 v33, v33
v_exp_f32 v34, v34
v_exp_f32 v35, v35
v_mfma_f32_32x32x16_bf16 v[80:95], v[128:131], v[116:119], v[80:95]
s_waitcnt lgkmcnt(0)
buffer_load_dwordx4 a[72:75], v4, s[8:11], s63 offen offset:2048 sc1
buffer_load_dwordx4 a[76:79], v4, s[8:11], s63 offen offset:3072 sc1
s_mov_b32 s63, 20480
buffer_load_dwordx4 a[80:83], v4, s[8:11], s63 offen sc1
v_exp_f32 v36, v36
v_exp_f32 v37, v37
v_cvt_pk_bf16_f32 v112, v32, v33
v_exp_f32 v38, v38
v_cvt_pk_bf16_f32 v113, v34, v35
v_exp_f32 v39, v39
v_mfma_f32_32x32x16_bf16 v[16:31], v[120:123], v[136:139], v[16:31]
v_exp_f32 v40, v40
v_exp_f32 v41, v41
v_cvt_pk_bf16_f32 v114, v36, v37
v_exp_f32 v42, v42
v_cvt_pk_bf16_f32 v115, v38, v39
v_exp_f32 v43, v43
v_exp_f32 v44, v44
v_mfma_f32_32x32x16_bf16 v[96:111], v[124:127], v[112:115], v[96:111]
ds_read_b128 v[124:127], v9 offset:25024
v_exp_f32 v45, v45
v_exp_f32 v46, v46
v_cvt_pk_bf16_f32 v116, v40, v41
v_exp_f32 v47, v47
v_cvt_pk_bf16_f32 v117, v42, v43
v_cvt_pk_bf16_f32 v118, v44, v45
v_cvt_pk_bf16_f32 v119, v46, v47
v_mfma_f32_32x32x8_bf16 v[32:47], v[132:133], v[154:155], v[64:79]
v_exp_f32 v16, v16
v_exp_f32 v17, v17
v_exp_f32 v18, v18
v_exp_f32 v19, v19
v_mfma_f32_32x32x16_bf16 v[96:111], v[128:131], v[116:119], v[96:111]
ds_read_b128 v[128:131], v9 offset:25632
buffer_load_dwordx4 a[84:87], v4, s[8:11], s63 offen offset:1024 sc1
buffer_load_dwordx4 a[88:91], v4, s[8:11], s63 offen offset:2048 sc1
buffer_load_dwordx4 a[92:95], v4, s[8:11], s63 offen offset:3072 sc1
v_exp_f32 v20, v20
v_exp_f32 v21, v21
v_cvt_pk_bf16_f32 v112, v16, v17
v_exp_f32 v22, v22
v_cvt_pk_bf16_f32 v113, v18, v19
v_exp_f32 v23, v23
v_mfma_f32_32x32x16_bf16 v[32:47], v[120:123], v[140:143], v[32:47]
s_waitcnt lgkmcnt(1)
ds_read_b128 v[120:123], v6 offset:25088
v_exp_f32 v24, v24
v_exp_f32 v25, v25
v_cvt_pk_bf16_f32 v114, v20, v21
v_exp_f32 v26, v26
v_cvt_pk_bf16_f32 v115, v22, v23
v_exp_f32 v27, v27
v_exp_f32 v28, v28
v_mfma_f32_32x32x16_bf16 v[80:95], v[124:127], v[112:115], v[80:95]
s_waitcnt lgkmcnt(1)
v_exp_f32 v29, v29
v_exp_f32 v30, v30
v_cvt_pk_bf16_f32 v116, v24, v25
v_exp_f32 v31, v31
v_cvt_pk_bf16_f32 v117, v26, v27
v_cvt_pk_bf16_f32 v118, v28, v29
v_cvt_pk_bf16_f32 v119, v30, v31
v_mfma_f32_32x32x8_bf16 v[16:31], v[132:133], v[148:149], v[48:63]
v_exp_f32 v32, v32
v_exp_f32 v33, v33
v_exp_f32 v34, v34
v_exp_f32 v35, v35
v_mfma_f32_32x32x16_bf16 v[80:95], v[128:131], v[116:119], v[80:95]
s_waitcnt lgkmcnt(0)
s_mov_b32 s63, 24576
buffer_load_dwordx4 a[96:99], v4, s[8:11], s63 offen sc1
buffer_load_dwordx4 a[100:103], v4, s[8:11], s63 offen offset:1024 sc1
buffer_load_dwordx4 a[104:107], v4, s[8:11], s63 offen offset:2048 sc1
v_exp_f32 v36, v36
v_exp_f32 v37, v37
v_cvt_pk_bf16_f32 v112, v32, v33
v_exp_f32 v38, v38
v_cvt_pk_bf16_f32 v113, v34, v35
v_exp_f32 v39, v39
v_mfma_f32_32x32x16_bf16 v[16:31], v[120:123], v[136:139], v[16:31]
v_exp_f32 v40, v40
v_exp_f32 v41, v41
v_cvt_pk_bf16_f32 v114, v36, v37
v_exp_f32 v42, v42
v_cvt_pk_bf16_f32 v115, v38, v39
v_exp_f32 v43, v43
v_exp_f32 v44, v44
v_mfma_f32_32x32x16_bf16 v[96:111], v[124:127], v[112:115], v[96:111]
LW0_6_%=:
s_cbranch_execz LWEND0_%=
ds_read_b128 v[124:127], v9 offset:26240
v_exp_f32 v45, v45
v_exp_f32 v46, v46
v_cvt_pk_bf16_f32 v116, v40, v41
v_exp_f32 v47, v47
v_cvt_pk_bf16_f32 v117, v42, v43
v_cvt_pk_bf16_f32 v118, v44, v45
v_cvt_pk_bf16_f32 v119, v46, v47
v_mfma_f32_32x32x8_bf16 v[32:47], v[132:133], v[156:157], v[64:79]
v_exp_f32 v16, v16
v_exp_f32 v17, v17
v_exp_f32 v18, v18
v_exp_f32 v19, v19
v_mfma_f32_32x32x16_bf16 v[96:111], v[128:131], v[116:119], v[96:111]
ds_read_b128 v[128:131], v9 offset:26848
buffer_load_dwordx4 a[108:111], v4, s[8:11], s63 offen offset:3072 sc1
s_mov_b32 s63, 28672
buffer_load_dwordx4 a[112:115], v4, s[8:11], s63 offen sc1
buffer_load_dwordx4 a[116:119], v4, s[8:11], s63 offen offset:1024 sc1
v_exp_f32 v20, v20
v_exp_f32 v21, v21
v_cvt_pk_bf16_f32 v112, v16, v17
v_exp_f32 v22, v22
v_cvt_pk_bf16_f32 v113, v18, v19
v_exp_f32 v23, v23
v_mfma_f32_32x32x16_bf16 v[32:47], v[120:123], v[140:143], v[32:47]
s_waitcnt lgkmcnt(1)
ds_read_b128 v[120:123], v6 offset:26112
v_exp_f32 v24, v24
v_exp_f32 v25, v25
v_cvt_pk_bf16_f32 v114, v20, v21
v_exp_f32 v26, v26
v_cvt_pk_bf16_f32 v115, v22, v23
v_exp_f32 v27, v27
v_exp_f32 v28, v28
v_mfma_f32_32x32x16_bf16 v[80:95], v[124:127], v[112:115], v[80:95]
s_waitcnt lgkmcnt(1)
v_exp_f32 v29, v29
v_exp_f32 v30, v30
v_cvt_pk_bf16_f32 v116, v24, v25
v_exp_f32 v31, v31
v_cvt_pk_bf16_f32 v117, v26, v27
v_cvt_pk_bf16_f32 v118, v28, v29
v_cvt_pk_bf16_f32 v119, v30, v31
v_mfma_f32_32x32x8_bf16 v[16:31], v[132:133], v[150:151], v[48:63]
v_exp_f32 v32, v32
v_exp_f32 v33, v33
v_exp_f32 v34, v34
v_exp_f32 v35, v35
v_mfma_f32_32x32x16_bf16 v[80:95], v[128:131], v[116:119], v[80:95]
s_waitcnt lgkmcnt(0)
buffer_load_dwordx4 a[120:123], v4, s[8:11], s63 offen offset:2048 sc1
buffer_load_dwordx4 a[124:127], v4, s[8:11], s63 offen offset:3072 sc1
s_mov_b32 s63, 32768
buffer_load_dwordx4 a[128:131], v4, s[8:11], s63 offen sc1
v_exp_f32 v36, v36
v_exp_f32 v37, v37
v_cvt_pk_bf16_f32 v112, v32, v33
v_exp_f32 v38, v38
v_cvt_pk_bf16_f32 v113, v34, v35
v_exp_f32 v39, v39
v_mfma_f32_32x32x16_bf16 v[16:31], v[120:123], v[136:139], v[16:31]
v_exp_f32 v40, v40
v_exp_f32 v41, v41
v_cvt_pk_bf16_f32 v114, v36, v37
v_exp_f32 v42, v42
v_cvt_pk_bf16_f32 v115, v38, v39
v_exp_f32 v43, v43
v_exp_f32 v44, v44
v_mfma_f32_32x32x16_bf16 v[96:111], v[124:127], v[112:115], v[96:111]
ds_read_b128 v[124:127], v9 offset:27456
v_exp_f32 v45, v45
v_exp_f32 v46, v46
v_cvt_pk_bf16_f32 v116, v40, v41
v_exp_f32 v47, v47
v_cvt_pk_bf16_f32 v117, v42, v43
v_cvt_pk_bf16_f32 v118, v44, v45
v_cvt_pk_bf16_f32 v119, v46, v47
v_mfma_f32_32x32x8_bf16 v[32:47], v[132:133], v[158:159], v[64:79]
v_exp_f32 v16, v16
v_exp_f32 v17, v17
v_exp_f32 v18, v18
v_exp_f32 v19, v19
v_mfma_f32_32x32x16_bf16 v[96:111], v[128:131], v[116:119], v[96:111]
ds_read_b128 v[128:131], v9 offset:28064
v_exp_f32 v20, v20
v_exp_f32 v21, v21
v_cvt_pk_bf16_f32 v112, v16, v17
v_exp_f32 v22, v22
v_cvt_pk_bf16_f32 v113, v18, v19
v_exp_f32 v23, v23
v_mfma_f32_32x32x16_bf16 v[32:47], v[120:123], v[140:143], v[32:47]
s_waitcnt lgkmcnt(1)
v_exp_f32 v24, v24
v_exp_f32 v25, v25
v_cvt_pk_bf16_f32 v114, v20, v21
v_exp_f32 v26, v26
v_cvt_pk_bf16_f32 v115, v22, v23
v_exp_f32 v27, v27
v_exp_f32 v28, v28
v_mfma_f32_32x32x16_bf16 v[80:95], v[124:127], v[112:115], v[80:95]
s_waitcnt lgkmcnt(0)
v_exp_f32 v29, v29
v_exp_f32 v30, v30
v_cvt_pk_bf16_f32 v116, v24, v25
v_exp_f32 v31, v31
v_cvt_pk_bf16_f32 v117, v26, v27
v_cvt_pk_bf16_f32 v118, v28, v29
v_cvt_pk_bf16_f32 v119, v30, v31
v_exp_f32 v32, v32
v_exp_f32 v33, v33
v_exp_f32 v34, v34
v_exp_f32 v35, v35
v_mfma_f32_32x32x16_bf16 v[80:95], v[128:131], v[116:119], v[80:95]
v_exp_f32 v36, v36
v_exp_f32 v37, v37
v_cvt_pk_bf16_f32 v112, v32, v33
)"
R"(v_exp_f32 v38, v38
v_cvt_pk_bf16_f32 v113, v34, v35
v_exp_f32 v39, v39
v_exp_f32 v40, v40
v_exp_f32 v41, v41
v_cvt_pk_bf16_f32 v114, v36, v37
v_exp_f32 v42, v42
v_cvt_pk_bf16_f32 v115, v38, v39
v_exp_f32 v43, v43
v_exp_f32 v44, v44
v_mfma_f32_32x32x16_bf16 v[96:111], v[124:127], v[112:115], v[96:111]
v_exp_f32 v45, v45
v_exp_f32 v46, v46
v_cvt_pk_bf16_f32 v116, v40, v41
v_exp_f32 v47, v47
v_cvt_pk_bf16_f32 v117, v42, v43
v_cvt_pk_bf16_f32 v118, v44, v45
v_cvt_pk_bf16_f32 v119, v46, v47
s_nop 1
v_mfma_f32_32x32x16_bf16 v[96:111], v[128:131], v[116:119], v[96:111]
; guard: the reference point is safe iff the row sum and the accumulators stayed in range; else redo with the true row max
v_mov_b32 v163, 0x5d800000
v_mov_b32 v162, 0x7e800000
v_add_u32 v221, 110592, v221
v_add_u32 v222, 110592, v222
s_add_u32 s46, s50, s82
s_addc_u32 s47, s51, 0
s_add_u32 s48, s46, 2048
s_addc_u32 s49, s47, 0
s_add_u32 s4, s46, 8192
s_addc_u32 s5, s47, 0
v_add_f32 v164, |v80|, |v81|
v_add_f32 v164, v164, |v82|
v_add_f32 v164, v164, |v83|
v_add_f32 v164, v164, |v84|
v_add_f32 v164, v164, |v85|
v_add_f32 v164, v164, |v86|
v_add_f32 v164, v164, |v87|
v_add_f32 v164, v164, |v88|
v_add_f32 v164, v164, |v89|
v_cmp_nlt_f32 vcc, v90, v163
v_cmp_nlt_f32 s[96:97], v164, v162
s_nop 0
s_or_b64 vcc, vcc, s[96:97]
s_cbranch_scc1 Lx_22_%=
Lr_23_%=:
v_rcp_f32 v164, v90
s_nop 0
v_pk_mul_f32 v[80:81], v[80:81], v[164:165] op_sel_hi:[1,0]
v_pk_mul_f32 v[82:83], v[82:83], v[164:165] op_sel_hi:[1,0]
v_pk_mul_f32 v[84:85], v[84:85], v[164:165] op_sel_hi:[1,0]
v_pk_mul_f32 v[86:87], v[86:87], v[164:165] op_sel_hi:[1,0]
v_pk_mul_f32 v[88:89], v[88:89], v[164:165] op_sel_hi:[1,0]
v_cvt_pk_bf16_f32 v184, v80, v81
v_min_u32 v184, -2, v184
v_cvt_pk_bf16_f32 v185, v82, v83
v_min_u32 v185, -2, v185
v_cvt_pk_bf16_f32 v186, v84, v85
v_min_u32 v186, -2, v186
v_cvt_pk_bf16_f32 v187, v86, v87
v_min_u32 v187, -2, v187
v_cvt_pk_bf16_f32 v188, v88, v89
v_min_u32 v188, -2, v188
ds_write2_b32 v221, v184, v185 offset0:0 offset1:1
ds_write2_b32 v221, v186, v187 offset0:2 offset1:3
v_add_f32 v164, |v96|, |v97|
v_add_f32 v164, v164, |v98|
v_add_f32 v164, v164, |v99|
v_add_f32 v164, v164, |v100|
v_add_f32 v164, v164, |v101|
v_add_f32 v164, v164, |v102|
v_add_f32 v164, v164, |v103|
v_add_f32 v164, v164, |v104|
v_add_f32 v164, v164, |v105|
v_cmp_nlt_f32 vcc, v106, v163
v_cmp_nlt_f32 s[96:97], v164, v162
s_nop 0
s_or_b64 vcc, vcc, s[96:97]
s_cbranch_scc1 Lx_26_%=
Lr_27_%=:
v_rcp_f32 v164, v106
s_nop 0
v_pk_mul_f32 v[96:97], v[96:97], v[164:165] op_sel_hi:[1,0]
v_pk_mul_f32 v[98:99], v[98:99], v[164:165] op_sel_hi:[1,0]
v_pk_mul_f32 v[100:101], v[100:101], v[164:165] op_sel_hi:[1,0]
v_pk_mul_f32 v[102:103], v[102:103], v[164:165] op_sel_hi:[1,0]
v_pk_mul_f32 v[104:105], v[104:105], v[164:165] op_sel_hi:[1,0]
v_cvt_pk_bf16_f32 v190, v96, v97
v_min_u32 v190, -2, v190
v_cvt_pk_bf16_f32 v191, v98, v99
v_min_u32 v191, -2, v191
v_cvt_pk_bf16_f32 v192, v100, v101
v_min_u32 v192, -2, v192
v_cvt_pk_bf16_f32 v193, v102, v103
v_min_u32 v193, -2, v193
v_cvt_pk_bf16_f32 v194, v104, v105
v_min_u32 v194, -2, v194
ds_write2_b32 v221, v190, v191 offset0:18 offset1:19
ds_write2_b32 v221, v192, v193 offset0:20 offset1:21
s_mov_b32 exec_lo, -1
s_mov_b32 exec_hi, 0
ds_write2_b32 v222, v188, v194 offset0:8 offset1:26
s_mov_b64 exec, -1
ds_read_b128 v[164:167], v223
ds_read_b128 v[168:171], v223 offset:64
ds_read_b128 v[172:175], v223 offset:128
s_waitcnt lgkmcnt(2)
global_store_dwordx4 v14, v[164:167], s[48:49] offset:-2048 sc1
s_waitcnt lgkmcnt(1)
global_store_dwordx4 v14, v[168:171], s[48:49] offset:2048 sc1
s_mov_b32 exec_lo, 0xffff
s_mov_b32 exec_hi, 0
s_waitcnt lgkmcnt(0)
global_store_dwordx4 v14, v[172:175], s[4:5] sc1
s_mov_b64 exec, -1
s_waitcnt vmcnt(36)
s_barrier
; ===== output projection of 64 tokens x 72 columns (o gathered from the group, sentinel-checked), h = x + attn
v_subrev_u32 v217, 52096, v217
v_subrev_u32 v218, 52096, v218
v_subrev_u32 v219, 52096, v219
v_subrev_u32 v220, 52096, v220
v_lshlrev_b32 v184, 16, v204
v_and_b32 v185, 0xffff0000, v204
v_lshlrev_b32 v186, 16, v205
v_and_b32 v187, 0xffff0000, v205
v_lshlrev_b32 v188, 16, v206
v_and_b32 v189, 0xffff0000, v206
v_lshlrev_b32 v190, 16, v207
v_and_b32 v191, 0xffff0000, v207
v_lshlrev_b32 v192, 16, v208
v_and_b32 v193, 0xffff0000, v208
v_lshlrev_b32 v194, 16, v209
v_and_b32 v195, 0xffff0000, v209
v_lshlrev_b32 v196, 16, v210
v_and_b32 v197, 0xffff0000, v210
v_lshlrev_b32 v198, 16, v211
v_and_b32 v199, 0xffff0000, v211
v_lshlrev_b32 v200, 16, v212
v_and_b32 v201, 0xffff0000, v212
v_lshlrev_b32 v202, 16, v213
v_and_b32 v203, 0xffff0000, v213
ds_read_b64_tr_b16 v[16:17], v217
ds_read_b64_tr_b16 v[18:19], v217 offset:768
ds_read_b64_tr_b16 v[20:21], v217 offset:32
ds_read_b64_tr_b16 v[22:23], v217 offset:800
ds_read_b64_tr_b16 v[24:25], v217 offset:64
ds_read_b64_tr_b16 v[26:27], v217 offset:832
ds_read_b64_tr_b16 v[28:29], v217 offset:96
ds_read_b64_tr_b16 v[30:31], v217 offset:864
ds_read_b64_tr_b16 v[8:9], v219 offset:128
ds_read_b64_tr_b16 v[10:11], v219 offset:896
ds_read_b64_tr_b16 v[92:93], v217 offset:6208
ds_read_b64_tr_b16 v[94:95], v217 offset:6976
ds_read_b64_tr_b16 v[96:97], v217 offset:6240
ds_read_b64_tr_b16 v[98:99], v217 offset:7008
ds_read_b64_tr_b16 v[100:101], v217 offset:6272
ds_read_b64_tr_b16 v[102:103], v217 offset:7040
ds_read_b64_tr_b16 v[104:105], v217 offset:6304
ds_read_b64_tr_b16 v[106:107], v217 offset:7072
ds_read_b64_tr_b16 v[108:109], v219 offset:6336
ds_read_b64_tr_b16 v[110:111], v219 offset:7104
s_mov_b32 s52, s46
s_and_b32 s53, s47, 0xffff
s_mov_b32 s54, 73728
s_mov_b32 s55, 131072
buffer_load_dwordx4 a[132:135], v4, s[8:11], s63 offen offset:1024 sc1
buffer_load_dwordx4 a[136:139], v4, s[8:11], s63 offen offset:2048 sc1
buffer_load_dwordx4 a[140:143], v4, s[8:11], s63 offen offset:3072 sc1
s_mov_b32 s63, 36864
buffer_load_dwordx4 a[144:147], v4, s[8:11], s63 offen sc1
buffer_load_dwordx4 a[148:151], v4, s[8:11], s63 offen offset:1024 sc1
buffer_load_dwordx4 a[152:155], v4, s[8:11], s63 offen offset:2048 sc1
buffer_load_dwordx4 a[156:159], v4, s[8:11], s63 offen offset:3072 sc1
s_mov_b32 s63, 40960
buffer_load_dwordx4 a[160:163], v4, s[8:11], s63 offen sc1
buffer_load_dwordx4 a[164:167], v4, s[8:11], s63 offen offset:1024 sc1
; instruction-cache warm-up: with EXEC = 0 hop over the branch stubs placed at the head of the coming 4-KiB code pages
s_mov_b64 exec, 0
s_cmp_eq_u32 s39, 0
s_cbranch_scc1 LW1_0_%=
s_cmp_eq_u32 s39, 1
s_cbranch_scc1 LW1_1_%=
s_cmp_eq_u32 s39, 2
s_cbranch_scc1 LW1_2_%=
s_branch LW1_3_%=
LWEND1_%=:
s_mov_b64 exec, -1
s_mov_b32 s96, 0
buffer_load_dwordx4 v[112:115], v4, s[52:55], s96 offen sc1
ds_read_b64_tr_b16 v[72:73], v217 offset:12416
ds_read_b64_tr_b16 v[74:75], v217 offset:13184
s_mov_b32 s96, 4096
buffer_load_dwordx4 v[116:119], v4, s[52:55], s96 offen sc1
ds_read_b64_tr_b16 v[76:77], v217 offset:12448
ds_read_b64_tr_b16 v[78:79], v217 offset:13216
s_mov_b32 s96, 8192
buffer_load_dwordx4 v[120:123], v4, s[52:55], s96 offen sc1
ds_read_b64_tr_b16 v[80:81], v217 offset:12480
ds_read_b64_tr_b16 v[82:83], v217 offset:13248
s_mov_b32 s96, 12288
buffer_load_dwordx4 v[124:127], v4, s[52:55], s96 offen sc1
ds_read_b64_tr_b16 v[84:85], v217 offset:12512
ds_read_b64_tr_b16 v[86:87], v217 offset:13280
s_mov_b32 s96, 16384
buffer_load_dwordx4 v[128:131], v4, s[52:55], s96 offen sc1
ds_read_b64_tr_b16 v[88:89], v219 offset:12544
ds_read_b64_tr_b16 v[90:91], v219 offset:13312
s_mov_b32 s96, 20480
buffer_load_dwordx4 v[132:135], v4, s[52:55], s96 offen sc1
ds_read_b64_tr_b16 v[52:53], v217 offset:18624
ds_read_b64_tr_b16 v[54:55], v217 offset:19392
s_mov_b32 s96, 24576
buffer_load_dwordx4 v[136:139], v4, s[52:55], s96 offen sc1
ds_read_b64_tr_b16 v[56:57], v217 offset:18656
ds_read_b64_tr_b16 v[58:59], v217 offset:19424
s_mov_b32 s96, 28672
buffer_load_dwordx4 v[140:143], v4, s[52:55], s96 offen sc1
ds_read_b64_tr_b16 v[60:61], v217 offset:18688
ds_read_b64_tr_b16 v[62:63], v217 offset:19456
s_mov_b32 s96, 32768
buffer_load_dwordx4 v[144:147], v4, s[52:55], s96 offen sc1
ds_read_b64_tr_b16 v[64:65], v217 offset:18720
ds_read_b64_tr_b16 v[66:67], v217 offset:19488
s_mov_b32 s96, 36864
buffer_load_dwordx4 v[148:151], v4, s[52:55], s96 offen sc1
ds_read_b64_tr_b16 v[68:69], v219 offset:18752
ds_read_b64_tr_b16 v[70:71], v219 offset:19520
s_mov_b32 s96, 40960
buffer_load_dwordx4 v[152:155], v4, s[52:55], s96 offen sc1
ds_read_b64_tr_b16 v[32:33], v217 offset:24832
ds_read_b64_tr_b16 v[34:35], v217 offset:25600
s_mov_b32 s96, 45056
buffer_load_dwordx4 v[156:159], v4, s[52:55], s96 offen sc1
ds_read_b64_tr_b16 v[36:37], v217 offset:24864
ds_read_b64_tr_b16 v[38:39], v217 offset:25632
s_mov_b32 s96, 49152
buffer_load_dwordx4 v[160:163], v4, s[52:55], s96 offen sc1
ds_read_b64_tr_b16 v[40:41], v217 offset:24896
ds_read_b64_tr_b16 v[42:43], v217 offset:25664
s_mov_b32 s96, 53248
buffer_load_dwordx4 v[164:167], v4, s[52:55], s96 offen sc1
ds_read_b64_tr_b16 v[44:45], v217 offset:24928
ds_read_b64_tr_b16 v[46:47], v217 offset:25696
s_mov_b32 s96, 57344
buffer_load_dwordx4 v[168:171], v4, s[52:55], s96 offen sc1
ds_read_b64_tr_b16 v[48:49], v219 offset:24960
ds_read_b64_tr_b16 v[50:51], v219 offset:25728
s_mov_b32 s96, 61440
buffer_load_dwordx4 v[172:175], v4, s[52:55], s96 offen sc1
s_mov_b32 s96, 65536
buffer_load_dwordx4 v[176:179], v4, s[52:55], s96 offen sc1
s_mov_b32 s96, 69632
buffer_load_dwordx4 v[180:183], v4, s[52:55], s96 offen sc1
s_waitcnt vmcnt(17)
v_max_u32 v12, v112, v113
v_max3_u32 v12, v12, v114, v115
s_waitcnt vmcnt(16)
v_max3_u32 v12, v12, v116, v117
v_max3_u32 v12, v12, v118, v119
s_waitcnt vmcnt(15)
v_max3_u32 v12, v12, v120, v121
v_max3_u32 v12, v12, v122, v123
s_waitcnt vmcnt(14)
v_max3_u32 v12, v12, v124, v125
v_max3_u32 v12, v12, v126, v127
s_waitcnt vmcnt(13)
v_max3_u32 v12, v12, v128, v129
v_max3_u32 v12, v12, v130, v131
s_waitcnt vmcnt(12)
v_max3_u32 v12, v12, v132, v133
v_max3_u32 v12, v12, v134, v135
v_cmp_eq_u32 vcc, -1, v12
s_nop 0
s_cmp_eq_u64 vcc, 0
s_cbranch_scc1 Lcd_30_%=
s_mov_b32 s97, 32768
Lcp_31_%=:
s_mov_b32 s96, 0
buffer_load_dwordx4 v[112:115], v4, s[52:55], s96 offen sc1
s_mov_b32 s96, 4096
buffer_load_dwordx4 v[116:119], v4, s[52:55], s96 offen sc1
s_mov_b32 s96, 8192
buffer_load_dwordx4 v[120:123], v4, s[52:55], s96 offen sc1
s_mov_b32 s96, 12288
buffer_load_dwordx4 v[124:127], v4, s[52:55], s96 offen sc1
s_mov_b32 s96, 16384
buffer_load_dwordx4 v[128:131], v4, s[52:55], s96 offen sc1
s_mov_b32 s96, 20480
buffer_load_dwordx4 v[132:135], v4, s[52:55], s96 offen sc1
s_waitcnt vmcnt(5)
v_max_u32 v12, v112, v113
v_max3_u32 v12, v12, v114, v115
s_waitcnt vmcnt(4)
v_max3_u32 v12, v12, v116, v117
v_max3_u32 v12, v12, v118, v119
s_waitcnt vmcnt(3)
v_max3_u32 v12, v12, v120, v121
v_max3_u32 v12, v12, v122, v123
s_waitcnt vmcnt(2)
v_max3_u32 v12, v12, v124, v125
v_max3_u32 v12, v12, v126, v127
s_waitcnt vmcnt(1)
v_max3_u32 v12, v12, v128, v129
v_max3_u32 v12, v12, v130, v131
s_waitcnt vmcnt(0)
v_max3_u32 v12, v12, v132, v133
v_max3_u32 v12, v12, v134, v135
v_cmp_eq_u32 vcc, -1, v12
s_nop 0
s_cmp_eq_u64 vcc, 0
s_cbranch_scc1 Lcd_30_%=
s_sub_u32 s97, s97, 1
s_cmp_lg_u32 s97, 0
s_cbranch_scc1 Lcp_31_%=
Lcd_30_%=:
s_waitcnt lgkmcnt(15)
v_mfma_f32_16x16x32_bf16 v[184:187], v[16:19], v[112:115], v[184:187]
v_mfma_f32_16x16x32_bf16 v[188:191], v[20:23], v[112:115], v[188:191]
v_mfma_f32_16x16x32_bf16 v[192:195], v[24:27], v[112:115], v[192:195]
v_mfma_f32_16x16x32_bf16 v[196:199], v[28:31], v[112:115], v[196:199]
v_mfma_f32_16x16x32_bf16 v[200:203], v[8:11], v[112:115], v[200:203]
v_mfma_f32_16x16x32_bf16 v[184:187], v[92:95], v[116:119], v[184:187]
ds_read_b64_tr_b16 v[16:17], v217 offset:31040
ds_read_b64_tr_b16 v[18:19], v217 offset:31808
v_mfma_f32_16x16x32_bf16 v[188:191], v[96:99], v[116:119], v[188:191]
ds_read_b64_tr_b16 v[20:21], v217 offset:31072
v_mfma_f32_16x16x32_bf16 v[192:195], v[100:103], v[116:119], v[192:195]
ds_read_b64_tr_b16 v[22:23], v217 offset:31840
ds_read_b64_tr_b16 v[24:25], v217 offset:31104
v_mfma_f32_16x16x32_bf16 v[196:199], v[104:107], v[116:119], v[196:199]
ds_read_b64_tr_b16 v[26:27], v217 offset:31872
v_mfma_f32_16x16x32_bf16 v[200:203], v[108:111], v[116:119], v[200:203]
ds_read_b64_tr_b16 v[28:29], v217 offset:31136
ds_read_b64_tr_b16 v[30:31], v217 offset:31904
v_mfma_f32_16x16x32_bf16 v[184:187], v[72:75], v[120:123], v[184:187]
ds_read_b64_tr_b16 v[8:9], v219 offset:31168
ds_read_b64_tr_b16 v[10:11], v219 offset:31936
v_mfma_f32_16x16x32_bf16 v[188:191], v[76:79], v[120:123], v[188:191]
ds_read_b64_tr_b16 v[92:93], v217 offset:37248
v_mfma_f32_16x16x32_bf16 v[192:195], v[80:83], v[120:123], v[192:195]
ds_read_b64_tr_b16 v[94:95], v217 offset:38016
ds_read_b64_tr_b16 v[96:97], v217 offset:37280
v_mfma_f32_16x16x32_bf16 v[196:199], v[84:87], v[120:123], v[196:199]
ds_read_b64_tr_b16 v[98:99], v217 offset:38048
v_mfma_f32_16x16x32_bf16 v[200:203], v[88:91], v[120:123], v[200:203]
s_waitcnt lgkmcnt(15)
ds_read_b64_tr_b16 v[100:101], v217 offset:37312
ds_read_b64_tr_b16 v[102:103], v217 offset:38080
buffer_load_dwordx4 a[168:171], v4, s[8:11], s63 offen offset:2048 sc1
v_mfma_f32_16x16x32_bf16 v[184:187], v[52:55], v[124:127], v[184:187]
ds_read_b64_tr_b16 v[104:105], v217 offset:37344
ds_read_b64_tr_b16 v[106:107], v217 offset:38112
v_mfma_f32_16x16x32_bf16 v[188:191], v[56:59], v[124:127], v[188:191]
ds_read_b64_tr_b16 v[108:109], v219 offset:37376
v_mfma_f32_16x16x32_bf16 v[192:195], v[60:63], v[124:127], v[192:195]
ds_read_b64_tr_b16 v[110:111], v219 offset:38144
ds_read_b64_tr_b16 v[72:73], v217 offset:43456
v_mfma_f32_16x16x32_bf16 v[196:199], v[64:67], v[124:127], v[196:199]
ds_read_b64_tr_b16 v[74:75], v217 offset:44224
v_mfma_f32_16x16x32_bf16 v[200:203], v[68:71], v[124:127], v[200:203]
s_waitcnt lgkmcnt(15)
ds_read_b64_tr_b16 v[76:77], v217 offset:43488
ds_read_b64_tr_b16 v[78:79], v217 offset:44256
buffer_load_dwordx4 a[172:175], v4, s[8:11], s63 offen offset:3072 sc1
v_mfma_f32_16x16x32_bf16 v[184:187], v[32:35], v[128:131], v[184:187]
ds_read_b64_tr_b16 v[80:81], v217 offset:43520
ds_read_b64_tr_b16 v[82:83], v217 offset:44288
v_mfma_f32_16x16x32_bf16 v[188:191], v[36:39], v[128:131], v[188:191]
ds_read_b64_tr_b16 v[84:85], v217 offset:43552
s_waitcnt vmcnt(13)
v_max_u32 v12, v136, v137
v_max3_u32 v12, v12, v138, v139
s_waitcnt vmcnt(12)
v_max3_u32 v12, v12, v140, v141
v_mfma_f32_16x16x32_bf16 v[192:195], v[40:43], v[128:131], v[192:195]
ds_read_b64_tr_b16 v[86:87], v217 offset:44320
ds_read_b64_tr_b16 v[88:89], v219 offset:43584
v_mfma_f32_16x16x32_bf16 v[196:199], v[44:47], v[128:131], v[196:199]
ds_read_b64_tr_b16 v[90:91], v219 offset:44352
v_max3_u32 v12, v12, v142, v143
s_waitcnt vmcnt(11)
v_max3_u32 v12, v12, v144, v145
v_max3_u32 v12, v12, v146, v147
v_mfma_f32_16x16x32_bf16 v[200:203], v[48:51], v[128:131], v[200:203]
s_waitcnt lgkmcnt(15)
ds_read_b64_tr_b16 v[52:53], v217 offset:49664
ds_read_b64_tr_b16 v[54:55], v217 offset:50432
s_mov_b32 s63, 45056
buffer_load_dwordx4 a[176:179], v4, s[8:11], s63 offen sc1
v_mfma_f32_16x16x32_bf16 v[184:187], v[16:19], v[132:135], v[184:187]
ds_read_b64_tr_b16 v[56:57], v217 offset:49696
ds_read_b64_tr_b16 v[58:59], v217 offset:50464
v_mfma_f32_16x16x32_bf16 v[188:191], v[20:23], v[132:135], v[188:191]
ds_read_b64_tr_b16 v[60:61], v217 offset:49728
s_waitcnt vmcnt(11)
v_max3_u32 v12, v12, v148, v149
v_max3_u32 v12, v12, v150, v151
s_waitcnt vmcnt(10)
v_max3_u32 v12, v12, v152, v153
)"
R"(v_mfma_f32_16x16x32_bf16 v[192:195], v[24:27], v[132:135], v[192:195]
ds_read_b64_tr_b16 v[62:63], v217 offset:50496
ds_read_b64_tr_b16 v[64:65], v217 offset:49760
v_mfma_f32_16x16x32_bf16 v[196:199], v[28:31], v[132:135], v[196:199]
ds_read_b64_tr_b16 v[66:67], v217 offset:50528
v_max3_u32 v12, v12, v154, v155
s_waitcnt vmcnt(9)
v_max3_u32 v12, v12, v156, v157
v_max3_u32 v12, v12, v158, v159
v_mfma_f32_16x16x32_bf16 v[200:203], v[8:11], v[132:135], v[200:203]
s_waitcnt lgkmcnt(15)
ds_read_b64_tr_b16 v[68:69], v219 offset:49792
ds_read_b64_tr_b16 v[70:71], v219 offset:50560
buffer_load_dwordx4 a[180:183], v4, s[8:11], s63 offen offset:1024 sc1
v_cmp_eq_u32 vcc, -1, v12
s_nop 0
s_cmp_eq_u64 vcc, 0
s_cbranch_scc0 Lx_34_%=
Lr_35_%=:
v_mfma_f32_16x16x32_bf16 v[184:187], v[92:95], v[136:139], v[184:187]
ds_read_b64_tr_b16 v[32:33], v218
ds_read_b64_tr_b16 v[34:35], v218 offset:768
v_mfma_f32_16x16x32_bf16 v[188:191], v[96:99], v[136:139], v[188:191]
ds_read_b64_tr_b16 v[36:37], v218 offset:32
v_mfma_f32_16x16x32_bf16 v[192:195], v[100:103], v[136:139], v[192:195]
ds_read_b64_tr_b16 v[38:39], v218 offset:800
ds_read_b64_tr_b16 v[40:41], v218 offset:64
v_mfma_f32_16x16x32_bf16 v[196:199], v[104:107], v[136:139], v[196:199]
ds_read_b64_tr_b16 v[42:43], v218 offset:832
v_mfma_f32_16x16x32_bf16 v[200:203], v[108:111], v[136:139], v[200:203]
s_waitcnt lgkmcnt(15)
ds_read_b64_tr_b16 v[44:45], v218 offset:96
ds_read_b64_tr_b16 v[46:47], v218 offset:864
buffer_load_dwordx4 a[184:187], v4, s[8:11], s63 offen offset:2048 sc1
v_mfma_f32_16x16x32_bf16 v[184:187], v[72:75], v[140:143], v[184:187]
ds_read_b64_tr_b16 v[48:49], v220 offset:128
ds_read_b64_tr_b16 v[50:51], v220 offset:896
v_mfma_f32_16x16x32_bf16 v[188:191], v[76:79], v[140:143], v[188:191]
ds_read_b64_tr_b16 v[16:17], v218 offset:6208
v_mfma_f32_16x16x32_bf16 v[192:195], v[80:83], v[140:143], v[192:195]
ds_read_b64_tr_b16 v[18:19], v218 offset:6976
ds_read_b64_tr_b16 v[20:21], v218 offset:6240
v_mfma_f32_16x16x32_bf16 v[196:199], v[84:87], v[140:143], v[196:199]
ds_read_b64_tr_b16 v[22:23], v218 offset:7008
v_mfma_f32_16x16x32_bf16 v[200:203], v[88:91], v[140:143], v[200:203]
s_waitcnt lgkmcnt(14)
ds_read_b64_tr_b16 v[24:25], v218 offset:6272
ds_read_b64_tr_b16 v[26:27], v218 offset:7040
buffer_load_dwordx4 a[188:191], v4, s[8:11], s63 offen offset:3072 sc1
v_mfma_f32_16x16x32_bf16 v[184:187], v[52:55], v[144:147], v[184:187]
ds_read_b64_tr_b16 v[28:29], v218 offset:6304
ds_read_b64_tr_b16 v[30:31], v218 offset:7072
v_mfma_f32_16x16x32_bf16 v[188:191], v[56:59], v[144:147], v[188:191]
ds_read_b64_tr_b16 v[8:9], v220 offset:6336
v_mfma_f32_16x16x32_bf16 v[192:195], v[60:63], v[144:147], v[192:195]
ds_read_b64_tr_b16 v[10:11], v220 offset:7104
ds_read_b64_tr_b16 v[92:93], v218 offset:12416
v_mfma_f32_16x16x32_bf16 v[196:199], v[64:67], v[144:147], v[196:199]
ds_read_b64_tr_b16 v[94:95], v218 offset:13184
v_mfma_f32_16x16x32_bf16 v[200:203], v[68:71], v[144:147], v[200:203]
s_waitcnt lgkmcnt(12)
ds_read_b64_tr_b16 v[96:97], v218 offset:12448
ds_read_b64_tr_b16 v[98:99], v218 offset:13216
s_mov_b32 s63, 49152
buffer_load_dwordx4 a[192:195], v4, s[8:11], s63 offen sc1
v_mfma_f32_16x16x32_bf16 v[184:187], v[32:35], v[148:151], v[184:187]
ds_read_b64_tr_b16 v[100:101], v218 offset:12480
ds_read_b64_tr_b16 v[102:103], v218 offset:13248
v_mfma_f32_16x16x32_bf16 v[188:191], v[36:39], v[148:151], v[188:191]
ds_read_b64_tr_b16 v[104:105], v218 offset:12512
v_mfma_f32_16x16x32_bf16 v[192:195], v[40:43], v[148:151], v[192:195]
ds_read_b64_tr_b16 v[106:107], v218 offset:13280
ds_read_b64_tr_b16 v[108:109], v220 offset:12544
v_mfma_f32_16x16x32_bf16 v[196:199], v[44:47], v[148:151], v[196:199]
ds_read_b64_tr_b16 v[110:111], v220 offset:13312
v_mfma_f32_16x16x32_bf16 v[200:203], v[48:51], v[148:151], v[200:203]
s_waitcnt lgkmcnt(10)
ds_read_b64_tr_b16 v[72:73], v218 offset:18624
ds_read_b64_tr_b16 v[74:75], v218 offset:19392
buffer_load_dwordx4 a[196:199], v4, s[8:11], s63 offen offset:1024 sc1
v_mfma_f32_16x16x32_bf16 v[184:187], v[16:19], v[152:155], v[184:187]
ds_read_b64_tr_b16 v[76:77], v218 offset:18656
ds_read_b64_tr_b16 v[78:79], v218 offset:19424
v_mfma_f32_16x16x32_bf16 v[188:191], v[20:23], v[152:155], v[188:191]
ds_read_b64_tr_b16 v[80:81], v218 offset:18688
s_waitcnt vmcnt(13)
v_max_u32 v12, v160, v161
v_max3_u32 v12, v12, v162, v163
s_waitcnt vmcnt(12)
v_max3_u32 v12, v12, v164, v165
v_mfma_f32_16x16x32_bf16 v[192:195], v[24:27], v[152:155], v[192:195]
LW1_0_%=:
s_cbranch_execz LW1_4_%=
ds_read_b64_tr_b16 v[82:83], v218 offset:19456
ds_read_b64_tr_b16 v[84:85], v218 offset:18720
v_mfma_f32_16x16x32_bf16 v[196:199], v[28:31], v[152:155], v[196:199]
ds_read_b64_tr_b16 v[86:87], v218 offset:19488
v_max3_u32 v12, v12, v166, v167
s_waitcnt vmcnt(11)
v_max3_u32 v12, v12, v168, v169
v_max3_u32 v12, v12, v170, v171
v_mfma_f32_16x16x32_bf16 v[200:203], v[8:11], v[152:155], v[200:203]
s_waitcnt lgkmcnt(8)
ds_read_b64_tr_b16 v[88:89], v220 offset:18752
ds_read_b64_tr_b16 v[90:91], v220 offset:19520
buffer_load_dwordx4 a[200:203], v4, s[8:11], s63 offen offset:2048 sc1
v_mfma_f32_16x16x32_bf16 v[184:187], v[92:95], v[156:159], v[184:187]
ds_read_b64_tr_b16 v[52:53], v218 offset:24832
ds_read_b64_tr_b16 v[54:55], v218 offset:25600
v_mfma_f32_16x16x32_bf16 v[188:191], v[96:99], v[156:159], v[188:191]
ds_read_b64_tr_b16 v[56:57], v218 offset:24864
s_waitcnt vmcnt(11)
v_max3_u32 v12, v12, v172, v173
v_max3_u32 v12, v12, v174, v175
s_waitcnt vmcnt(10)
v_max3_u32 v12, v12, v176, v177
v_mfma_f32_16x16x32_bf16 v[192:195], v[100:103], v[156:159], v[192:195]
ds_read_b64_tr_b16 v[58:59], v218 offset:25632
ds_read_b64_tr_b16 v[60:61], v218 offset:24896
v_mfma_f32_16x16x32_bf16 v[196:199], v[104:107], v[156:159], v[196:199]
ds_read_b64_tr_b16 v[62:63], v218 offset:25664
v_max3_u32 v12, v12, v178, v179
s_waitcnt vmcnt(9)
v_max3_u32 v12, v12, v180, v181
v_max3_u32 v12, v12, v182, v183
v_mfma_f32_16x16x32_bf16 v[200:203], v[108:111], v[156:159], v[200:203]
s_waitcnt lgkmcnt(6)
ds_read_b64_tr_b16 v[64:65], v218 offset:24928
ds_read_b64_tr_b16 v[66:67], v218 offset:25696
buffer_load_dwordx4 a[204:207], v4, s[8:11], s63 offen offset:3072 sc1
v_cmp_eq_u32 vcc, -1, v12
s_nop 0
s_cmp_eq_u64 vcc, 0
s_cbranch_scc0 Lx_38_%=
Lr_39_%=:
ds_read_b64_tr_b16 v[68:69], v220 offset:24960
ds_read_b64_tr_b16 v[70:71], v220 offset:25728
v_mfma_f32_16x16x32_bf16 v[184:187], v[72:75], v[160:163], v[184:187]
ds_read_b64_tr_b16 v[32:33], v218 offset:31040
ds_read_b64_tr_b16 v[34:35], v218 offset:31808
v_mfma_f32_16x16x32_bf16 v[188:191], v[76:79], v[160:163], v[188:191]
ds_read_b64_tr_b16 v[36:37], v218 offset:31072
v_mfma_f32_16x16x32_bf16 v[192:195], v[80:83], v[160:163], v[192:195]
ds_read_b64_tr_b16 v[38:39], v218 offset:31840
ds_read_b64_tr_b16 v[40:41], v218 offset:31104
v_mfma_f32_16x16x32_bf16 v[196:199], v[84:87], v[160:163], v[196:199]
ds_read_b64_tr_b16 v[42:43], v218 offset:31872
v_mfma_f32_16x16x32_bf16 v[200:203], v[88:91], v[160:163], v[200:203]
s_waitcnt lgkmcnt(6)
ds_read_b64_tr_b16 v[44:45], v218 offset:31136
ds_read_b64_tr_b16 v[46:47], v218 offset:31904
s_mov_b32 s63, 53248
buffer_load_dwordx4 a[208:211], v4, s[8:11], s63 offen sc1
ds_read_b64_tr_b16 v[48:49], v220 offset:31168
ds_read_b64_tr_b16 v[50:51], v220 offset:31936
v_mfma_f32_16x16x32_bf16 v[184:187], v[52:55], v[164:167], v[184:187]
ds_read_b64_tr_b16 v[16:17], v218 offset:37248
ds_read_b64_tr_b16 v[18:19], v218 offset:38016
v_mfma_f32_16x16x32_bf16 v[188:191], v[56:59], v[164:167], v[188:191]
ds_read_b64_tr_b16 v[20:21], v218 offset:37280
v_mfma_f32_16x16x32_bf16 v[192:195], v[60:63], v[164:167], v[192:195]
ds_read_b64_tr_b16 v[22:23], v218 offset:38048
ds_read_b64_tr_b16 v[24:25], v218 offset:37312
v_mfma_f32_16x16x32_bf16 v[196:199], v[64:67], v[164:167], v[196:199]
ds_read_b64_tr_b16 v[26:27], v218 offset:38080
v_mfma_f32_16x16x32_bf16 v[200:203], v[68:71], v[164:167], v[200:203]
s_waitcnt lgkmcnt(6)
ds_read_b64_tr_b16 v[28:29], v218 offset:37344
ds_read_b64_tr_b16 v[30:31], v218 offset:38112
buffer_load_dwordx4 a[212:215], v4, s[8:11], s63 offen offset:1024 sc1
ds_read_b64_tr_b16 v[8:9], v220 offset:37376
ds_read_b64_tr_b16 v[10:11], v220 offset:38144
v_mfma_f32_16x16x32_bf16 v[184:187], v[32:35], v[168:171], v[184:187]
ds_read_b64_tr_b16 v[92:93], v218 offset:43456
ds_read_b64_tr_b16 v[94:95], v218 offset:44224
v_mfma_f32_16x16x32_bf16 v[188:191], v[36:39], v[168:171], v[188:191]
ds_read_b64_tr_b16 v[96:97], v218 offset:43488
v_mfma_f32_16x16x32_bf16 v[192:195], v[40:43], v[168:171], v[192:195]
ds_read_b64_tr_b16 v[98:99], v218 offset:44256
ds_read_b64_tr_b16 v[100:101], v218 offset:43520
v_mfma_f32_16x16x32_bf16 v[196:199], v[44:47], v[168:171], v[196:199]
ds_read_b64_tr_b16 v[102:103], v218 offset:44288
v_mfma_f32_16x16x32_bf16 v[200:203], v[48:51], v[168:171], v[200:203]
s_waitcnt lgkmcnt(6)
ds_read_b64_tr_b16 v[104:105], v218 offset:43552
ds_read_b64_tr_b16 v[106:107], v218 offset:44320
buffer_load_dwordx4 a[216:219], v4, s[8:11], s63 offen offset:2048 sc1
ds_read_b64_tr_b16 v[108:109], v220 offset:43584
ds_read_b64_tr_b16 v[110:111], v220 offset:44352
v_mfma_f32_16x16x32_bf16 v[184:187], v[16:19], v[172:175], v[184:187]
ds_read_b64_tr_b16 v[72:73], v218 offset:49664
ds_read_b64_tr_b16 v[74:75], v218 offset:50432
v_mfma_f32_16x16x32_bf16 v[188:191], v[20:23], v[172:175], v[188:191]
ds_read_b64_tr_b16 v[76:77], v218 offset:49696
v_mfma_f32_16x16x32_bf16 v[192:195], v[24:27], v[172:175], v[192:195]
ds_read_b64_tr_b16 v[78:79], v218 offset:50464
ds_read_b64_tr_b16 v[80:81], v218 offset:49728
v_mfma_f32_16x16x32_bf16 v[196:199], v[28:31], v[172:175], v[196:199]
ds_read_b64_tr_b16 v[82:83], v218 offset:50496
v_mfma_f32_16x16x32_bf16 v[200:203], v[8:11], v[172:175], v[200:203]
s_waitcnt lgkmcnt(6)
ds_read_b64_tr_b16 v[84:85], v218 offset:49760
ds_read_b64_tr_b16 v[86:87], v218 offset:50528
buffer_load_dwordx4 a[220:223], v4, s[8:11], s63 offen offset:3072 sc1
ds_read_b64_tr_b16 v[88:89], v220 offset:49792
ds_read_b64_tr_b16 v[90:91], v220 offset:50560
v_mfma_f32_16x16x32_bf16 v[184:187], v[92:95], v[176:179], v[184:187]
v_mfma_f32_16x16x32_bf16 v[188:191], v[96:99], v[176:179], v[188:191]
v_mfma_f32_16x16x32_bf16 v[192:195], v[100:103], v[176:179], v[192:195]
v_mfma_f32_16x16x32_bf16 v[196:199], v[104:107], v[176:179], v[196:199]
v_mfma_f32_16x16x32_bf16 v[200:203], v[108:111], v[176:179], v[200:203]
s_waitcnt lgkmcnt(0)
s_mov_b32 s63, 57344
buffer_load_dwordx4 a[224:227], v4, s[8:11], s63 offen sc1
v_mfma_f32_16x16x32_bf16 v[184:187], v[72:75], v[180:183], v[184:187]
v_mfma_f32_16x16x32_bf16 v[188:191], v[76:79], v[180:183], v[188:191]
v_mfma_f32_16x16x32_bf16 v[192:195], v[80:83], v[180:183], v[192:195]
v_mfma_f32_16x16x32_bf16 v[196:199], v[84:87], v[180:183], v[196:199]
v_mfma_f32_16x16x32_bf16 v[200:203], v[88:91], v[180:183], v[200:203]
buffer_load_dwordx4 a[228:231], v4, s[8:11], s63 offen offset:1024 sc1
v_mov_b32 v136, 0x3f803f80
v_mov_b32 v137, 0x3f803f80
v_mov_b32 v138, 0x3f803f80
v_mov_b32 v139, 0x3f803f80
v_mov_b32 v122, 0
v_mov_b32 v123, 0
v_lshlrev_b32 v142, 3, v3
s_add_u32 s46, s50, s84
s_addc_u32 s47, s51, 0
s_add_u32 s48, s50, s85
s_addc_u32 s49, s51, 0
s_add_u32 s4, s50, s86
s_addc_u32 s5, s51, 0
s_mov_b32 vcc_lo, -1
s_mov_b32 vcc_hi, 0
v_cvt_pk_bf16_f32 v112, v184, v185
v_cvt_pk_bf16_f32 v113, v186, v187
v_cvt_pk_bf16_f32 v114, v188, v189
v_cvt_pk_bf16_f32 v115, v190, v191
v_min_u32 v112, -2, v112
v_min_u32 v113, -2, v113
v_min_u32 v114, -2, v114
v_min_u32 v115, -2, v115
global_store_dwordx4 v4, v[112:115], s[46:47] offset:-2048 sc1
s_nop 0
v_mfma_f32_16x16x32_bf16 v[128:131], v[112:115], v[112:115], 0
v_mfma_f32_16x16x32_bf16 v[132:135], v[136:139], v[112:115], 0
v_cvt_pk_bf16_f32 v116, v192, v193
v_cvt_pk_bf16_f32 v117, v194, v195
v_cvt_pk_bf16_f32 v118, v196, v197
v_cvt_pk_bf16_f32 v119, v198, v199
v_min_u32 v116, -2, v116
v_min_u32 v117, -2, v117
v_min_u32 v118, -2, v118
v_min_u32 v119, -2, v119
global_store_dwordx4 v4, v[116:119], s[46:47] offset:2048 sc1
s_nop 0
v_mfma_f32_16x16x32_bf16 v[128:131], v[116:119], v[116:119], v[128:131]
v_mfma_f32_16x16x32_bf16 v[132:135], v[136:139], v[116:119], v[132:135]
v_cvt_pk_bf16_f32 v124, v200, v201
v_min_u32 v124, -2, v124
v_cndmask_b32 v120, 0, v124, vcc
v_mov_b32 v126, v124
v_cvt_pk_bf16_f32 v125, v202, v203
v_min_u32 v125, -2, v125
v_cndmask_b32 v121, 0, v125, vcc
v_mov_b32 v127, v125
s_nop 0
v_mfma_f32_16x16x32_bf16 v[128:131], v[120:123], v[120:123], v[128:131]
v_mfma_f32_16x16x32_bf16 v[132:135], v[136:139], v[120:123], v[132:135]
v_permlane16_swap_b32 v124, v126
v_permlane16_swap_b32 v125, v127
s_mov_b32 exec_lo, 0xffff
s_mov_b32 exec_hi, 0
global_store_dwordx4 v4, v[124:127], s[48:49] sc1
s_mov_b64 exec, -1
s_nop 0
v_mov_b32 v141, v128
s_mov_b32 vcc_lo, 0x22222222
s_mov_b32 vcc_hi, 0x22222222
v_cndmask_b32 v141, v141, v129, vcc
s_mov_b32 vcc_lo, 0x44444444
s_mov_b32 vcc_hi, 0x44444444
v_cndmask_b32 v141, v141, v130, vcc
s_mov_b32 vcc_lo, 0x88888888
s_mov_b32 vcc_hi, 0x88888888
v_cndmask_b32 v141, v141, v131, vcc
v_min_u32 v140, -2, v132
v_min_u32 v141, -2, v141
s_mov_b32 exec_lo, 0x00f0000f
s_mov_b32 exec_hi, 0xf0000f00
global_store_dwordx2 v142, v[140:141], s[4:5] sc1
s_mov_b64 exec, -1
s_barrier
; ===== gather h (64 tokens) -> LDS, every dword checked against the sentinel; LayerNorm 2 applied on the way (wave w: its 16 tokens)
s_add_u32 s52, s50, s87
s_addc_u32 s53, s51, 0
s_and_b32 s53, s53, 0xffff
s_mov_b32 s54, 73728
s_mov_b32 s55, 131072
s_lshl_b32 s62, s39, 10
s_mul_i32 s46, s39, 18432
s_add_u32 s46, s46, 73728
v_add_u32 v5, 49152, v4
v_add_u32 v191, 98304, v4
v_add_u32 v10, s62, v4
v_add_u32 v11, 65536, v10
v_add_u32 v14, s46, v4
buffer_load_dwordx4 a[232:235], v4, s[8:11], s63 offen offset:2048 sc1
s_lshr_b32 s47, s39, 1
s_lshl_b32 s47, s47, 12
s_and_b32 s48, s39, 1
s_lshl_b32 s48, s48, 3
s_add_u32 s47, s47, s48
s_add_u32 s47, s47, 73728
v_add_u32 v6, s47, v4
v_add_u32 v7, 65536, v6
s_add_u32 s6, s50, s81
s_addc_u32 s7, s51, 0
s_add_u32 s58, s50, s88
s_addc_u32 s59, s51, 0
v_lshlrev_b32 v190, 3, v3
v_lshl_add_u32 v190, v2, 8, v190
v_lshlrev_b32 v13, 3, v2
buffer_load_dwordx4 a[236:239], v4, s[8:11], s63 offen offset:3072 sc1
s_lshl_b32 s48, s39, 5
s_add_u32 s48, s48, 161792
v_add_u32 v13, s48, v13
ds_read_b64 v[200:201], v13
ds_read_b64 v[202:203], v13 offset:128
ds_read_b64 v[204:205], v13 offset:256
ds_read_b64 v[206:207], v13 offset:384
ds_read_b64 v[198:199], v13 offset:512
v_mov_b32 v184, 0
v_mov_b32 v185, 0
v_mov_b32 v188, 0
v_mov_b32 v189, 0
s_mov_b32 s63, 61440
buffer_load_dwordx4 a[240:243], v4, s[8:11], s63 offen sc1
v_and_b32 v195, 1, v3
v_lshlrev_b32 v195, 4, v195
v_mov_b32 v186, 16256
v_lshlrev_b32 v195, v195, v186
s_mov_b32 vcc_lo, 0x00300003
s_mov_b32 vcc_hi, 0x30000300
v_cndmask_b32 v186, 0, v195, vcc
buffer_load_dwordx4 a[244:247], v4, s[8:11], s63 offen offset:1024 sc1
s_mov_b32 vcc_lo, 0x00c0000c
s_mov_b32 vcc_hi, 0xc0000c00
v_cndmask_b32 v187, 0, v195, vcc
s_waitcnt lgkmcnt(4)
v_lshlrev_b32 v64, 16, v200
v_and_b32 v65, 0xffff0000, v200
v_lshlrev_b32 v66, 16, v201
v_and_b32 v67, 0xffff0000, v201
buffer_load_dwordx4 a[248:251], v4, s[8:11], s63 offen offset:2048 sc1
s_waitcnt lgkmcnt(3)
v_lshlrev_b32 v80, 16, v202
v_and_b32 v81, 0xffff0000, v202
v_lshlrev_b32 v82, 16, v203
v_and_b32 v83, 0xffff0000, v203
s_waitcnt lgkmcnt(2)
v_lshlrev_b32 v96, 16, v204
buffer_load_dwordx4 a[252:255], v4, s[8:11], s63 offen offset:3072 sc1
v_and_b32 v97, 0xffff0000, v204
v_lshlrev_b32 v98, 16, v205
v_and_b32 v99, 0xffff0000, v205
s_waitcnt lgkmcnt(1)
v_lshlrev_b32 v112, 16, v206
v_and_b32 v113, 0xffff0000, v206
s_mov_b32 s63, 65536
buffer_load_dwordx4 v[224:227], v4, s[8:11], s63 offen sc1
v_lshlrev_b32 v114, 16, v207
v_and_b32 v115, 0xffff0000, v207
s_waitcnt lgkmcnt(0)
v_lshlrev_b32 v128, 16, v198
v_and_b32 v129, 0xffff0000, v198
v_lshlrev_b32 v130, 16, v199
buffer_load_dwordx4 v[228:231], v4, s[8:11], s63 offen offset:1024 sc1
v_and_b32 v131, 0xffff0000, v199
buffer_load_dwordx4 v[232:235], v4, s[8:11], s63 offen offset:2048 sc1
buffer_load_dwordx4 v[236:239], v4, s[8:11], s63 offen offset:3072 sc1
s_mov_b32 s63, 69632
buffer_load_dwordx4 v[240:243], v4, s[8:11], s63 offen sc1
buffer_load_dwordx4 v[244:247], v4, s[8:11], s63 offen offset:1024 sc1
buffer_load_dwordx4 v[248:251], v4, s[8:11], s63 offen offset:2048 sc1
buffer_load_dwordx4 v[252:255], v4, s[8:11], s63 offen offset:3072 sc1
global_load_dwordx2 v[160:161], v190, s[58:59] sc0 sc1
global_load_dwordx2 v[162:163], v190, s[58:59] offset:128 sc0 sc1
s_mov_b32 s63, 73728
buffer_load_dwordx4 v[208:211], v4, s[8:11], s63 offen sc1
buffer_load_dwordx4 v[212:215], v4, s[8:11], s63 offen offset:1024 sc1
buffer_load_dwordx4 v[216:219], v4, s[8:11], s63 offen offset:2048 sc1
buffer_load_dwordx4 v[220:223], v4, s[8:11], s63 offen offset:3072 sc1
s_waitcnt vmcnt(5)
v_max_u32 v12, v160, v161
s_waitcnt vmcnt(4)
v_max3_u32 v12, v12, v162, v163
v_cmp_eq_u32 vcc, -1, v12
s_nop 0
)"
R"(s_cmp_eq_u64 vcc, 0
s_cbranch_scc1 Lcd_40_%=
s_mov_b32 s97, 32768
Lcp_41_%=:
global_load_dwordx2 v[160:161], v190, s[58:59] sc0 sc1
global_load_dwordx2 v[162:163], v190, s[58:59] offset:128 sc0 sc1
s_waitcnt vmcnt(1)
v_max_u32 v12, v160, v161
s_waitcnt vmcnt(0)
v_max3_u32 v12, v12, v162, v163
v_cmp_eq_u32 vcc, -1, v12
s_nop 0
s_cmp_eq_u64 vcc, 0
s_cbranch_scc1 Lcd_40_%=
s_sub_u32 s97, s97, 1
s_cmp_lg_u32 s97, 0
s_cbranch_scc1 Lcp_41_%=
Lcd_40_%=:
s_add_u32 m0, s62, 0
s_nop 0
buffer_load_dwordx4 v4, s[52:55], m0 offen sc1 lds
s_add_u32 m0, s62, 4096
s_nop 0
buffer_load_dwordx4 v4, s[52:55], m0 offen sc1 lds
s_add_u32 m0, s62, 8192
s_nop 0
buffer_load_dwordx4 v4, s[52:55], m0 offen sc1 lds
s_add_u32 m0, s62, 12288
s_nop 0
buffer_load_dwordx4 v4, s[52:55], m0 offen sc1 lds
s_add_u32 m0, s62, 16384
s_nop 0
buffer_load_dwordx4 v4, s[52:55], m0 offen sc1 lds
s_add_u32 m0, s62, 20480
s_nop 0
buffer_load_dwordx4 v4, s[52:55], m0 offen sc1 lds
s_add_u32 m0, s62, 24576
s_nop 0
buffer_load_dwordx4 v4, s[52:55], m0 offen sc1 lds
s_add_u32 m0, s62, 28672
s_nop 0
buffer_load_dwordx4 v4, s[52:55], m0 offen sc1 lds
s_waitcnt vmcnt(7)
ds_read_b128 v[116:119], v10
s_waitcnt vmcnt(6)
ds_read_b128 v[120:123], v10 offset:4096
s_waitcnt vmcnt(5)
ds_read_b128 v[124:127], v10 offset:8192
s_waitcnt vmcnt(4)
ds_read_b128 v[108:111], v10 offset:12288
v_add_f32 v160, v160, v162
v_add_f32 v161, v161, v163
v_mov_b32 v162, v160
v_mov_b32 v163, v161
s_nop 0
v_permlane16_swap_b32 v160, v162
v_permlane16_swap_b32 v161, v163
v_add_f32 v160, v160, v162
v_add_f32 v161, v161, v163
v_mov_b32 v162, v160
v_mov_b32 v163, v161
s_nop 0
v_permlane32_swap_b32 v160, v162
v_permlane32_swap_b32 v161, v163
v_add_f32 v160, v160, v162
v_add_f32 v161, v161, v163
v_mul_f32 v9, 0x3ae38e39, v160
v_mul_f32 v8, 0x3ae38e39, v161
v_fma_f32 v8, -v9, v9, v8
v_max_f32 v8, 0, v8
v_cvt_pk_bf16_f32 v162, v8, v9
v_lshlrev_b32 v8, 16, v162
v_and_b32 v9, 0xffff0000, v162
v_add_f32 v8, 0x3727c5ac, v8
v_cvt_pk_bf16_f32 v8, v8, v8
v_lshlrev_b32 v8, 16, v8
v_sqrt_f32 v8, v8
s_nop 0
v_cvt_pk_bf16_f32 v8, v8, v8
v_lshlrev_b32 v8, 16, v8
v_rcp_f32 v8, v8
s_nop 0
v_mul_f32 v9, -v9, v8
s_waitcnt lgkmcnt(3)
v_max_u32 v12, v116, v117
v_max3_u32 v12, v12, v118, v119
s_waitcnt lgkmcnt(2)
v_max3_u32 v12, v12, v120, v121
v_max3_u32 v12, v12, v122, v123
s_waitcnt lgkmcnt(1)
v_max3_u32 v12, v12, v124, v125
v_max3_u32 v12, v12, v126, v127
s_waitcnt lgkmcnt(0)
v_max3_u32 v12, v12, v108, v109
v_max3_u32 v12, v12, v110, v111
v_cmp_eq_u32 vcc, -1, v12
s_nop 0
s_cmp_eq_u64 vcc, 0
s_cbranch_scc1 Lcd_42_%=
s_mov_b32 s97, 32768
Lcp_43_%=:
s_waitcnt vmcnt(0)
s_add_u32 m0, s62, 0
s_nop 0
buffer_load_dwordx4 v4, s[52:55], m0 offen sc1 lds
s_add_u32 m0, s62, 4096
s_nop 0
buffer_load_dwordx4 v4, s[52:55], m0 offen sc1 lds
s_add_u32 m0, s62, 8192
s_nop 0
buffer_load_dwordx4 v4, s[52:55], m0 offen sc1 lds
s_add_u32 m0, s62, 12288
s_nop 0
buffer_load_dwordx4 v4, s[52:55], m0 offen sc1 lds
s_waitcnt vmcnt(3)
ds_read_b128 v[116:119], v10
s_waitcnt vmcnt(2)
ds_read_b128 v[120:123], v10 offset:4096
s_waitcnt vmcnt(1)
ds_read_b128 v[124:127], v10 offset:8192
s_waitcnt vmcnt(0)
ds_read_b128 v[108:111], v10 offset:12288
s_waitcnt lgkmcnt(3)
v_max_u32 v12, v116, v117
v_max3_u32 v12, v12, v118, v119
s_waitcnt lgkmcnt(2)
v_max3_u32 v12, v12, v120, v121
v_max3_u32 v12, v12, v122, v123
s_waitcnt lgkmcnt(1)
v_max3_u32 v12, v12, v124, v125
v_max3_u32 v12, v12, v126, v127
s_waitcnt lgkmcnt(0)
v_max3_u32 v12, v12, v108, v109
v_max3_u32 v12, v12, v110, v111
v_cmp_eq_u32 vcc, -1, v12
s_nop 0
s_cmp_eq_u64 vcc, 0
s_cbranch_scc1 Lcd_42_%=
s_sub_u32 s97, s97, 1
s_cmp_lg_u32 s97, 0
s_cbranch_scc1 Lcp_43_%=
Lcd_42_%=:
v_lshlrev_b32 v144, 16, v116
v_and_b32 v145, 0xffff0000, v116
v_lshlrev_b32 v146, 16, v117
v_and_b32 v147, 0xffff0000, v117
v_lshlrev_b32 v148, 16, v118
s_add_u32 m0, s62, 32768
s_nop 0
buffer_load_dwordx4 v4, s[52:55], m0 offen sc1 lds
v_and_b32 v149, 0xffff0000, v118
v_lshlrev_b32 v150, 16, v119
v_and_b32 v151, 0xffff0000, v119
v_pk_fma_f32 v[144:145], v[144:145], v[8:9], v[8:9] op_sel:[0,0,1] op_sel_hi:[1,0,1]
v_pk_fma_f32 v[146:147], v[146:147], v[8:9], v[8:9] op_sel:[0,0,1] op_sel_hi:[1,0,1]
v_pk_fma_f32 v[148:149], v[148:149], v[8:9], v[8:9] op_sel:[0,0,1] op_sel_hi:[1,0,1]
v_pk_fma_f32 v[150:151], v[150:151], v[8:9], v[8:9] op_sel:[0,0,1] op_sel_hi:[1,0,1]
v_cvt_pk_bf16_f32 v116, v144, v145
v_cvt_pk_bf16_f32 v117, v146, v147
v_cvt_pk_bf16_f32 v118, v148, v149
s_add_u32 m0, s62, 36864
s_nop 0
buffer_load_dwordx4 v4, s[52:55], m0 offen sc1 lds
v_cvt_pk_bf16_f32 v119, v150, v151
ds_write_b128 v10, v[116:119]
v_lshlrev_b32 v144, 16, v120
v_and_b32 v145, 0xffff0000, v120
v_lshlrev_b32 v146, 16, v121
v_and_b32 v147, 0xffff0000, v121
v_lshlrev_b32 v148, 16, v122
v_and_b32 v149, 0xffff0000, v122
v_lshlrev_b32 v150, 16, v123
v_and_b32 v151, 0xffff0000, v123
v_pk_fma_f32 v[144:145], v[144:145], v[8:9], v[8:9] op_sel:[0,0,1] op_sel_hi:[1,0,1]
s_add_u32 m0, s62, 40960
s_nop 0
buffer_load_dwordx4 v4, s[52:55], m0 offen sc1 lds
v_pk_fma_f32 v[146:147], v[146:147], v[8:9], v[8:9] op_sel:[0,0,1] op_sel_hi:[1,0,1]
v_pk_fma_f32 v[148:149], v[148:149], v[8:9], v[8:9] op_sel:[0,0,1] op_sel_hi:[1,0,1]
v_pk_fma_f32 v[150:151], v[150:151], v[8:9], v[8:9] op_sel:[0,0,1] op_sel_hi:[1,0,1]
v_cvt_pk_bf16_f32 v120, v144, v145
v_cvt_pk_bf16_f32 v121, v146, v147
v_cvt_pk_bf16_f32 v122, v148, v149
v_cvt_pk_bf16_f32 v123, v150, v151
ds_write_b128 v10, v[120:123] offset:4096
s_waitcnt lgkmcnt(0)
s_barrier
ds_read_b128 v[16:19], v4
ds_read_b128 v[20:23], v4 offset:1024
ds_read_b128 v[24:27], v4 offset:2048
ds_read_b128 v[28:31], v4 offset:3072
ds_read_b128 v[32:35], v4 offset:4096
ds_read_b128 v[36:39], v4 offset:5120
ds_read_b128 v[40:43], v4 offset:6144
ds_read_b128 v[44:47], v4 offset:7168
s_waitcnt vmcnt(6)
ds_read_b128 v[152:155], v10 offset:16384
v_lshlrev_b32 v144, 16, v124
v_and_b32 v145, 0xffff0000, v124
v_lshlrev_b32 v146, 16, v125
s_add_u32 m0, s62, 45056
s_nop 0
buffer_load_dwordx4 v4, s[52:55], m0 offen sc1 lds
v_and_b32 v147, 0xffff0000, v125
v_lshlrev_b32 v148, 16, v126
v_and_b32 v149, 0xffff0000, v126
v_lshlrev_b32 v150, 16, v127
v_and_b32 v151, 0xffff0000, v127
v_pk_fma_f32 v[144:145], v[144:145], v[8:9], v[8:9] op_sel:[0,0,1] op_sel_hi:[1,0,1]
v_pk_fma_f32 v[146:147], v[146:147], v[8:9], v[8:9] op_sel:[0,0,1] op_sel_hi:[1,0,1]
v_pk_fma_f32 v[148:149], v[148:149], v[8:9], v[8:9] op_sel:[0,0,1] op_sel_hi:[1,0,1]
v_pk_fma_f32 v[150:151], v[150:151], v[8:9], v[8:9] op_sel:[0,0,1] op_sel_hi:[1,0,1]
v_cvt_pk_bf16_f32 v124, v144, v145
s_add_u32 m0, s62, 49152
s_nop 0
buffer_load_dwordx4 v4, s[52:55], m0 offen sc1 lds
v_cvt_pk_bf16_f32 v125, v146, v147
v_cvt_pk_bf16_f32 v126, v148, v149
v_cvt_pk_bf16_f32 v127, v150, v151
ds_write_b128 v10, v[124:127] offset:8192
v_lshlrev_b32 v144, 16, v108
v_and_b32 v145, 0xffff0000, v108
v_lshlrev_b32 v146, 16, v109
v_and_b32 v147, 0xffff0000, v109
v_lshlrev_b32 v148, 16, v110
v_and_b32 v149, 0xffff0000, v110
v_lshlrev_b32 v150, 16, v111
s_add_u32 m0, s62, 53248
s_nop 0
buffer_load_dwordx4 v4, s[52:55], m0 offen sc1 lds
v_and_b32 v151, 0xffff0000, v111
v_pk_fma_f32 v[144:145], v[144:145], v[8:9], v[8:9] op_sel:[0,0,1] op_sel_hi:[1,0,1]
v_pk_fma_f32 v[146:147], v[146:147], v[8:9], v[8:9] op_sel:[0,0,1] op_sel_hi:[1,0,1]
v_pk_fma_f32 v[148:149], v[148:149], v[8:9], v[8:9] op_sel:[0,0,1] op_sel_hi:[1,0,1]
v_pk_fma_f32 v[150:151], v[150:151], v[8:9], v[8:9] op_sel:[0,0,1] op_sel_hi:[1,0,1]
v_cvt_pk_bf16_f32 v108, v144, v145
v_cvt_pk_bf16_f32 v109, v146, v147
v_cvt_pk_bf16_f32 v110, v148, v149
v_cvt_pk_bf16_f32 v111, v150, v151
ds_write_b128 v10, v[108:111] offset:12288
; column tiles 0..4 of the weight stream
s_waitcnt lgkmcnt(9)
v_mfma_f32_16x16x32_bf16 v[68:71], a[0:3], v[20:23], v[64:67]
s_waitcnt lgkmcnt(2)
v_max_u32 v12, v152, v153
v_max3_u32 v12, v12, v154, v155
v_mfma_f32_16x16x32_bf16 v[72:75], a[0:3], v[24:27], v[64:67]
v_lshlrev_b32 v144, 16, v152
v_and_b32 v145, 0xffff0000, v152
v_mfma_f32_16x16x32_bf16 v[76:79], a[0:3], v[28:31], v[64:67]
v_lshlrev_b32 v146, 16, v153
v_and_b32 v147, 0xffff0000, v153
v_mfma_f32_16x16x32_bf16 v[64:67], a[0:3], v[16:19], v[64:67]
s_mov_b32 s63, 77824
buffer_load_dwordx4 a[0:3], v4, s[8:11], s63 offen sc1
s_waitcnt lgkmcnt(0)
s_barrier
v_mfma_f32_16x16x32_bf16 v[84:87], a[4:7], v[20:23], v[80:83]
v_lshlrev_b32 v148, 16, v154
v_and_b32 v149, 0xffff0000, v154
v_mfma_f32_16x16x32_bf16 v[88:91], a[4:7], v[24:27], v[80:83]
v_lshlrev_b32 v150, 16, v155
v_and_b32 v151, 0xffff0000, v155
v_mfma_f32_16x16x32_bf16 v[92:95], a[4:7], v[28:31], v[80:83]
v_fma_f32 v144, v144, v8, v9
v_fma_f32 v145, v145, v8, v9
v_mfma_f32_16x16x32_bf16 v[80:83], a[4:7], v[16:19], v[80:83]
s_add_u32 m0, s62, 57344
s_nop 0
buffer_load_dwordx4 v4, s[52:55], m0 offen sc1 lds
buffer_load_dwordx4 a[4:7], v4, s[8:11], s63 offen offset:1024 sc1
v_mfma_f32_16x16x32_bf16 v[100:103], a[8:11], v[20:23], v[96:99]
s_waitcnt vmcnt(11)
ds_read_b128 v[156:159], v10 offset:20480
v_fma_f32 v146, v146, v8, v9
v_mfma_f32_16x16x32_bf16 v[104:107], a[8:11], v[24:27], v[96:99]
v_fma_f32 v147, v147, v8, v9
v_fma_f32 v148, v148, v8, v9
v_mfma_f32_16x16x32_bf16 v[108:111], a[8:11], v[28:31], v[96:99]
v_fma_f32 v149, v149, v8, v9
v_fma_f32 v150, v150, v8, v9
v_mfma_f32_16x16x32_bf16 v[96:99], a[8:11], v[16:19], v[96:99]
buffer_load_dwordx4 a[8:11], v4, s[8:11], s63 offen offset:2048 sc1
v_fma_f32 v151, v151, v8, v9
v_mfma_f32_16x16x32_bf16 v[116:119], a[12:15], v[20:23], v[112:115]
v_cvt_pk_bf16_f32 v152, v144, v145
v_cvt_pk_bf16_f32 v153, v146, v147
v_mfma_f32_16x16x32_bf16 v[120:123], a[12:15], v[24:27], v[112:115]
v_cvt_pk_bf16_f32 v154, v148, v149
v_cvt_pk_bf16_f32 v155, v150, v151
v_mfma_f32_16x16x32_bf16 v[124:127], a[12:15], v[28:31], v[112:115]
ds_write_b128 v10, v[152:155] offset:16384
v_mfma_f32_16x16x32_bf16 v[112:115], a[12:15], v[16:19], v[112:115]
buffer_load_dwordx4 a[12:15], v4, s[8:11], s63 offen offset:3072 sc1
v_mfma_f32_16x16x32_bf16 v[132:135], a[16:19], v[20:23], v[128:131]
v_mfma_f32_16x16x32_bf16 v[136:139], a[16:19], v[24:27], v[128:131]
v_mfma_f32_16x16x32_bf16 v[140:143], a[16:19], v[28:31], v[128:131]
v_mfma_f32_16x16x32_bf16 v[128:131], a[16:19], v[16:19], v[128:131]
s_mov_b32 s63, 81920
buffer_load_dwordx4 a[16:19], v4, s[8:11], s63 offen sc1
v_mfma_f32_16x16x32_bf16 v[64:67], a[20:23], v[32:35], v[64:67]
ds_read_b128 v[48:51], v4 offset:8192
ds_read_b128 v[52:55], v4 offset:9216
v_mfma_f32_16x16x32_bf16 v[68:71], a[20:23], v[36:39], v[68:71]
ds_read_b128 v[56:59], v4 offset:10240
ds_read_b128 v[60:63], v4 offset:11264
v_mfma_f32_16x16x32_bf16 v[72:75], a[20:23], v[40:43], v[72:75]
s_waitcnt lgkmcnt(5)
v_max3_u32 v12, v12, v156, v157
v_max3_u32 v12, v12, v158, v159
v_mfma_f32_16x16x32_bf16 v[76:79], a[20:23], v[44:47], v[76:79]
buffer_load_dwordx4 a[20:23], v4, s[8:11], s63 offen offset:1024 sc1
v_lshlrev_b32 v144, 16, v156
v_mfma_f32_16x16x32_bf16 v[80:83], a[24:27], v[32:35], v[80:83]
v_and_b32 v145, 0xffff0000, v156
v_lshlrev_b32 v146, 16, v157
v_mfma_f32_16x16x32_bf16 v[84:87], a[24:27], v[36:39], v[84:87]
v_and_b32 v147, 0xffff0000, v157
v_lshlrev_b32 v148, 16, v158
v_mfma_f32_16x16x32_bf16 v[88:91], a[24:27], v[40:43], v[88:91]
v_and_b32 v149, 0xffff0000, v158
v_lshlrev_b32 v150, 16, v159
v_mfma_f32_16x16x32_bf16 v[92:95], a[24:27], v[44:47], v[92:95]
s_add_u32 m0, s62, 61440
s_nop 0
buffer_load_dwordx4 v4, s[52:55], m0 offen sc1 lds
buffer_load_dwordx4 a[24:27], v4, s[8:11], s63 offen offset:2048 sc1
v_mfma_f32_16x16x32_bf16 v[96:99], a[28:31], v[32:35], v[96:99]
LW1_1_%=:
s_cbranch_execz LW1_5_%=
v_and_b32 v151, 0xffff0000, v159
v_fma_f32 v144, v144, v8, v9
v_mfma_f32_16x16x32_bf16 v[100:103], a[28:31], v[36:39], v[100:103]
v_fma_f32 v145, v145, v8, v9
v_mfma_f32_16x16x32_bf16 v[104:107], a[28:31], v[40:43], v[104:107]
s_waitcnt vmcnt(16)
ds_read_b128 v[152:155], v10 offset:24576
v_fma_f32 v146, v146, v8, v9
v_mfma_f32_16x16x32_bf16 v[108:111], a[28:31], v[44:47], v[108:111]
buffer_load_dwordx4 a[28:31], v4, s[8:11], s63 offen offset:3072 sc1
v_fma_f32 v147, v147, v8, v9
v_mfma_f32_16x16x32_bf16 v[112:115], a[32:35], v[32:35], v[112:115]
v_fma_f32 v148, v148, v8, v9
v_fma_f32 v149, v149, v8, v9
v_mfma_f32_16x16x32_bf16 v[116:119], a[32:35], v[36:39], v[116:119]
v_fma_f32 v150, v150, v8, v9
v_fma_f32 v151, v151, v8, v9
v_mfma_f32_16x16x32_bf16 v[120:123], a[32:35], v[40:43], v[120:123]
v_cvt_pk_bf16_f32 v156, v144, v145
v_cvt_pk_bf16_f32 v157, v146, v147
v_mfma_f32_16x16x32_bf16 v[124:127], a[32:35], v[44:47], v[124:127]
s_mov_b32 s63, 86016
buffer_load_dwordx4 a[32:35], v4, s[8:11], s63 offen sc1
v_cvt_pk_bf16_f32 v158, v148, v149
v_mfma_f32_16x16x32_bf16 v[128:131], a[36:39], v[32:35], v[128:131]
v_cvt_pk_bf16_f32 v159, v150, v151
v_mfma_f32_16x16x32_bf16 v[132:135], a[36:39], v[36:39], v[132:135]
ds_write_b128 v10, v[156:159] offset:20480
v_mfma_f32_16x16x32_bf16 v[136:139], a[36:39], v[40:43], v[136:139]
v_mfma_f32_16x16x32_bf16 v[140:143], a[36:39], v[44:47], v[140:143]
s_waitcnt lgkmcnt(2)
buffer_load_dwordx4 a[36:39], v4, s[8:11], s63 offen offset:1024 sc1
v_mfma_f32_16x16x32_bf16 v[64:67], a[40:43], v[48:51], v[64:67]
ds_read_b128 v[16:19], v4 offset:12288
ds_read_b128 v[20:23], v4 offset:13312
v_mfma_f32_16x16x32_bf16 v[68:71], a[40:43], v[52:55], v[68:71]
ds_read_b128 v[24:27], v4 offset:14336
ds_read_b128 v[28:31], v4 offset:15360
v_mfma_f32_16x16x32_bf16 v[72:75], a[40:43], v[56:59], v[72:75]
s_waitcnt lgkmcnt(5)
v_max3_u32 v12, v12, v152, v153
v_max3_u32 v12, v12, v154, v155
v_mfma_f32_16x16x32_bf16 v[76:79], a[40:43], v[60:63], v[76:79]
buffer_load_dwordx4 a[40:43], v4, s[8:11], s63 offen offset:2048 sc1
v_lshlrev_b32 v144, 16, v152
v_mfma_f32_16x16x32_bf16 v[80:83], a[44:47], v[48:51], v[80:83]
v_and_b32 v145, 0xffff0000, v152
v_lshlrev_b32 v146, 16, v153
v_mfma_f32_16x16x32_bf16 v[84:87], a[44:47], v[52:55], v[84:87]
v_and_b32 v147, 0xffff0000, v153
v_lshlrev_b32 v148, 16, v154
v_mfma_f32_16x16x32_bf16 v[88:91], a[44:47], v[56:59], v[88:91]
v_and_b32 v149, 0xffff0000, v154
v_lshlrev_b32 v150, 16, v155
v_mfma_f32_16x16x32_bf16 v[92:95], a[44:47], v[60:63], v[92:95]
s_add_u32 m0, s62, 65536
s_nop 0
buffer_load_dwordx4 v4, s[52:55], m0 offen sc1 lds
buffer_load_dwordx4 a[44:47], v4, s[8:11], s63 offen offset:3072 sc1
v_mfma_f32_16x16x32_bf16 v[96:99], a[48:51], v[48:51], v[96:99]
v_and_b32 v151, 0xffff0000, v155
v_fma_f32 v144, v144, v8, v9
v_mfma_f32_16x16x32_bf16 v[100:103], a[48:51], v[52:55], v[100:103]
v_fma_f32 v145, v145, v8, v9
v_mfma_f32_16x16x32_bf16 v[104:107], a[48:51], v[56:59], v[104:107]
s_waitcnt vmcnt(21)
ds_read_b128 v[156:159], v10 offset:28672
v_fma_f32 v146, v146, v8, v9
v_mfma_f32_16x16x32_bf16 v[108:111], a[48:51], v[60:63], v[108:111]
s_mov_b32 s63, 90112
)"
R"(buffer_load_dwordx4 a[48:51], v4, s[8:11], s63 offen sc1
v_fma_f32 v147, v147, v8, v9
v_mfma_f32_16x16x32_bf16 v[112:115], a[52:55], v[48:51], v[112:115]
v_fma_f32 v148, v148, v8, v9
v_fma_f32 v149, v149, v8, v9
v_mfma_f32_16x16x32_bf16 v[116:119], a[52:55], v[52:55], v[116:119]
v_fma_f32 v150, v150, v8, v9
v_fma_f32 v151, v151, v8, v9
v_mfma_f32_16x16x32_bf16 v[120:123], a[52:55], v[56:59], v[120:123]
v_cvt_pk_bf16_f32 v152, v144, v145
v_cvt_pk_bf16_f32 v153, v146, v147
v_mfma_f32_16x16x32_bf16 v[124:127], a[52:55], v[60:63], v[124:127]
buffer_load_dwordx4 a[52:55], v4, s[8:11], s63 offen offset:1024 sc1
v_cvt_pk_bf16_f32 v154, v148, v149
v_mfma_f32_16x16x32_bf16 v[128:131], a[56:59], v[48:51], v[128:131]
v_cvt_pk_bf16_f32 v155, v150, v151
v_mfma_f32_16x16x32_bf16 v[132:135], a[56:59], v[52:55], v[132:135]
ds_write_b128 v10, v[152:155] offset:24576
v_cmp_eq_u32 vcc, -1, v12
s_nop 0
s_cmp_eq_u64 vcc, 0
s_cbranch_scc0 Lx_46_%=
Lr_47_%=:
v_mfma_f32_16x16x32_bf16 v[136:139], a[56:59], v[56:59], v[136:139]
v_mfma_f32_16x16x32_bf16 v[140:143], a[56:59], v[60:63], v[140:143]
s_waitcnt lgkmcnt(2)
buffer_load_dwordx4 a[56:59], v4, s[8:11], s63 offen offset:2048 sc1
v_mfma_f32_16x16x32_bf16 v[64:67], a[60:63], v[16:19], v[64:67]
s_waitcnt lgkmcnt(1)
v_max_u32 v12, v156, v157
v_max3_u32 v12, v12, v158, v159
v_mfma_f32_16x16x32_bf16 v[68:71], a[60:63], v[20:23], v[68:71]
v_lshlrev_b32 v144, 16, v156
v_and_b32 v145, 0xffff0000, v156
v_mfma_f32_16x16x32_bf16 v[72:75], a[60:63], v[24:27], v[72:75]
s_waitcnt lgkmcnt(0)
s_barrier
ds_read_b128 v[32:35], v4 offset:16384
v_mfma_f32_16x16x32_bf16 v[76:79], a[60:63], v[28:31], v[76:79]
buffer_load_dwordx4 a[60:63], v4, s[8:11], s63 offen offset:3072 sc1
ds_read_b128 v[36:39], v4 offset:17408
v_mfma_f32_16x16x32_bf16 v[80:83], a[64:67], v[16:19], v[80:83]
ds_read_b128 v[40:43], v4 offset:18432
ds_read_b128 v[44:47], v4 offset:19456
v_mfma_f32_16x16x32_bf16 v[84:87], a[64:67], v[20:23], v[84:87]
v_lshlrev_b32 v146, 16, v157
v_and_b32 v147, 0xffff0000, v157
v_mfma_f32_16x16x32_bf16 v[88:91], a[64:67], v[24:27], v[88:91]
v_lshlrev_b32 v148, 16, v158
v_and_b32 v149, 0xffff0000, v158
v_mfma_f32_16x16x32_bf16 v[92:95], a[64:67], v[28:31], v[92:95]
s_add_u32 m0, s62, 69632
s_nop 0
buffer_load_dwordx4 v4, s[52:55], m0 offen sc1 lds
s_mov_b32 s63, 94208
buffer_load_dwordx4 a[64:67], v4, s[8:11], s63 offen sc1
v_mfma_f32_16x16x32_bf16 v[96:99], a[68:71], v[16:19], v[96:99]
v_lshlrev_b32 v150, 16, v159
v_and_b32 v151, 0xffff0000, v159
v_mfma_f32_16x16x32_bf16 v[100:103], a[68:71], v[20:23], v[100:103]
v_fma_f32 v144, v144, v8, v9
v_fma_f32 v145, v145, v8, v9
v_mfma_f32_16x16x32_bf16 v[104:107], a[68:71], v[24:27], v[104:107]
s_waitcnt vmcnt(26)
ds_read_b128 v[152:155], v10 offset:32768
v_fma_f32 v146, v146, v8, v9
v_mfma_f32_16x16x32_bf16 v[108:111], a[68:71], v[28:31], v[108:111]
buffer_load_dwordx4 a[68:71], v4, s[8:11], s63 offen offset:1024 sc1
v_fma_f32 v147, v147, v8, v9
v_mfma_f32_16x16x32_bf16 v[112:115], a[72:75], v[16:19], v[112:115]
v_fma_f32 v148, v148, v8, v9
v_fma_f32 v149, v149, v8, v9
v_mfma_f32_16x16x32_bf16 v[116:119], a[72:75], v[20:23], v[116:119]
v_fma_f32 v150, v150, v8, v9
v_fma_f32 v151, v151, v8, v9
v_mfma_f32_16x16x32_bf16 v[120:123], a[72:75], v[24:27], v[120:123]
v_cvt_pk_bf16_f32 v156, v144, v145
v_cvt_pk_bf16_f32 v157, v146, v147
v_mfma_f32_16x16x32_bf16 v[124:127], a[72:75], v[28:31], v[124:127]
buffer_load_dwordx4 a[72:75], v4, s[8:11], s63 offen offset:2048 sc1
v_cvt_pk_bf16_f32 v158, v148, v149
v_mfma_f32_16x16x32_bf16 v[128:131], a[76:79], v[16:19], v[128:131]
v_cvt_pk_bf16_f32 v159, v150, v151
v_mfma_f32_16x16x32_bf16 v[132:135], a[76:79], v[20:23], v[132:135]
ds_write_b128 v10, v[156:159] offset:28672
v_mfma_f32_16x16x32_bf16 v[136:139], a[76:79], v[24:27], v[136:139]
v_mfma_f32_16x16x32_bf16 v[140:143], a[76:79], v[28:31], v[140:143]
s_waitcnt lgkmcnt(2)
buffer_load_dwordx4 a[76:79], v4, s[8:11], s63 offen offset:3072 sc1
v_mfma_f32_16x16x32_bf16 v[64:67], a[80:83], v[32:35], v[64:67]
ds_read_b128 v[48:51], v4 offset:20480
ds_read_b128 v[52:55], v4 offset:21504
v_mfma_f32_16x16x32_bf16 v[68:71], a[80:83], v[36:39], v[68:71]
ds_read_b128 v[56:59], v4 offset:22528
ds_read_b128 v[60:63], v4 offset:23552
v_mfma_f32_16x16x32_bf16 v[72:75], a[80:83], v[40:43], v[72:75]
s_waitcnt lgkmcnt(5)
v_max3_u32 v12, v12, v152, v153
v_max3_u32 v12, v12, v154, v155
v_mfma_f32_16x16x32_bf16 v[76:79], a[80:83], v[44:47], v[76:79]
s_mov_b32 s63, 98304
buffer_load_dwordx4 a[80:83], v4, s[8:11], s63 offen sc1
v_lshlrev_b32 v144, 16, v152
v_mfma_f32_16x16x32_bf16 v[80:83], a[84:87], v[32:35], v[80:83]
v_and_b32 v145, 0xffff0000, v152
v_lshlrev_b32 v146, 16, v153
v_mfma_f32_16x16x32_bf16 v[84:87], a[84:87], v[36:39], v[84:87]
v_and_b32 v147, 0xffff0000, v153
v_lshlrev_b32 v148, 16, v154
v_mfma_f32_16x16x32_bf16 v[88:91], a[84:87], v[40:43], v[88:91]
v_and_b32 v149, 0xffff0000, v154
v_lshlrev_b32 v150, 16, v155
v_mfma_f32_16x16x32_bf16 v[92:95], a[84:87], v[44:47], v[92:95]
buffer_load_dwordx4 a[84:87], v4, s[8:11], s63 offen offset:1024 sc1
v_and_b32 v151, 0xffff0000, v155
v_mfma_f32_16x16x32_bf16 v[96:99], a[88:91], v[32:35], v[96:99]
v_fma_f32 v144, v144, v8, v9
v_fma_f32 v145, v145, v8, v9
v_mfma_f32_16x16x32_bf16 v[100:103], a[88:91], v[36:39], v[100:103]
s_waitcnt vmcnt(30)
ds_read_b128 v[156:159], v10 offset:36864
v_fma_f32 v146, v146, v8, v9
v_mfma_f32_16x16x32_bf16 v[104:107], a[88:91], v[40:43], v[104:107]
v_fma_f32 v147, v147, v8, v9
v_fma_f32 v148, v148, v8, v9
v_mfma_f32_16x16x32_bf16 v[108:111], a[88:91], v[44:47], v[108:111]
buffer_load_dwordx4 a[88:91], v4, s[8:11], s63 offen offset:2048 sc1
v_fma_f32 v149, v149, v8, v9
v_mfma_f32_16x16x32_bf16 v[112:115], a[92:95], v[32:35], v[112:115]
v_fma_f32 v150, v150, v8, v9
v_fma_f32 v151, v151, v8, v9
v_mfma_f32_16x16x32_bf16 v[116:119], a[92:95], v[36:39], v[116:119]
v_cvt_pk_bf16_f32 v152, v144, v145
v_cvt_pk_bf16_f32 v153, v146, v147
v_mfma_f32_16x16x32_bf16 v[120:123], a[92:95], v[40:43], v[120:123]
v_cvt_pk_bf16_f32 v154, v148, v149
v_cvt_pk_bf16_f32 v155, v150, v151
v_mfma_f32_16x16x32_bf16 v[124:127], a[92:95], v[44:47], v[124:127]
buffer_load_dwordx4 a[92:95], v4, s[8:11], s63 offen offset:3072 sc1
ds_write_b128 v10, v[152:155] offset:32768
v_mfma_f32_16x16x32_bf16 v[128:131], a[96:99], v[32:35], v[128:131]
v_mfma_f32_16x16x32_bf16 v[132:135], a[96:99], v[36:39], v[132:135]
v_mfma_f32_16x16x32_bf16 v[136:139], a[96:99], v[40:43], v[136:139]
v_mfma_f32_16x16x32_bf16 v[140:143], a[96:99], v[44:47], v[140:143]
s_waitcnt lgkmcnt(2)
s_mov_b32 s63, 102400
buffer_load_dwordx4 a[96:99], v4, s[8:11], s63 offen sc1
v_mfma_f32_16x16x32_bf16 v[64:67], a[100:103], v[48:51], v[64:67]
ds_read_b128 v[16:19], v4 offset:24576
ds_read_b128 v[20:23], v4 offset:25600
v_mfma_f32_16x16x32_bf16 v[68:71], a[100:103], v[52:55], v[68:71]
ds_read_b128 v[24:27], v4 offset:26624
ds_read_b128 v[28:31], v4 offset:27648
v_mfma_f32_16x16x32_bf16 v[72:75], a[100:103], v[56:59], v[72:75]
s_waitcnt lgkmcnt(5)
v_max3_u32 v12, v12, v156, v157
v_max3_u32 v12, v12, v158, v159
v_mfma_f32_16x16x32_bf16 v[76:79], a[100:103], v[60:63], v[76:79]
buffer_load_dwordx4 a[100:103], v4, s[8:11], s63 offen offset:1024 sc1
v_lshlrev_b32 v144, 16, v156
v_mfma_f32_16x16x32_bf16 v[80:83], a[104:107], v[48:51], v[80:83]
v_and_b32 v145, 0xffff0000, v156
v_lshlrev_b32 v146, 16, v157
v_mfma_f32_16x16x32_bf16 v[84:87], a[104:107], v[52:55], v[84:87]
v_and_b32 v147, 0xffff0000, v157
v_lshlrev_b32 v148, 16, v158
v_mfma_f32_16x16x32_bf16 v[88:91], a[104:107], v[56:59], v[88:91]
v_and_b32 v149, 0xffff0000, v158
v_lshlrev_b32 v150, 16, v159
v_mfma_f32_16x16x32_bf16 v[92:95], a[104:107], v[60:63], v[92:95]
buffer_load_dwordx4 a[104:107], v4, s[8:11], s63 offen offset:2048 sc1
v_and_b32 v151, 0xffff0000, v159
v_mfma_f32_16x16x32_bf16 v[96:99], a[108:111], v[48:51], v[96:99]
v_fma_f32 v144, v144, v8, v9
v_fma_f32 v145, v145, v8, v9
v_mfma_f32_16x16x32_bf16 v[100:103], a[108:111], v[52:55], v[100:103]
s_waitcnt vmcnt(34)
ds_read_b128 v[152:155], v10 offset:40960
v_fma_f32 v146, v146, v8, v9
v_mfma_f32_16x16x32_bf16 v[104:107], a[108:111], v[56:59], v[104:107]
v_fma_f32 v147, v147, v8, v9
v_fma_f32 v148, v148, v8, v9
v_mfma_f32_16x16x32_bf16 v[108:111], a[108:111], v[60:63], v[108:111]
buffer_load_dwordx4 a[108:111], v4, s[8:11], s63 offen offset:3072 sc1
v_fma_f32 v149, v149, v8, v9
v_mfma_f32_16x16x32_bf16 v[112:115], a[112:115], v[48:51], v[112:115]
v_fma_f32 v150, v150, v8, v9
v_fma_f32 v151, v151, v8, v9
v_mfma_f32_16x16x32_bf16 v[116:119], a[112:115], v[52:55], v[116:119]
v_cvt_pk_bf16_f32 v156, v144, v145
v_cvt_pk_bf16_f32 v157, v146, v147
v_mfma_f32_16x16x32_bf16 v[120:123], a[112:115], v[56:59], v[120:123]
v_cvt_pk_bf16_f32 v158, v148, v149
v_cvt_pk_bf16_f32 v159, v150, v151
v_mfma_f32_16x16x32_bf16 v[124:127], a[112:115], v[60:63], v[124:127]
s_mov_b32 s63, 106496
buffer_load_dwordx4 a[112:115], v4, s[8:11], s63 offen sc1
ds_write_b128 v10, v[156:159] offset:36864
v_mfma_f32_16x16x32_bf16 v[128:131], a[116:119], v[48:51], v[128:131]
v_cmp_eq_u32 vcc, -1, v12
s_nop 0
s_cmp_eq_u64 vcc, 0
s_cbranch_scc0 Lx_50_%=
Lr_51_%=:
v_mfma_f32_16x16x32_bf16 v[132:135], a[116:119], v[52:55], v[132:135]
v_mfma_f32_16x16x32_bf16 v[136:139], a[116:119], v[56:59], v[136:139]
v_mfma_f32_16x16x32_bf16 v[140:143], a[116:119], v[60:63], v[140:143]
s_waitcnt lgkmcnt(2)
buffer_load_dwordx4 a[116:119], v4, s[8:11], s63 offen offset:1024 sc1
v_mfma_f32_16x16x32_bf16 v[64:67], a[120:123], v[16:19], v[64:67]
s_waitcnt lgkmcnt(1)
v_max_u32 v12, v152, v153
v_max3_u32 v12, v12, v154, v155
v_mfma_f32_16x16x32_bf16 v[68:71], a[120:123], v[20:23], v[68:71]
v_lshlrev_b32 v144, 16, v152
v_and_b32 v145, 0xffff0000, v152
v_mfma_f32_16x16x32_bf16 v[72:75], a[120:123], v[24:27], v[72:75]
s_waitcnt lgkmcnt(0)
s_barrier
ds_read_b128 v[32:35], v4 offset:28672
v_mfma_f32_16x16x32_bf16 v[76:79], a[120:123], v[28:31], v[76:79]
buffer_load_dwordx4 a[120:123], v4, s[8:11], s63 offen offset:2048 sc1
ds_read_b128 v[36:39], v4 offset:29696
v_mfma_f32_16x16x32_bf16 v[80:83], a[124:127], v[16:19], v[80:83]
ds_read_b128 v[40:43], v4 offset:30720
ds_read_b128 v[44:47], v4 offset:31744
v_mfma_f32_16x16x32_bf16 v[84:87], a[124:127], v[20:23], v[84:87]
v_lshlrev_b32 v146, 16, v153
v_and_b32 v147, 0xffff0000, v153
v_mfma_f32_16x16x32_bf16 v[88:91], a[124:127], v[24:27], v[88:91]
v_lshlrev_b32 v148, 16, v154
v_and_b32 v149, 0xffff0000, v154
v_mfma_f32_16x16x32_bf16 v[92:95], a[124:127], v[28:31], v[92:95]
buffer_load_dwordx4 a[124:127], v4, s[8:11], s63 offen offset:3072 sc1
v_lshlrev_b32 v150, 16, v155
v_mfma_f32_16x16x32_bf16 v[96:99], a[128:131], v[16:19], v[96:99]
v_and_b32 v151, 0xffff0000, v155
v_fma_f32 v144, v144, v8, v9
v_mfma_f32_16x16x32_bf16 v[100:103], a[128:131], v[20:23], v[100:103]
v_fma_f32 v145, v145, v8, v9
v_mfma_f32_16x16x32_bf16 v[104:107], a[128:131], v[24:27], v[104:107]
s_waitcnt vmcnt(38)
ds_read_b128 v[156:159], v10 offset:45056
v_fma_f32 v146, v146, v8, v9
v_mfma_f32_16x16x32_bf16 v[108:111], a[128:131], v[28:31], v[108:111]
s_mov_b32 s63, 110592
buffer_load_dwordx4 a[128:131], v4, s[8:11], s63 offen sc1
v_fma_f32 v147, v147, v8, v9
v_mfma_f32_16x16x32_bf16 v[112:115], a[132:135], v[16:19], v[112:115]
v_fma_f32 v148, v148, v8, v9
v_fma_f32 v149, v149, v8, v9
v_mfma_f32_16x16x32_bf16 v[116:119], a[132:135], v[20:23], v[116:119]
v_fma_f32 v150, v150, v8, v9
v_fma_f32 v151, v151, v8, v9
v_mfma_f32_16x16x32_bf16 v[120:123], a[132:135], v[24:27], v[120:123]
v_cvt_pk_bf16_f32 v152, v144, v145
v_cvt_pk_bf16_f32 v153, v146, v147
v_mfma_f32_16x16x32_bf16 v[124:127], a[132:135], v[28:31], v[124:127]
buffer_load_dwordx4 a[132:135], v4, s[8:11], s63 offen offset:1024 sc1
v_cvt_pk_bf16_f32 v154, v148, v149
v_mfma_f32_16x16x32_bf16 v[128:131], a[136:139], v[16:19], v[128:131]
v_cvt_pk_bf16_f32 v155, v150, v151
v_mfma_f32_16x16x32_bf16 v[132:135], a[136:139], v[20:23], v[132:135]
ds_write_b128 v10, v[152:155] offset:40960
v_mfma_f32_16x16x32_bf16 v[136:139], a[136:139], v[24:27], v[136:139]
v_mfma_f32_16x16x32_bf16 v[140:143], a[136:139], v[28:31], v[140:143]
s_waitcnt lgkmcnt(2)
buffer_load_dwordx4 a[136:139], v4, s[8:11], s63 offen offset:2048 sc1
v_mfma_f32_16x16x32_bf16 v[64:67], a[140:143], v[32:35], v[64:67]
ds_read_b128 v[48:51], v4 offset:32768
ds_read_b128 v[52:55], v4 offset:33792
v_mfma_f32_16x16x32_bf16 v[68:71], a[140:143], v[36:39], v[68:71]
ds_read_b128 v[56:59], v4 offset:34816
ds_read_b128 v[60:63], v4 offset:35840
v_mfma_f32_16x16x32_bf16 v[72:75], a[140:143], v[40:43], v[72:75]
s_waitcnt lgkmcnt(5)
v_max3_u32 v12, v12, v156, v157
v_max3_u32 v12, v12, v158, v159
v_mfma_f32_16x16x32_bf16 v[76:79], a[140:143], v[44:47], v[76:79]
buffer_load_dwordx4 a[140:143], v4, s[8:11], s63 offen offset:3072 sc1
v_lshlrev_b32 v144, 16, v156
v_mfma_f32_16x16x32_bf16 v[80:83], a[144:147], v[32:35], v[80:83]
v_and_b32 v145, 0xffff0000, v156
v_lshlrev_b32 v146, 16, v157
v_mfma_f32_16x16x32_bf16 v[84:87], a[144:147], v[36:39], v[84:87]
v_and_b32 v147, 0xffff0000, v157
v_lshlrev_b32 v148, 16, v158
v_mfma_f32_16x16x32_bf16 v[88:91], a[144:147], v[40:43], v[88:91]
v_and_b32 v149, 0xffff0000, v158
v_lshlrev_b32 v150, 16, v159
v_mfma_f32_16x16x32_bf16 v[92:95], a[144:147], v[44:47], v[92:95]
s_mov_b32 s63, 114688
buffer_load_dwordx4 a[144:147], v4, s[8:11], s63 offen sc1
v_and_b32 v151, 0xffff0000, v159
v_mfma_f32_16x16x32_bf16 v[96:99], a[148:151], v[32:35], v[96:99]
v_fma_f32 v144, v144, v8, v9
v_fma_f32 v145, v145, v8, v9
v_mfma_f32_16x16x32_bf16 v[100:103], a[148:151], v[36:39], v[100:103]
s_waitcnt vmcnt(42)
ds_read_b128 v[152:155], v10 offset:49152
v_fma_f32 v146, v146, v8, v9
v_mfma_f32_16x16x32_bf16 v[104:107], a[148:151], v[40:43], v[104:107]
v_fma_f32 v147, v147, v8, v9
v_fma_f32 v148, v148, v8, v9
v_mfma_f32_16x16x32_bf16 v[108:111], a[148:151], v[44:47], v[108:111]
buffer_load_dwordx4 a[148:151], v4, s[8:11], s63 offen offset:1024 sc1
v_fma_f32 v149, v149, v8, v9
v_mfma_f32_16x16x32_bf16 v[112:115], a[152:155], v[32:35], v[112:115]
v_fma_f32 v150, v150, v8, v9
v_fma_f32 v151, v151, v8, v9
v_mfma_f32_16x16x32_bf16 v[116:119], a[152:155], v[36:39], v[116:119]
v_cvt_pk_bf16_f32 v156, v144, v145
v_cvt_pk_bf16_f32 v157, v146, v147
v_mfma_f32_16x16x32_bf16 v[120:123], a[152:155], v[40:43], v[120:123]
v_cvt_pk_bf16_f32 v158, v148, v149
v_cvt_pk_bf16_f32 v159, v150, v151
v_mfma_f32_16x16x32_bf16 v[124:127], a[152:155], v[44:47], v[124:127]
buffer_load_dwordx4 a[152:155], v4, s[8:11], s63 offen offset:2048 sc1
ds_write_b128 v10, v[156:159] offset:45056
v_mfma_f32_16x16x32_bf16 v[128:131], a[156:159], v[32:35], v[128:131]
v_mfma_f32_16x16x32_bf16 v[132:135], a[156:159], v[36:39], v[132:135]
v_mfma_f32_16x16x32_bf16 v[136:139], a[156:159], v[40:43], v[136:139]
v_mfma_f32_16x16x32_bf16 v[140:143], a[156:159], v[44:47], v[140:143]
s_waitcnt lgkmcnt(2)
buffer_load_dwordx4 a[156:159], v4, s[8:11], s63 offen offset:3072 sc1
v_mfma_f32_16x16x32_bf16 v[64:67], a[160:163], v[48:51], v[64:67]
ds_read_b128 v[16:19], v4 offset:36864
ds_read_b128 v[20:23], v4 offset:37888
v_mfma_f32_16x16x32_bf16 v[68:71], a[160:163], v[52:55], v[68:71]
ds_read_b128 v[24:27], v4 offset:38912
ds_read_b128 v[28:31], v4 offset:39936
v_mfma_f32_16x16x32_bf16 v[72:75], a[160:163], v[56:59], v[72:75]
s_waitcnt lgkmcnt(5)
v_max3_u32 v12, v12, v152, v153
v_max3_u32 v12, v12, v154, v155
v_mfma_f32_16x16x32_bf16 v[76:79], a[160:163], v[60:63], v[76:79]
s_mov_b32 s63, 118784
buffer_load_dwordx4 a[160:163], v4, s[8:11], s63 offen sc1
v_lshlrev_b32 v144, 16, v152
v_mfma_f32_16x16x32_bf16 v[80:83], a[164:167], v[48:51], v[80:83]
v_and_b32 v145, 0xffff0000, v152
v_lshlrev_b32 v146, 16, v153
v_mfma_f32_16x16x32_bf16 v[84:87], a[164:167], v[52:55], v[84:87]
v_and_b32 v147, 0xffff0000, v153
v_lshlrev_b32 v148, 16, v154
v_mfma_f32_16x16x32_bf16 v[88:91], a[164:167], v[56:59], v[88:91]
v_and_b32 v149, 0xffff0000, v154
v_lshlrev_b32 v150, 16, v155
v_mfma_f32_16x16x32_bf16 v[92:95], a[164:167], v[60:63], v[92:95]
buffer_load_dwordx4 a[164:167], v4, s[8:11], s63 offen offset:1024 sc1
v_and_b32 v151, 0xffff0000, v155
v_mfma_f32_16x16x32_bf16 v[96:99], a[168:171], v[48:51], v[96:99]
v_fma_f32 v144, v144, v8, v9
v_fma_f32 v145, v145, v8, v9
v_mfma_f32_16x16x32_bf16 v[100:103], a[168:171], v[52:55], v[100:103]
s_waitcnt vmcnt(46)
ds_read_b128 v[156:159], v10 offset:53248
v_fma_f32 v146, v146, v8, v9
v_mfma_f32_16x16x32_bf16 v[104:107], a[168:171], v[56:59], v[104:107]
v_fma_f32 v147, v147, v8, v9
v_fma_f32 v148, v148, v8, v9
v_mfma_f32_16x16x32_bf16 v[108:111], a[168:171], v[60:63], v[108:111]
buffer_load_dwordx4 a[168:171], v4, s[8:11], s63 offen offset:2048 sc1
v_fma_f32 v149, v149, v8, v9
v_mfma_f32_16x16x32_bf16 v[112:115], a[172:175], v[48:51], v[112:115]
v_fma_f32 v150, v150, v8, v9
v_fma_f32 v151, v151, v8, v9
v_mfma_f32_16x16x32_bf16 v[116:119], a[172:175], v[52:55], v[116:119]
v_cvt_pk_bf16_f32 v152, v144, v145
v_cvt_pk_bf16_f32 v153, v146, v147
v_mfma_f32_16x16x32_bf16 v[120:123], a[172:175], v[56:59], v[120:123]
v_cvt_pk_bf16_f32 v154, v148, v149
v_cvt_pk_bf16_f32 v155, v150, v151
v_mfma_f32_16x16x32_bf16 v[124:127], a[172:175], v[60:63], v[124:127]
buffer_load_dwordx4 a[172:175], v4, s[8:11], s63 offen offset:3072 sc1
ds_write_b128 v10, v[152:155] offset:49152
v_mfma_f32_16x16x32_bf16 v[128:131], a[176:179], v[48:51], v[128:131]
v_cmp_eq_u32 vcc, -1, v12
s_nop 0
s_cmp_eq_u64 vcc, 0
s_cbranch_scc0 Lx_54_%=
Lr_55_%=:
v_mfma_f32_16x16x32_bf16 v[132:135], a[176:179], v[52:55], v[132:135]
v_mfma_f32_16x16x32_bf16 v[136:139], a[176:179], v[56:59], v[136:139]
v_mfma_f32_16x16x32_bf16 v[140:143], a[176:179], v[60:63], v[140:143]
s_waitcnt lgkmcnt(2)
s_mov_b32 s63, 122880
buffer_load_dwordx4 a[176:179], v4, s[8:11], s63 offen sc1
v_mfma_f32_16x16x32_bf16 v[64:67], a[180:183], v[16:19], v[64:67]
s_waitcnt lgkmcnt(1)
v_max_u32 v12, v156, v157
v_max3_u32 v12, v12, v158, v159
v_mfma_f32_16x16x32_bf16 v[68:71], a[180:183], v[20:23], v[68:71]
v_lshlrev_b32 v144, 16, v156
v_and_b32 v145, 0xffff0000, v156
v_mfma_f32_16x16x32_bf16 v[72:75], a[180:183], v[24:27], v[72:75]
s_waitcnt lgkmcnt(0)
s_barrier
ds_read_b128 v[32:35], v4 offset:40960
v_mfma_f32_16x16x32_bf16 v[76:79], a[180:183], v[28:31], v[76:79]
)"
R"(buffer_load_dwordx4 a[180:183], v4, s[8:11], s63 offen offset:1024 sc1
ds_read_b128 v[36:39], v4 offset:41984
v_mfma_f32_16x16x32_bf16 v[80:83], a[184:187], v[16:19], v[80:83]
ds_read_b128 v[40:43], v4 offset:43008
ds_read_b128 v[44:47], v4 offset:44032
v_mfma_f32_16x16x32_bf16 v[84:87], a[184:187], v[20:23], v[84:87]
v_lshlrev_b32 v146, 16, v157
v_and_b32 v147, 0xffff0000, v157
v_mfma_f32_16x16x32_bf16 v[88:91], a[184:187], v[24:27], v[88:91]
v_lshlrev_b32 v148, 16, v158
v_and_b32 v149, 0xffff0000, v158
v_mfma_f32_16x16x32_bf16 v[92:95], a[184:187], v[28:31], v[92:95]
buffer_load_dwordx4 a[184:187], v4, s[8:11], s63 offen offset:2048 sc1
v_lshlrev_b32 v150, 16, v159
v_mfma_f32_16x16x32_bf16 v[96:99], a[188:191], v[16:19], v[96:99]
v_and_b32 v151, 0xffff0000, v159
v_fma_f32 v144, v144, v8, v9
v_mfma_f32_16x16x32_bf16 v[100:103], a[188:191], v[20:23], v[100:103]
v_fma_f32 v145, v145, v8, v9
v_mfma_f32_16x16x32_bf16 v[104:107], a[188:191], v[24:27], v[104:107]
s_waitcnt vmcnt(49)
ds_read_b128 v[152:155], v10 offset:57344
v_fma_f32 v146, v146, v8, v9
v_mfma_f32_16x16x32_bf16 v[108:111], a[188:191], v[28:31], v[108:111]
buffer_load_dwordx4 a[188:191], v4, s[8:11], s63 offen offset:3072 sc1
v_fma_f32 v147, v147, v8, v9
v_mfma_f32_16x16x32_bf16 v[112:115], a[192:195], v[16:19], v[112:115]
v_fma_f32 v148, v148, v8, v9
v_fma_f32 v149, v149, v8, v9
v_mfma_f32_16x16x32_bf16 v[116:119], a[192:195], v[20:23], v[116:119]
v_fma_f32 v150, v150, v8, v9
v_fma_f32 v151, v151, v8, v9
v_mfma_f32_16x16x32_bf16 v[120:123], a[192:195], v[24:27], v[120:123]
v_cvt_pk_bf16_f32 v156, v144, v145
v_cvt_pk_bf16_f32 v157, v146, v147
v_mfma_f32_16x16x32_bf16 v[124:127], a[192:195], v[28:31], v[124:127]
s_mov_b32 s63, 126976
buffer_load_dwordx4 a[192:195], v4, s[8:11], s63 offen sc1
v_cvt_pk_bf16_f32 v158, v148, v149
v_mfma_f32_16x16x32_bf16 v[128:131], a[196:199], v[16:19], v[128:131]
v_cvt_pk_bf16_f32 v159, v150, v151
v_mfma_f32_16x16x32_bf16 v[132:135], a[196:199], v[20:23], v[132:135]
ds_write_b128 v10, v[156:159] offset:53248
v_mfma_f32_16x16x32_bf16 v[136:139], a[196:199], v[24:27], v[136:139]
v_mfma_f32_16x16x32_bf16 v[140:143], a[196:199], v[28:31], v[140:143]
s_waitcnt lgkmcnt(2)
buffer_load_dwordx4 a[196:199], v4, s[8:11], s63 offen offset:1024 sc1
v_mfma_f32_16x16x32_bf16 v[64:67], a[200:203], v[32:35], v[64:67]
ds_read_b128 v[48:51], v4 offset:45056
ds_read_b128 v[52:55], v4 offset:46080
v_mfma_f32_16x16x32_bf16 v[68:71], a[200:203], v[36:39], v[68:71]
ds_read_b128 v[56:59], v4 offset:47104
ds_read_b128 v[60:63], v4 offset:48128
v_mfma_f32_16x16x32_bf16 v[72:75], a[200:203], v[40:43], v[72:75]
s_waitcnt lgkmcnt(5)
v_max3_u32 v12, v12, v152, v153
v_max3_u32 v12, v12, v154, v155
v_mfma_f32_16x16x32_bf16 v[76:79], a[200:203], v[44:47], v[76:79]
buffer_load_dwordx4 a[200:203], v4, s[8:11], s63 offen offset:2048 sc1
v_lshlrev_b32 v144, 16, v152
v_mfma_f32_16x16x32_bf16 v[80:83], a[204:207], v[32:35], v[80:83]
v_and_b32 v145, 0xffff0000, v152
v_lshlrev_b32 v146, 16, v153
v_mfma_f32_16x16x32_bf16 v[84:87], a[204:207], v[36:39], v[84:87]
v_and_b32 v147, 0xffff0000, v153
v_lshlrev_b32 v148, 16, v154
v_mfma_f32_16x16x32_bf16 v[88:91], a[204:207], v[40:43], v[88:91]
v_and_b32 v149, 0xffff0000, v154
v_lshlrev_b32 v150, 16, v155
v_mfma_f32_16x16x32_bf16 v[92:95], a[204:207], v[44:47], v[92:95]
buffer_load_dwordx4 a[204:207], v4, s[8:11], s63 offen offset:3072 sc1
v_and_b32 v151, 0xffff0000, v155
v_mfma_f32_16x16x32_bf16 v[96:99], a[208:211], v[32:35], v[96:99]
v_fma_f32 v144, v144, v8, v9
v_fma_f32 v145, v145, v8, v9
v_mfma_f32_16x16x32_bf16 v[100:103], a[208:211], v[36:39], v[100:103]
s_waitcnt vmcnt(48)
ds_read_b128 v[156:159], v10 offset:61440
v_fma_f32 v146, v146, v8, v9
v_mfma_f32_16x16x32_bf16 v[104:107], a[208:211], v[40:43], v[104:107]
v_fma_f32 v147, v147, v8, v9
v_fma_f32 v148, v148, v8, v9
v_mfma_f32_16x16x32_bf16 v[108:111], a[208:211], v[44:47], v[108:111]
s_mov_b32 s63, 131072
buffer_load_dwordx4 a[208:211], v4, s[8:11], s63 offen sc1
v_fma_f32 v149, v149, v8, v9
v_mfma_f32_16x16x32_bf16 v[112:115], a[212:215], v[32:35], v[112:115]
LW1_2_%=:
s_cbranch_execz LW1_6_%=
v_fma_f32 v150, v150, v8, v9
v_fma_f32 v151, v151, v8, v9
v_mfma_f32_16x16x32_bf16 v[116:119], a[212:215], v[36:39], v[116:119]
v_cvt_pk_bf16_f32 v152, v144, v145
v_cvt_pk_bf16_f32 v153, v146, v147
v_mfma_f32_16x16x32_bf16 v[120:123], a[212:215], v[40:43], v[120:123]
v_cvt_pk_bf16_f32 v154, v148, v149
v_cvt_pk_bf16_f32 v155, v150, v151
v_mfma_f32_16x16x32_bf16 v[124:127], a[212:215], v[44:47], v[124:127]
buffer_load_dwordx4 a[212:215], v4, s[8:11], s63 offen offset:1024 sc1
ds_write_b128 v10, v[152:155] offset:57344
v_mfma_f32_16x16x32_bf16 v[128:131], a[216:219], v[32:35], v[128:131]
v_mfma_f32_16x16x32_bf16 v[132:135], a[216:219], v[36:39], v[132:135]
v_mfma_f32_16x16x32_bf16 v[136:139], a[216:219], v[40:43], v[136:139]
v_mfma_f32_16x16x32_bf16 v[140:143], a[216:219], v[44:47], v[140:143]
s_waitcnt lgkmcnt(2)
buffer_load_dwordx4 a[216:219], v4, s[8:11], s63 offen offset:2048 sc1
v_mfma_f32_16x16x32_bf16 v[64:67], a[220:223], v[48:51], v[64:67]
ds_read_b128 v[16:19], v5
ds_read_b128 v[20:23], v5 offset:1024
v_mfma_f32_16x16x32_bf16 v[68:71], a[220:223], v[52:55], v[68:71]
ds_read_b128 v[24:27], v5 offset:2048
ds_read_b128 v[28:31], v5 offset:3072
v_mfma_f32_16x16x32_bf16 v[72:75], a[220:223], v[56:59], v[72:75]
s_waitcnt lgkmcnt(5)
v_max3_u32 v12, v12, v156, v157
v_max3_u32 v12, v12, v158, v159
v_mfma_f32_16x16x32_bf16 v[76:79], a[220:223], v[60:63], v[76:79]
buffer_load_dwordx4 a[220:223], v4, s[8:11], s63 offen offset:3072 sc1
v_lshlrev_b32 v144, 16, v156
v_mfma_f32_16x16x32_bf16 v[80:83], a[224:227], v[48:51], v[80:83]
v_and_b32 v145, 0xffff0000, v156
v_lshlrev_b32 v146, 16, v157
v_mfma_f32_16x16x32_bf16 v[84:87], a[224:227], v[52:55], v[84:87]
v_and_b32 v147, 0xffff0000, v157
v_lshlrev_b32 v148, 16, v158
v_mfma_f32_16x16x32_bf16 v[88:91], a[224:227], v[56:59], v[88:91]
v_and_b32 v149, 0xffff0000, v158
v_lshlrev_b32 v150, 16, v159
v_mfma_f32_16x16x32_bf16 v[92:95], a[224:227], v[60:63], v[92:95]
s_mov_b32 s63, 135168
buffer_load_dwordx4 a[224:227], v4, s[8:11], s63 offen sc1
v_and_b32 v151, 0xffff0000, v159
v_mfma_f32_16x16x32_bf16 v[96:99], a[228:231], v[48:51], v[96:99]
v_fma_f32 v144, v144, v8, v9
v_fma_f32 v145, v145, v8, v9
v_mfma_f32_16x16x32_bf16 v[100:103], a[228:231], v[52:55], v[100:103]
s_waitcnt vmcnt(47)
ds_read_b128 v[152:155], v11
v_fma_f32 v146, v146, v8, v9
v_mfma_f32_16x16x32_bf16 v[104:107], a[228:231], v[56:59], v[104:107]
v_fma_f32 v147, v147, v8, v9
v_fma_f32 v148, v148, v8, v9
v_mfma_f32_16x16x32_bf16 v[108:111], a[228:231], v[60:63], v[108:111]
buffer_load_dwordx4 a[228:231], v4, s[8:11], s63 offen offset:1024 sc1
v_fma_f32 v149, v149, v8, v9
v_mfma_f32_16x16x32_bf16 v[112:115], a[232:235], v[48:51], v[112:115]
v_fma_f32 v150, v150, v8, v9
v_fma_f32 v151, v151, v8, v9
v_mfma_f32_16x16x32_bf16 v[116:119], a[232:235], v[52:55], v[116:119]
v_cvt_pk_bf16_f32 v156, v144, v145
v_cvt_pk_bf16_f32 v157, v146, v147
v_mfma_f32_16x16x32_bf16 v[120:123], a[232:235], v[56:59], v[120:123]
v_cvt_pk_bf16_f32 v158, v148, v149
v_cvt_pk_bf16_f32 v159, v150, v151
v_mfma_f32_16x16x32_bf16 v[124:127], a[232:235], v[60:63], v[124:127]
buffer_load_dwordx4 a[232:235], v4, s[8:11], s63 offen offset:2048 sc1
ds_write_b128 v10, v[156:159] offset:61440
v_mfma_f32_16x16x32_bf16 v[128:131], a[236:239], v[48:51], v[128:131]
v_cmp_eq_u32 vcc, -1, v12
s_nop 0
s_cmp_eq_u64 vcc, 0
s_cbranch_scc0 Lx_58_%=
Lr_59_%=:
v_mfma_f32_16x16x32_bf16 v[132:135], a[236:239], v[52:55], v[132:135]
v_mfma_f32_16x16x32_bf16 v[136:139], a[236:239], v[56:59], v[136:139]
v_mfma_f32_16x16x32_bf16 v[140:143], a[236:239], v[60:63], v[140:143]
s_waitcnt lgkmcnt(2)
buffer_load_dwordx4 a[236:239], v4, s[8:11], s63 offen offset:3072 sc1
v_mfma_f32_16x16x32_bf16 v[64:67], a[240:243], v[16:19], v[64:67]
s_waitcnt lgkmcnt(1)
v_max_u32 v12, v152, v153
v_max3_u32 v12, v12, v154, v155
v_mfma_f32_16x16x32_bf16 v[68:71], a[240:243], v[20:23], v[68:71]
v_lshlrev_b32 v144, 16, v152
v_and_b32 v145, 0xffff0000, v152
v_mfma_f32_16x16x32_bf16 v[72:75], a[240:243], v[24:27], v[72:75]
s_waitcnt lgkmcnt(0)
s_barrier
ds_read_b128 v[32:35], v5 offset:4096
v_mfma_f32_16x16x32_bf16 v[76:79], a[240:243], v[28:31], v[76:79]
s_mov_b32 s63, 139264
buffer_load_dwordx4 a[240:243], v4, s[8:11], s63 offen sc1
ds_read_b128 v[36:39], v5 offset:5120
v_mfma_f32_16x16x32_bf16 v[80:83], a[244:247], v[16:19], v[80:83]
ds_read_b128 v[40:43], v5 offset:6144
ds_read_b128 v[44:47], v5 offset:7168
v_mfma_f32_16x16x32_bf16 v[84:87], a[244:247], v[20:23], v[84:87]
v_lshlrev_b32 v146, 16, v153
v_and_b32 v147, 0xffff0000, v153
v_mfma_f32_16x16x32_bf16 v[88:91], a[244:247], v[24:27], v[88:91]
v_lshlrev_b32 v148, 16, v154
v_and_b32 v149, 0xffff0000, v154
v_mfma_f32_16x16x32_bf16 v[92:95], a[244:247], v[28:31], v[92:95]
buffer_load_dwordx4 a[244:247], v4, s[8:11], s63 offen offset:1024 sc1
v_lshlrev_b32 v150, 16, v155
v_mfma_f32_16x16x32_bf16 v[96:99], a[248:251], v[16:19], v[96:99]
v_and_b32 v151, 0xffff0000, v155
v_fma_f32 v144, v144, v8, v9
v_mfma_f32_16x16x32_bf16 v[100:103], a[248:251], v[20:23], v[100:103]
v_fma_f32 v145, v145, v8, v9
v_mfma_f32_16x16x32_bf16 v[104:107], a[248:251], v[24:27], v[104:107]
s_waitcnt vmcnt(46)
ds_read_b128 v[156:159], v11 offset:4096
v_fma_f32 v146, v146, v8, v9
v_mfma_f32_16x16x32_bf16 v[108:111], a[248:251], v[28:31], v[108:111]
buffer_load_dwordx4 a[248:251], v4, s[8:11], s63 offen offset:2048 sc1
v_fma_f32 v147, v147, v8, v9
v_mfma_f32_16x16x32_bf16 v[112:115], a[252:255], v[16:19], v[112:115]
v_fma_f32 v148, v148, v8, v9
v_fma_f32 v149, v149, v8, v9
v_mfma_f32_16x16x32_bf16 v[116:119], a[252:255], v[20:23], v[116:119]
v_fma_f32 v150, v150, v8, v9
v_fma_f32 v151, v151, v8, v9
v_mfma_f32_16x16x32_bf16 v[120:123], a[252:255], v[24:27], v[120:123]
v_cvt_pk_bf16_f32 v152, v144, v145
v_cvt_pk_bf16_f32 v153, v146, v147
v_mfma_f32_16x16x32_bf16 v[124:127], a[252:255], v[28:31], v[124:127]
buffer_load_dwordx4 a[252:255], v4, s[8:11], s63 offen offset:3072 sc1
v_cvt_pk_bf16_f32 v154, v148, v149
v_mfma_f32_16x16x32_bf16 v[128:131], v[224:227], v[16:19], v[128:131]
v_cvt_pk_bf16_f32 v155, v150, v151
v_mfma_f32_16x16x32_bf16 v[132:135], v[224:227], v[20:23], v[132:135]
ds_write_b128 v11, v[152:155]
v_mfma_f32_16x16x32_bf16 v[136:139], v[224:227], v[24:27], v[136:139]
v_mfma_f32_16x16x32_bf16 v[140:143], v[224:227], v[28:31], v[140:143]
s_waitcnt lgkmcnt(2)
s_mov_b32 s63, 143360
buffer_load_dwordx4 v[224:227], v4, s[8:11], s63 offen sc1
v_mfma_f32_16x16x32_bf16 v[64:67], v[228:231], v[32:35], v[64:67]
ds_read_b128 v[48:51], v5 offset:8192
ds_read_b128 v[52:55], v5 offset:9216
v_mfma_f32_16x16x32_bf16 v[68:71], v[228:231], v[36:39], v[68:71]
ds_read_b128 v[56:59], v5 offset:10240
ds_read_b128 v[60:63], v5 offset:11264
v_mfma_f32_16x16x32_bf16 v[72:75], v[228:231], v[40:43], v[72:75]
s_waitcnt lgkmcnt(5)
v_max3_u32 v12, v12, v156, v157
v_max3_u32 v12, v12, v158, v159
v_mfma_f32_16x16x32_bf16 v[76:79], v[228:231], v[44:47], v[76:79]
buffer_load_dwordx4 v[228:231], v4, s[8:11], s63 offen offset:1024 sc1
v_lshlrev_b32 v144, 16, v156
v_mfma_f32_16x16x32_bf16 v[80:83], v[232:235], v[32:35], v[80:83]
v_and_b32 v145, 0xffff0000, v156
v_lshlrev_b32 v146, 16, v157
v_mfma_f32_16x16x32_bf16 v[84:87], v[232:235], v[36:39], v[84:87]
v_and_b32 v147, 0xffff0000, v157
v_lshlrev_b32 v148, 16, v158
v_mfma_f32_16x16x32_bf16 v[88:91], v[232:235], v[40:43], v[88:91]
v_and_b32 v149, 0xffff0000, v158
v_lshlrev_b32 v150, 16, v159
v_mfma_f32_16x16x32_bf16 v[92:95], v[232:235], v[44:47], v[92:95]
buffer_load_dwordx4 v[232:235], v4, s[8:11], s63 offen offset:2048 sc1
v_and_b32 v151, 0xffff0000, v159
v_mfma_f32_16x16x32_bf16 v[96:99], v[236:239], v[32:35], v[96:99]
v_fma_f32 v144, v144, v8, v9
v_fma_f32 v145, v145, v8, v9
v_mfma_f32_16x16x32_bf16 v[100:103], v[236:239], v[36:39], v[100:103]
v_fma_f32 v146, v146, v8, v9
v_fma_f32 v147, v147, v8, v9
v_mfma_f32_16x16x32_bf16 v[104:107], v[236:239], v[40:43], v[104:107]
v_fma_f32 v148, v148, v8, v9
v_fma_f32 v149, v149, v8, v9
v_mfma_f32_16x16x32_bf16 v[108:111], v[236:239], v[44:47], v[108:111]
buffer_load_dwordx4 v[236:239], v4, s[8:11], s63 offen offset:3072 sc1
v_fma_f32 v150, v150, v8, v9
v_mfma_f32_16x16x32_bf16 v[112:115], v[240:243], v[32:35], v[112:115]
v_fma_f32 v151, v151, v8, v9
v_cvt_pk_bf16_f32 v156, v144, v145
v_mfma_f32_16x16x32_bf16 v[116:119], v[240:243], v[36:39], v[116:119]
v_cvt_pk_bf16_f32 v157, v146, v147
v_cvt_pk_bf16_f32 v158, v148, v149
v_mfma_f32_16x16x32_bf16 v[120:123], v[240:243], v[40:43], v[120:123]
v_cvt_pk_bf16_f32 v159, v150, v151
v_mfma_f32_16x16x32_bf16 v[124:127], v[240:243], v[44:47], v[124:127]
s_mov_b32 s63, 147456
buffer_load_dwordx4 v[240:243], v4, s[8:11], s63 offen sc1
ds_write_b128 v11, v[156:159] offset:4096
v_mfma_f32_16x16x32_bf16 v[128:131], v[244:247], v[32:35], v[128:131]
v_cmp_eq_u32 vcc, -1, v12
s_nop 0
s_cmp_eq_u64 vcc, 0
s_cbranch_scc0 Lx_62_%=
Lr_63_%=:
v_mfma_f32_16x16x32_bf16 v[132:135], v[244:247], v[36:39], v[132:135]
v_mfma_f32_16x16x32_bf16 v[136:139], v[244:247], v[40:43], v[136:139]
v_mfma_f32_16x16x32_bf16 v[140:143], v[244:247], v[44:47], v[140:143]
s_waitcnt lgkmcnt(1)
buffer_load_dwordx4 v[244:247], v4, s[8:11], s63 offen offset:1024 sc1
v_mfma_f32_16x16x32_bf16 v[64:67], v[248:251], v[48:51], v[64:67]
ds_read_b64 v[200:201], v13 offset:640
v_mfma_f32_16x16x32_bf16 v[68:71], v[248:251], v[52:55], v[68:71]
ds_read_b64 v[202:203], v13 offset:768
v_mfma_f32_16x16x32_bf16 v[72:75], v[248:251], v[56:59], v[72:75]
s_waitcnt lgkmcnt(2)
s_barrier
ds_read_b128 v[16:19], v5 offset:12288
v_mfma_f32_16x16x32_bf16 v[76:79], v[248:251], v[60:63], v[76:79]
buffer_load_dwordx4 v[248:251], v4, s[8:11], s63 offen offset:2048 sc1
ds_read_b128 v[20:23], v5 offset:13312
v_mfma_f32_16x16x32_bf16 v[80:83], v[252:255], v[48:51], v[80:83]
ds_read_b128 v[24:27], v5 offset:14336
ds_read_b128 v[28:31], v5 offset:15360
v_mfma_f32_16x16x32_bf16 v[84:87], v[252:255], v[52:55], v[84:87]
v_mfma_f32_16x16x32_bf16 v[88:91], v[252:255], v[56:59], v[88:91]
v_mfma_f32_16x16x32_bf16 v[92:95], v[252:255], v[60:63], v[92:95]
buffer_load_dwordx4 v[252:255], v4, s[8:11], s63 offen offset:3072 sc1
s_waitcnt lgkmcnt(5)
v_lshlrev_b32 v144, 16, v200
v_mfma_f32_16x16x32_bf16 v[96:99], v[208:211], v[48:51], v[96:99]
v_and_b32 v145, 0xffff0000, v200
v_lshlrev_b32 v146, 16, v201
v_mfma_f32_16x16x32_bf16 v[100:103], v[208:211], v[52:55], v[100:103]
v_and_b32 v147, 0xffff0000, v201
s_waitcnt lgkmcnt(4)
v_lshlrev_b32 v160, 16, v202
v_mfma_f32_16x16x32_bf16 v[104:107], v[208:211], v[56:59], v[104:107]
v_and_b32 v161, 0xffff0000, v202
v_lshlrev_b32 v162, 16, v203
v_mfma_f32_16x16x32_bf16 v[108:111], v[208:211], v[60:63], v[108:111]
s_mov_b32 s63, 151552
buffer_load_dwordx4 v[208:211], v4, s[8:11], s63 offen sc1
v_and_b32 v163, 0xffff0000, v203
v_mfma_f32_16x16x32_bf16 v[112:115], v[212:215], v[48:51], v[112:115]
v_and_b32 v11, 7, v3
v_mul_u32_u24 v10, 1152, v11
v_mfma_f32_16x16x32_bf16 v[116:119], v[212:215], v[52:55], v[116:119]
v_lshl_add_u32 v10, v2, 3, v10
v_add_u32 v10, s89, v10
v_mfma_f32_16x16x32_bf16 v[120:123], v[212:215], v[56:59], v[120:123]
v_lshlrev_b32 v11, 4, v11
v_mov_b32 v8, s64
v_mfma_f32_16x16x32_bf16 v[124:127], v[212:215], v[60:63], v[124:127]
buffer_load_dwordx4 v[212:215], v4, s[8:11], s63 offen offset:1024 sc1
s_mov_b32 exec_lo, 0xffff0000
s_mov_b32 exec_hi, -1
v_mov_b32 v8, s65
s_mov_b64 exec, -1
v_mfma_f32_16x16x32_bf16 v[128:131], v[216:219], v[48:51], v[128:131]
s_mov_b32 exec_lo, 0
s_mov_b32 exec_hi, -1
v_mov_b32 v8, s66
s_mov_b64 exec, -1
s_mov_b32 exec_lo, 0
s_mov_b32 exec_hi, 0xffff0000
v_mov_b32 v8, s67
s_mov_b64 exec, -1
v_mfma_f32_16x16x32_bf16 v[132:135], v[216:219], v[52:55], v[132:135]
v_mov_b32 v9, s68
s_mov_b32 exec_lo, 0xffff0000
s_mov_b32 exec_hi, -1
v_mov_b32 v9, s69
s_mov_b64 exec, -1
v_mfma_f32_16x16x32_bf16 v[136:139], v[216:219], v[56:59], v[136:139]
s_mov_b32 exec_lo, 0
s_mov_b32 exec_hi, -1
v_mov_b32 v9, s70
s_mov_b64 exec, -1
s_mov_b32 exec_lo, 0
s_mov_b32 exec_hi, 0xffff0000
v_mov_b32 v9, s71
s_mov_b64 exec, -1
v_mfma_f32_16x16x32_bf16 v[140:143], v[216:219], v[60:63], v[140:143]
s_waitcnt lgkmcnt(0)
buffer_load_dwordx4 v[216:219], v4, s[8:11], s63 offen offset:2048 sc1
v_lshl_add_u32 v8, v3, 4, v8
v_mfma_f32_16x16x32_bf16 v[64:67], v[220:223], v[16:19], v[64:67]
ds_read_b128 v[32:35], v5 offset:16384
ds_read_b128 v[36:39], v5 offset:17408
v_mfma_f32_16x16x32_bf16 v[68:71], v[220:223], v[20:23], v[68:71]
ds_read_b128 v[40:43], v5 offset:18432
ds_read_b128 v[44:47], v5 offset:19456
v_mfma_f32_16x16x32_bf16 v[72:75], v[220:223], v[24:27], v[72:75]
v_add_u32 v9, v9, v11
v_lshl_add_u32 v11, v2, 8, v11
v_mfma_f32_16x16x32_bf16 v[76:79], v[220:223], v[28:31], v[76:79]
buffer_load_dwordx4 v[220:223], v4, s[8:11], s63 offen offset:3072 sc1
v_lshlrev_b32 v190, 3, v2
v_mfma_f32_16x16x32_bf16 v[80:83], a[0:3], v[16:19], v[80:83]
s_lshl_b32 s48, s39, 5
v_add_u32 v190, s48, v190
v_mfma_f32_16x16x32_bf16 v[84:87], a[0:3], v[20:23], v[84:87]
s_lshl_b32 s48, s93, 7
s_add_u32 s48, s28, s48
s_addc_u32 s49, s29, 0
v_mfma_f32_16x16x32_bf16 v[88:91], a[0:3], v[24:27], v[88:91]
s_add_u32 s56, s50, s87
s_addc_u32 s57, s51, 0
s_add_u32 s52, s50, s90
s_addc_u32 s53, s51, 0
v_mfma_f32_16x16x32_bf16 v[92:95], a[0:3], v[28:31], v[92:95]
s_mov_b32 s63, 155648
buffer_load_dwordx4 a[0:3], v4, s[8:11], s63 offen sc1
s_and_b32 s53, s53, 0xffff
v_mfma_f32_16x16x32_bf16 v[96:99], a[4:7], v[16:19], v[96:99]
s_mov_b32 s54, 14745600
v_mul_u32_u24 v206, 144, v3
v_mfma_f32_16x16x32_bf16 v[100:103], a[4:7], v[20:23], v[100:103]
v_add_u32 v206, v206, v190
v_lshrrev_b32 v194, 3, v1
v_mfma_f32_16x16x32_bf16 v[104:107], a[4:7], v[24:27], v[104:107]
)"
R"(v_and_b32 v195, 7, v1
v_lshlrev_b32 v195, 4, v195
v_mfma_f32_16x16x32_bf16 v[108:111], a[4:7], v[28:31], v[108:111]
buffer_load_dwordx4 a[4:7], v4, s[8:11], s63 offen offset:1024 sc1
v_mul_u32_u24 v192, 144, v194
v_mfma_f32_16x16x32_bf16 v[112:115], a[8:11], v[16:19], v[112:115]
s_mul_i32 s60, s39, 2304
v_add3_u32 v192, v192, v195, s60
v_mfma_f32_16x16x32_bf16 v[116:119], a[8:11], v[20:23], v[116:119]
v_mul_u32_u24 v193, 1152, v194
v_add_u32 v193, v193, v195
v_mfma_f32_16x16x32_bf16 v[120:123], a[8:11], v[24:27], v[120:123]
s_add_u32 s60, s30, s92
s_addc_u32 s61, s31, 0
s_add_u32 s4, s60, 9216
s_addc_u32 s5, s61, 0
v_mfma_f32_16x16x32_bf16 v[124:127], a[8:11], v[28:31], v[124:127]
buffer_load_dwordx4 a[8:11], v4, s[8:11], s63 offen offset:2048 sc1
v_lshrrev_b32 v194, 3, v3
v_mfma_f32_16x16x32_bf16 v[128:131], a[12:15], v[16:19], v[128:131]
v_lshl_add_u32 v194, v194, 13, v11
v_add_u32 v194, s91, v194
v_mfma_f32_16x16x32_bf16 v[132:135], a[12:15], v[20:23], v[132:135]
v_mfma_f32_16x16x32_bf16 v[136:139], a[12:15], v[24:27], v[136:139]
v_mfma_f32_16x16x32_bf16 v[140:143], a[12:15], v[28:31], v[140:143]
s_waitcnt lgkmcnt(0)
buffer_load_dwordx4 a[12:15], v4, s[8:11], s63 offen offset:3072 sc1
v_mfma_f32_16x16x32_bf16 v[64:67], a[16:19], v[32:35], v[64:67]
ds_read_b128 v[48:51], v5 offset:20480
ds_read_b128 v[52:55], v5 offset:21504
v_mfma_f32_16x16x32_bf16 v[68:71], a[16:19], v[36:39], v[68:71]
ds_read_b128 v[56:59], v5 offset:22528
ds_read_b128 v[60:63], v5 offset:23552
v_mfma_f32_16x16x32_bf16 v[72:75], a[16:19], v[40:43], v[72:75]
ds_read_b128 v[16:19], v4
ds_read_b128 v[20:23], v4 offset:1024
v_mfma_f32_16x16x32_bf16 v[76:79], a[16:19], v[44:47], v[76:79]
s_mov_b32 s63, 159744
buffer_load_dwordx4 a[16:19], v4, s[8:11], s63 offen sc1
ds_read_b128 v[24:27], v4 offset:2048
v_mfma_f32_16x16x32_bf16 v[80:83], a[20:23], v[32:35], v[80:83]
ds_read_b128 v[28:31], v4 offset:3072
v_mfma_f32_16x16x32_bf16 v[84:87], a[20:23], v[36:39], v[84:87]
v_mfma_f32_16x16x32_bf16 v[88:91], a[20:23], v[40:43], v[88:91]
v_mfma_f32_16x16x32_bf16 v[92:95], a[20:23], v[44:47], v[92:95]
buffer_load_dwordx4 a[20:23], v4, s[8:11], s63 offen offset:1024 sc1
v_mfma_f32_16x16x32_bf16 v[96:99], a[24:27], v[32:35], v[96:99]
v_mfma_f32_16x16x32_bf16 v[100:103], a[24:27], v[36:39], v[100:103]
v_mfma_f32_16x16x32_bf16 v[104:107], a[24:27], v[40:43], v[104:107]
v_mfma_f32_16x16x32_bf16 v[108:111], a[24:27], v[44:47], v[108:111]
buffer_load_dwordx4 a[24:27], v4, s[8:11], s63 offen offset:2048 sc1
v_mfma_f32_16x16x32_bf16 v[112:115], a[28:31], v[32:35], v[112:115]
v_mfma_f32_16x16x32_bf16 v[116:119], a[28:31], v[36:39], v[116:119]
v_mfma_f32_16x16x32_bf16 v[120:123], a[28:31], v[40:43], v[120:123]
v_mfma_f32_16x16x32_bf16 v[124:127], a[28:31], v[44:47], v[124:127]
buffer_load_dwordx4 a[28:31], v4, s[8:11], s63 offen offset:3072 sc1
v_mfma_f32_16x16x32_bf16 v[128:131], a[32:35], v[32:35], v[128:131]
v_mfma_f32_16x16x32_bf16 v[132:135], a[32:35], v[36:39], v[132:135]
v_mfma_f32_16x16x32_bf16 v[136:139], a[32:35], v[40:43], v[136:139]
v_mfma_f32_16x16x32_bf16 v[140:143], a[32:35], v[44:47], v[140:143]
s_waitcnt lgkmcnt(4)
s_mov_b32 s63, 163840
buffer_load_dwordx4 a[32:35], v4, s[8:11], s63 offen sc1
v_mfma_f32_16x16x32_bf16 v[64:67], a[36:39], v[48:51], v[64:67]
ds_read_b128 v[32:35], v4 offset:4096
ds_read_b128 v[36:39], v4 offset:5120
v_mfma_f32_16x16x32_bf16 v[68:71], a[36:39], v[52:55], v[68:71]
ds_read_b128 v[40:43], v4 offset:6144
ds_read_b128 v[44:47], v4 offset:7168
v_mfma_f32_16x16x32_bf16 v[72:75], a[36:39], v[56:59], v[72:75]
v_mfma_f32_16x16x32_bf16 v[76:79], a[36:39], v[60:63], v[76:79]
buffer_load_dwordx4 a[36:39], v4, s[8:11], s63 offen offset:1024 sc1
v_mfma_f32_16x16x32_bf16 v[80:83], a[40:43], v[48:51], v[80:83]
v_mfma_f32_16x16x32_bf16 v[84:87], a[40:43], v[52:55], v[84:87]
v_mfma_f32_16x16x32_bf16 v[88:91], a[40:43], v[56:59], v[88:91]
v_mfma_f32_16x16x32_bf16 v[92:95], a[40:43], v[60:63], v[92:95]
buffer_load_dwordx4 a[40:43], v4, s[8:11], s63 offen offset:2048 sc1
v_mfma_f32_16x16x32_bf16 v[96:99], a[44:47], v[48:51], v[96:99]
v_mfma_f32_16x16x32_bf16 v[100:103], a[44:47], v[52:55], v[100:103]
v_mfma_f32_16x16x32_bf16 v[104:107], a[44:47], v[56:59], v[104:107]
v_mfma_f32_16x16x32_bf16 v[108:111], a[44:47], v[60:63], v[108:111]
buffer_load_dwordx4 a[44:47], v4, s[8:11], s63 offen offset:3072 sc1
v_mfma_f32_16x16x32_bf16 v[112:115], a[48:51], v[48:51], v[112:115]
v_mfma_f32_16x16x32_bf16 v[116:119], a[48:51], v[52:55], v[116:119]
v_mfma_f32_16x16x32_bf16 v[120:123], a[48:51], v[56:59], v[120:123]
v_mfma_f32_16x16x32_bf16 v[124:127], a[48:51], v[60:63], v[124:127]
s_mov_b32 s63, 167936
buffer_load_dwordx4 a[48:51], v4, s[8:11], s63 offen sc1
v_mfma_f32_16x16x32_bf16 v[128:131], a[52:55], v[48:51], v[128:131]
v_mfma_f32_16x16x32_bf16 v[132:135], a[52:55], v[52:55], v[132:135]
v_mfma_f32_16x16x32_bf16 v[136:139], a[52:55], v[56:59], v[136:139]
v_mfma_f32_16x16x32_bf16 v[140:143], a[52:55], v[60:63], v[140:143]
s_waitcnt lgkmcnt(4)
buffer_load_dwordx4 a[52:55], v4, s[8:11], s63 offen offset:1024 sc1
; column tiles 5..6 of the weight stream
v_mfma_f32_16x16x32_bf16 v[148:151], a[56:59], v[20:23], v[144:147]
ds_read_b128 v[48:51], v4 offset:8192
ds_read_b128 v[52:55], v4 offset:9216
v_mfma_f32_16x16x32_bf16 v[152:155], a[56:59], v[24:27], v[144:147]
ds_read_b128 v[56:59], v4 offset:10240
ds_read_b128 v[60:63], v4 offset:11264
v_mfma_f32_16x16x32_bf16 v[156:159], a[56:59], v[28:31], v[144:147]
v_mfma_f32_16x16x32_bf16 v[144:147], a[56:59], v[16:19], v[144:147]
buffer_load_dwordx4 a[56:59], v4, s[8:11], s63 offen offset:2048 sc1
v_mfma_f32_16x16x32_bf16 v[164:167], a[60:63], v[20:23], v[160:163]
v_mul_f32 v64, v64, |v64|
v_mul_f32 v65, v65, |v65|
v_mfma_f32_16x16x32_bf16 v[168:171], a[60:63], v[24:27], v[160:163]
v_mul_f32 v66, v66, |v66|
v_mul_f32 v67, v67, |v67|
v_mfma_f32_16x16x32_bf16 v[172:175], a[60:63], v[28:31], v[160:163]
v_cvt_pk_bf16_f32 v176, v64, v65
v_cvt_pk_bf16_f32 v177, v66, v67
v_mfma_f32_16x16x32_bf16 v[160:163], a[60:63], v[16:19], v[160:163]
s_waitcnt vmcnt(63) lgkmcnt(4)
buffer_load_dwordx4 a[60:63], v4, s[8:11], s63 offen offset:3072 sc1
v_pk_max_i16 v176, v176, 0
v_mfma_f32_16x16x32_bf16 v[144:147], a[64:67], v[32:35], v[144:147]
ds_read_b128 v[16:19], v4 offset:12288
ds_read_b128 v[20:23], v4 offset:13312
v_mfma_f32_16x16x32_bf16 v[148:151], a[64:67], v[36:39], v[148:151]
ds_read_b128 v[24:27], v4 offset:14336
ds_read_b128 v[28:31], v4 offset:15360
v_mfma_f32_16x16x32_bf16 v[152:155], a[64:67], v[40:43], v[152:155]
v_pk_max_i16 v177, v177, 0
v_mfma_f32_16x16x32_bf16 v[156:159], a[64:67], v[44:47], v[156:159]
s_mov_b32 s63, 172032
buffer_load_dwordx4 a[64:67], v4, s[8:11], s63 offen sc1
ds_write_b64 v6, v[176:177]
v_mfma_f32_16x16x32_bf16 v[160:163], a[68:71], v[32:35], v[160:163]
v_mul_f32 v68, v68, |v68|
v_mul_f32 v69, v69, |v69|
v_mfma_f32_16x16x32_bf16 v[164:167], a[68:71], v[36:39], v[164:167]
v_mul_f32 v70, v70, |v70|
v_mul_f32 v71, v71, |v71|
v_mfma_f32_16x16x32_bf16 v[168:171], a[68:71], v[40:43], v[168:171]
v_cvt_pk_bf16_f32 v178, v68, v69
v_cvt_pk_bf16_f32 v179, v70, v71
v_mfma_f32_16x16x32_bf16 v[172:175], a[68:71], v[44:47], v[172:175]
s_waitcnt lgkmcnt(5)
buffer_load_dwordx4 a[68:71], v4, s[8:11], s63 offen offset:1024 sc1
v_pk_max_i16 v178, v178, 0
v_mfma_f32_16x16x32_bf16 v[144:147], a[72:75], v[48:51], v[144:147]
ds_read_b128 v[32:35], v4 offset:16384
ds_read_b128 v[36:39], v4 offset:17408
v_mfma_f32_16x16x32_bf16 v[148:151], a[72:75], v[52:55], v[148:151]
ds_read_b128 v[40:43], v4 offset:18432
ds_read_b128 v[44:47], v4 offset:19456
v_mfma_f32_16x16x32_bf16 v[152:155], a[72:75], v[56:59], v[152:155]
v_pk_max_i16 v179, v179, 0
v_mfma_f32_16x16x32_bf16 v[156:159], a[72:75], v[60:63], v[156:159]
buffer_load_dwordx4 a[72:75], v4, s[8:11], s63 offen offset:2048 sc1
ds_write_b64 v6, v[178:179] offset:1024
v_mfma_f32_16x16x32_bf16 v[160:163], a[76:79], v[48:51], v[160:163]
v_mul_f32 v72, v72, |v72|
v_mul_f32 v73, v73, |v73|
v_mfma_f32_16x16x32_bf16 v[164:167], a[76:79], v[52:55], v[164:167]
v_mul_f32 v74, v74, |v74|
v_mul_f32 v75, v75, |v75|
v_mfma_f32_16x16x32_bf16 v[168:171], a[76:79], v[56:59], v[168:171]
v_cvt_pk_bf16_f32 v180, v72, v73
v_cvt_pk_bf16_f32 v181, v74, v75
v_mfma_f32_16x16x32_bf16 v[172:175], a[76:79], v[60:63], v[172:175]
s_waitcnt lgkmcnt(6)
buffer_load_dwordx4 a[76:79], v4, s[8:11], s63 offen offset:3072 sc1
v_pk_max_i16 v180, v180, 0
v_mfma_f32_16x16x32_bf16 v[144:147], a[80:83], v[16:19], v[144:147]
ds_read_b128 v[48:51], v4 offset:20480
ds_read_b128 v[52:55], v4 offset:21504
v_mfma_f32_16x16x32_bf16 v[148:151], a[80:83], v[20:23], v[148:151]
ds_read_b128 v[56:59], v4 offset:22528
ds_read_b128 v[60:63], v4 offset:23552
v_mfma_f32_16x16x32_bf16 v[152:155], a[80:83], v[24:27], v[152:155]
v_pk_max_i16 v181, v181, 0
v_mfma_f32_16x16x32_bf16 v[156:159], a[80:83], v[28:31], v[156:159]
s_mov_b32 s63, 176128
buffer_load_dwordx4 a[80:83], v4, s[8:11], s63 offen sc1
ds_write_b64 v6, v[180:181] offset:2048
v_mfma_f32_16x16x32_bf16 v[160:163], a[84:87], v[16:19], v[160:163]
v_mul_f32 v76, v76, |v76|
v_mul_f32 v77, v77, |v77|
v_mfma_f32_16x16x32_bf16 v[164:167], a[84:87], v[20:23], v[164:167]
v_mul_f32 v78, v78, |v78|
v_mul_f32 v79, v79, |v79|
v_mfma_f32_16x16x32_bf16 v[168:171], a[84:87], v[24:27], v[168:171]
v_cvt_pk_bf16_f32 v182, v76, v77
v_cvt_pk_bf16_f32 v183, v78, v79
v_mfma_f32_16x16x32_bf16 v[172:175], a[84:87], v[28:31], v[172:175]
s_waitcnt lgkmcnt(6)
buffer_load_dwordx4 a[84:87], v4, s[8:11], s63 offen offset:1024 sc1
v_pk_max_i16 v182, v182, 0
v_mfma_f32_16x16x32_bf16 v[144:147], a[88:91], v[32:35], v[144:147]
ds_read_b128 v[16:19], v4 offset:24576
ds_read_b128 v[20:23], v4 offset:25600
v_mfma_f32_16x16x32_bf16 v[148:151], a[88:91], v[36:39], v[148:151]
ds_read_b128 v[24:27], v4 offset:26624
ds_read_b128 v[28:31], v4 offset:27648
v_mfma_f32_16x16x32_bf16 v[152:155], a[88:91], v[40:43], v[152:155]
v_pk_max_i16 v183, v183, 0
v_mfma_f32_16x16x32_bf16 v[156:159], a[88:91], v[44:47], v[156:159]
buffer_load_dwordx4 a[88:91], v4, s[8:11], s63 offen offset:2048 sc1
ds_write_b64 v6, v[182:183] offset:3072
v_mfma_f32_16x16x32_bf16 v[160:163], a[92:95], v[32:35], v[160:163]
v_mul_f32 v80, v80, |v80|
v_mul_f32 v81, v81, |v81|
v_mfma_f32_16x16x32_bf16 v[164:167], a[92:95], v[36:39], v[164:167]
v_mul_f32 v82, v82, |v82|
v_mul_f32 v83, v83, |v83|
v_mfma_f32_16x16x32_bf16 v[168:171], a[92:95], v[40:43], v[168:171]
v_cvt_pk_bf16_f32 v176, v80, v81
v_cvt_pk_bf16_f32 v177, v82, v83
v_mfma_f32_16x16x32_bf16 v[172:175], a[92:95], v[44:47], v[172:175]
s_waitcnt lgkmcnt(6)
buffer_load_dwordx4 a[92:95], v4, s[8:11], s63 offen offset:3072 sc1
v_pk_max_i16 v176, v176, 0
v_mfma_f32_16x16x32_bf16 v[144:147], a[96:99], v[48:51], v[144:147]
ds_read_b128 v[32:35], v4 offset:28672
ds_read_b128 v[36:39], v4 offset:29696
v_mfma_f32_16x16x32_bf16 v[148:151], a[96:99], v[52:55], v[148:151]
ds_read_b128 v[40:43], v4 offset:30720
ds_read_b128 v[44:47], v4 offset:31744
v_mfma_f32_16x16x32_bf16 v[152:155], a[96:99], v[56:59], v[152:155]
v_pk_max_i16 v177, v177, 0
v_mfma_f32_16x16x32_bf16 v[156:159], a[96:99], v[60:63], v[156:159]
s_mov_b32 s63, 180224
buffer_load_dwordx4 a[96:99], v4, s[8:11], s63 offen sc1
ds_write_b64 v6, v[176:177] offset:8192
v_mfma_f32_16x16x32_bf16 v[160:163], a[100:103], v[48:51], v[160:163]
v_mul_f32 v84, v84, |v84|
v_mul_f32 v85, v85, |v85|
v_mfma_f32_16x16x32_bf16 v[164:167], a[100:103], v[52:55], v[164:167]
v_mul_f32 v86, v86, |v86|
v_mul_f32 v87, v87, |v87|
v_mfma_f32_16x16x32_bf16 v[168:171], a[100:103], v[56:59], v[168:171]
v_cvt_pk_bf16_f32 v178, v84, v85
v_cvt_pk_bf16_f32 v179, v86, v87
v_mfma_f32_16x16x32_bf16 v[172:175], a[100:103], v[60:63], v[172:175]
s_waitcnt lgkmcnt(6)
buffer_load_dwordx4 a[100:103], v4, s[8:11], s63 offen offset:1024 sc1
v_pk_max_i16 v178, v178, 0
v_mfma_f32_16x16x32_bf16 v[144:147], a[104:107], v[16:19], v[144:147]
ds_read_b128 v[48:51], v4 offset:32768
ds_read_b128 v[52:55], v4 offset:33792
v_mfma_f32_16x16x32_bf16 v[148:151], a[104:107], v[20:23], v[148:151]
ds_read_b128 v[56:59], v4 offset:34816
ds_read_b128 v[60:63], v4 offset:35840
v_mfma_f32_16x16x32_bf16 v[152:155], a[104:107], v[24:27], v[152:155]
v_pk_max_i16 v179, v179, 0
v_mfma_f32_16x16x32_bf16 v[156:159], a[104:107], v[28:31], v[156:159]
LW1_3_%=:
s_cbranch_execz LW1_7_%=
buffer_load_dwordx4 a[104:107], v4, s[8:11], s63 offen offset:2048 sc1
ds_write_b64 v6, v[178:179] offset:9216
v_mfma_f32_16x16x32_bf16 v[160:163], a[108:111], v[16:19], v[160:163]
v_mul_f32 v88, v88, |v88|
v_mul_f32 v89, v89, |v89|
v_mfma_f32_16x16x32_bf16 v[164:167], a[108:111], v[20:23], v[164:167]
v_mul_f32 v90, v90, |v90|
v_mul_f32 v91, v91, |v91|
v_mfma_f32_16x16x32_bf16 v[168:171], a[108:111], v[24:27], v[168:171]
v_cvt_pk_bf16_f32 v180, v88, v89
v_cvt_pk_bf16_f32 v181, v90, v91
v_mfma_f32_16x16x32_bf16 v[172:175], a[108:111], v[28:31], v[172:175]
s_waitcnt vmcnt(63) lgkmcnt(6)
buffer_load_dwordx4 a[108:111], v4, s[8:11], s63 offen offset:3072 sc1
v_pk_max_i16 v180, v180, 0
v_mfma_f32_16x16x32_bf16 v[144:147], a[112:115], v[32:35], v[144:147]
ds_read_b128 v[16:19], v4 offset:36864
ds_read_b128 v[20:23], v4 offset:37888
v_mfma_f32_16x16x32_bf16 v[148:151], a[112:115], v[36:39], v[148:151]
ds_read_b128 v[24:27], v4 offset:38912
ds_read_b128 v[28:31], v4 offset:39936
v_mfma_f32_16x16x32_bf16 v[152:155], a[112:115], v[40:43], v[152:155]
v_pk_max_i16 v181, v181, 0
v_mfma_f32_16x16x32_bf16 v[156:159], a[112:115], v[44:47], v[156:159]
s_mov_b32 s63, 184320
buffer_load_dwordx4 a[112:115], v4, s[8:11], s63 offen sc1
ds_write_b64 v6, v[180:181] offset:10240
v_mfma_f32_16x16x32_bf16 v[160:163], a[116:119], v[32:35], v[160:163]
v_mul_f32 v92, v92, |v92|
v_mul_f32 v93, v93, |v93|
v_mfma_f32_16x16x32_bf16 v[164:167], a[116:119], v[36:39], v[164:167]
v_mul_f32 v94, v94, |v94|
v_mul_f32 v95, v95, |v95|
v_mfma_f32_16x16x32_bf16 v[168:171], a[116:119], v[40:43], v[168:171]
v_cvt_pk_bf16_f32 v182, v92, v93
v_cvt_pk_bf16_f32 v183, v94, v95
v_mfma_f32_16x16x32_bf16 v[172:175], a[116:119], v[44:47], v[172:175]
s_waitcnt lgkmcnt(6)
buffer_load_dwordx4 a[116:119], v4, s[8:11], s63 offen offset:1024 sc1
v_pk_max_i16 v182, v182, 0
v_mfma_f32_16x16x32_bf16 v[144:147], a[120:123], v[48:51], v[144:147]
ds_read_b128 v[32:35], v4 offset:40960
ds_read_b128 v[36:39], v4 offset:41984
v_mfma_f32_16x16x32_bf16 v[148:151], a[120:123], v[52:55], v[148:151]
ds_read_b128 v[40:43], v4 offset:43008
ds_read_b128 v[44:47], v4 offset:44032
v_mfma_f32_16x16x32_bf16 v[152:155], a[120:123], v[56:59], v[152:155]
v_pk_max_i16 v183, v183, 0
v_mfma_f32_16x16x32_bf16 v[156:159], a[120:123], v[60:63], v[156:159]
buffer_load_dwordx4 a[120:123], v4, s[8:11], s63 offen offset:2048 sc1
ds_write_b64 v6, v[182:183] offset:11264
v_mfma_f32_16x16x32_bf16 v[160:163], a[124:127], v[48:51], v[160:163]
v_mul_f32 v96, v96, |v96|
v_mul_f32 v97, v97, |v97|
v_mfma_f32_16x16x32_bf16 v[164:167], a[124:127], v[52:55], v[164:167]
v_mul_f32 v98, v98, |v98|
v_mul_f32 v99, v99, |v99|
v_mfma_f32_16x16x32_bf16 v[168:171], a[124:127], v[56:59], v[168:171]
v_cvt_pk_bf16_f32 v176, v96, v97
v_cvt_pk_bf16_f32 v177, v98, v99
v_mfma_f32_16x16x32_bf16 v[172:175], a[124:127], v[60:63], v[172:175]
s_waitcnt lgkmcnt(6)
buffer_load_dwordx4 a[124:127], v4, s[8:11], s63 offen offset:3072 sc1
v_pk_max_i16 v176, v176, 0
v_mfma_f32_16x16x32_bf16 v[144:147], a[128:131], v[16:19], v[144:147]
ds_read_b128 v[48:51], v4 offset:45056
ds_read_b128 v[52:55], v4 offset:46080
v_mfma_f32_16x16x32_bf16 v[148:151], a[128:131], v[20:23], v[148:151]
ds_read_b128 v[56:59], v4 offset:47104
ds_read_b128 v[60:63], v4 offset:48128
v_mfma_f32_16x16x32_bf16 v[152:155], a[128:131], v[24:27], v[152:155]
v_pk_max_i16 v177, v177, 0
v_mfma_f32_16x16x32_bf16 v[156:159], a[128:131], v[28:31], v[156:159]
s_mov_b32 s63, 188416
buffer_load_dwordx4 a[128:131], v4, s[8:11], s63 offen sc1
ds_write_b64 v6, v[176:177] offset:16384
v_mfma_f32_16x16x32_bf16 v[160:163], a[132:135], v[16:19], v[160:163]
v_mul_f32 v100, v100, |v100|
v_mul_f32 v101, v101, |v101|
v_mfma_f32_16x16x32_bf16 v[164:167], a[132:135], v[20:23], v[164:167]
v_mul_f32 v102, v102, |v102|
v_mul_f32 v103, v103, |v103|
v_mfma_f32_16x16x32_bf16 v[168:171], a[132:135], v[24:27], v[168:171]
v_cvt_pk_bf16_f32 v178, v100, v101
v_cvt_pk_bf16_f32 v179, v102, v103
v_mfma_f32_16x16x32_bf16 v[172:175], a[132:135], v[28:31], v[172:175]
s_waitcnt lgkmcnt(6)
buffer_load_dwordx4 a[132:135], v4, s[8:11], s63 offen offset:1024 sc1
v_pk_max_i16 v178, v178, 0
v_mfma_f32_16x16x32_bf16 v[144:147], a[136:139], v[32:35], v[144:147]
ds_read_b128 v[16:19], v5
ds_read_b128 v[20:23], v5 offset:1024
v_mfma_f32_16x16x32_bf16 v[148:151], a[136:139], v[36:39], v[148:151]
ds_read_b128 v[24:27], v5 offset:2048
ds_read_b128 v[28:31], v5 offset:3072
v_mfma_f32_16x16x32_bf16 v[152:155], a[136:139], v[40:43], v[152:155]
v_pk_max_i16 v179, v179, 0
v_mfma_f32_16x16x32_bf16 v[156:159], a[136:139], v[44:47], v[156:159]
buffer_load_dwordx4 a[136:139], v4, s[8:11], s63 offen offset:2048 sc1
ds_write_b64 v6, v[178:179] offset:17408
v_mfma_f32_16x16x32_bf16 v[160:163], a[140:143], v[32:35], v[160:163]
v_mul_f32 v104, v104, |v104|
v_mul_f32 v105, v105, |v105|
v_mfma_f32_16x16x32_bf16 v[164:167], a[140:143], v[36:39], v[164:167]
v_mul_f32 v106, v106, |v106|
v_mul_f32 v107, v107, |v107|
v_mfma_f32_16x16x32_bf16 v[168:171], a[140:143], v[40:43], v[168:171]
v_cvt_pk_bf16_f32 v180, v104, v105
v_cvt_pk_bf16_f32 v181, v106, v107
v_mfma_f32_16x16x32_bf16 v[172:175], a[140:143], v[44:47], v[172:175]
s_waitcnt lgkmcnt(6)
buffer_load_dwordx4 a[140:143], v4, s[8:11], s63 offen offset:3072 sc1
v_pk_max_i16 v180, v180, 0
v_mfma_f32_16x16x32_bf16 v[144:147], a[144:147], v[48:51], v[144:147]
ds_read_b128 v[32:35], v5 offset:4096
ds_read_b128 v[36:39], v5 offset:5120
v_mfma_f32_16x16x32_bf16 v[148:151], a[144:147], v[52:55], v[148:151]
ds_read_b128 v[40:43], v5 offset:6144
ds_read_b128 v[44:47], v5 offset:7168
v_mfma_f32_16x16x32_bf16 v[152:155], a[144:147], v[56:59], v[152:155]
v_pk_max_i16 v181, v181, 0
v_mfma_f32_16x16x32_bf16 v[156:159], a[144:147], v[60:63], v[156:159]
s_mov_b32 s63, 192512
buffer_load_dwordx4 a[144:147], v4, s[8:11], s63 offen sc1
ds_write_b64 v6, v[180:181] offset:18432
v_mfma_f32_16x16x32_bf16 v[160:163], a[148:151], v[48:51], v[160:163]
v_mul_f32 v108, v108, |v108|
v_mul_f32 v109, v109, |v109|
v_mfma_f32_16x16x32_bf16 v[164:167], a[148:151], v[52:55], v[164:167]
v_mul_f32 v110, v110, |v110|
v_mul_f32 v111, v111, |v111|
v_mfma_f32_16x16x32_bf16 v[168:171], a[148:151], v[56:59], v[168:171]
v_cvt_pk_bf16_f32 v182, v108, v109
v_cvt_pk_bf16_f32 v183, v110, v111
v_mfma_f32_16x16x32_bf16 v[172:175], a[148:151], v[60:63], v[172:175]
s_waitcnt lgkmcnt(6)
buffer_load_dwordx4 a[148:151], v4, s[8:11], s63 offen offset:1024 sc1
v_pk_max_i16 v182, v182, 0
v_mfma_f32_16x16x32_bf16 v[144:147], a[152:155], v[16:19], v[144:147]
ds_read_b128 v[48:51], v5 offset:8192
ds_read_b128 v[52:55], v5 offset:9216
v_mfma_f32_16x16x32_bf16 v[148:151], a[152:155], v[20:23], v[148:151]
ds_read_b128 v[56:59], v5 offset:10240
ds_read_b128 v[60:63], v5 offset:11264
v_mfma_f32_16x16x32_bf16 v[152:155], a[152:155], v[24:27], v[152:155]
v_pk_max_i16 v183, v183, 0
v_mfma_f32_16x16x32_bf16 v[156:159], a[152:155], v[28:31], v[156:159]
buffer_load_dwordx4 a[152:155], v4, s[8:11], s63 offen offset:2048 sc1
ds_write_b64 v6, v[182:183] offset:19456
)"
R"(v_mfma_f32_16x16x32_bf16 v[160:163], a[156:159], v[16:19], v[160:163]
v_mul_f32 v112, v112, |v112|
v_mul_f32 v113, v113, |v113|
v_mfma_f32_16x16x32_bf16 v[164:167], a[156:159], v[20:23], v[164:167]
v_mul_f32 v114, v114, |v114|
v_mul_f32 v115, v115, |v115|
v_mfma_f32_16x16x32_bf16 v[168:171], a[156:159], v[24:27], v[168:171]
v_cvt_pk_bf16_f32 v176, v112, v113
v_cvt_pk_bf16_f32 v177, v114, v115
v_mfma_f32_16x16x32_bf16 v[172:175], a[156:159], v[28:31], v[172:175]
s_waitcnt vmcnt(63) lgkmcnt(6)
buffer_load_dwordx4 a[156:159], v4, s[8:11], s63 offen offset:3072 sc1
v_pk_max_i16 v176, v176, 0
v_mfma_f32_16x16x32_bf16 v[144:147], a[160:163], v[32:35], v[144:147]
ds_read_b128 v[16:19], v5 offset:12288
ds_read_b128 v[20:23], v5 offset:13312
v_mfma_f32_16x16x32_bf16 v[148:151], a[160:163], v[36:39], v[148:151]
ds_read_b128 v[24:27], v5 offset:14336
ds_read_b128 v[28:31], v5 offset:15360
v_mfma_f32_16x16x32_bf16 v[152:155], a[160:163], v[40:43], v[152:155]
v_pk_max_i16 v177, v177, 0
v_mfma_f32_16x16x32_bf16 v[156:159], a[160:163], v[44:47], v[156:159]
s_mov_b32 s63, 196608
buffer_load_dwordx4 a[160:163], v4, s[8:11], s63 offen sc1
ds_write_b64 v6, v[176:177] offset:24576
v_mfma_f32_16x16x32_bf16 v[160:163], a[164:167], v[32:35], v[160:163]
v_mul_f32 v116, v116, |v116|
v_mul_f32 v117, v117, |v117|
v_mfma_f32_16x16x32_bf16 v[164:167], a[164:167], v[36:39], v[164:167]
v_mul_f32 v118, v118, |v118|
v_mul_f32 v119, v119, |v119|
v_mfma_f32_16x16x32_bf16 v[168:171], a[164:167], v[40:43], v[168:171]
v_cvt_pk_bf16_f32 v178, v116, v117
v_cvt_pk_bf16_f32 v179, v118, v119
v_mfma_f32_16x16x32_bf16 v[172:175], a[164:167], v[44:47], v[172:175]
s_waitcnt lgkmcnt(6)
buffer_load_dwordx4 a[164:167], v4, s[8:11], s63 offen offset:1024 sc1
v_pk_max_i16 v178, v178, 0
v_mfma_f32_16x16x32_bf16 v[144:147], a[168:171], v[48:51], v[144:147]
ds_read_b128 v[32:35], v5 offset:16384
ds_read_b128 v[36:39], v5 offset:17408
v_mfma_f32_16x16x32_bf16 v[148:151], a[168:171], v[52:55], v[148:151]
ds_read_b128 v[40:43], v5 offset:18432
ds_read_b128 v[44:47], v5 offset:19456
v_mfma_f32_16x16x32_bf16 v[152:155], a[168:171], v[56:59], v[152:155]
ds_read_b64 v[200:201], v13 offset:896
v_pk_max_i16 v179, v179, 0
v_mfma_f32_16x16x32_bf16 v[156:159], a[168:171], v[60:63], v[156:159]
buffer_load_dwordx4 a[168:171], v4, s[8:11], s63 offen offset:2048 sc1
ds_read_b64 v[202:203], v13 offset:1024
v_mfma_f32_16x16x32_bf16 v[160:163], a[172:175], v[48:51], v[160:163]
ds_write_b64 v6, v[178:179] offset:25600
v_mul_f32 v120, v120, |v120|
v_mfma_f32_16x16x32_bf16 v[164:167], a[172:175], v[52:55], v[164:167]
v_mul_f32 v121, v121, |v121|
v_mul_f32 v122, v122, |v122|
v_mfma_f32_16x16x32_bf16 v[168:171], a[172:175], v[56:59], v[168:171]
v_mul_f32 v123, v123, |v123|
v_cvt_pk_bf16_f32 v180, v120, v121
v_mfma_f32_16x16x32_bf16 v[172:175], a[172:175], v[60:63], v[172:175]
s_waitcnt lgkmcnt(8)
buffer_load_dwordx4 a[172:175], v4, s[8:11], s63 offen offset:3072 sc1
s_waitcnt lgkmcnt(2)
v_lshlrev_b32 v64, 16, v200
v_mfma_f32_16x16x32_bf16 v[144:147], a[176:179], v[16:19], v[144:147]
ds_read_b128 v[48:51], v5 offset:20480
ds_read_b128 v[52:55], v5 offset:21504
v_mfma_f32_16x16x32_bf16 v[148:151], a[176:179], v[20:23], v[148:151]
ds_read_b128 v[56:59], v5 offset:22528
ds_read_b128 v[60:63], v5 offset:23552
v_mfma_f32_16x16x32_bf16 v[152:155], a[176:179], v[24:27], v[152:155]
v_and_b32 v65, 0xffff0000, v200
v_lshlrev_b32 v66, 16, v201
v_mfma_f32_16x16x32_bf16 v[156:159], a[176:179], v[28:31], v[156:159]
s_mov_b32 s63, 200704
buffer_load_dwordx4 a[176:179], v4, s[8:11], s63 offen sc1
v_and_b32 v67, 0xffff0000, v201
v_mfma_f32_16x16x32_bf16 v[160:163], a[180:183], v[16:19], v[160:163]
s_waitcnt lgkmcnt(5)
v_lshlrev_b32 v80, 16, v202
v_and_b32 v81, 0xffff0000, v202
v_mfma_f32_16x16x32_bf16 v[164:167], a[180:183], v[20:23], v[164:167]
v_lshlrev_b32 v82, 16, v203
v_and_b32 v83, 0xffff0000, v203
v_mfma_f32_16x16x32_bf16 v[168:171], a[180:183], v[24:27], v[168:171]
v_cvt_pk_bf16_f32 v181, v122, v123
v_pk_max_i16 v180, v180, 0
v_mfma_f32_16x16x32_bf16 v[172:175], a[180:183], v[28:31], v[172:175]
buffer_load_dwordx4 a[180:183], v4, s[8:11], s63 offen offset:1024 sc1
v_pk_max_i16 v181, v181, 0
v_mfma_f32_16x16x32_bf16 v[144:147], a[184:187], v[32:35], v[144:147]
ds_read_b128 v[16:19], v4
ds_read_b128 v[20:23], v4 offset:1024
v_mfma_f32_16x16x32_bf16 v[148:151], a[184:187], v[36:39], v[148:151]
ds_read_b128 v[24:27], v4 offset:2048
ds_read_b128 v[28:31], v4 offset:3072
v_mfma_f32_16x16x32_bf16 v[152:155], a[184:187], v[40:43], v[152:155]
ds_write_b64 v6, v[180:181] offset:26624
v_mul_f32 v124, v124, |v124|
v_mfma_f32_16x16x32_bf16 v[156:159], a[184:187], v[44:47], v[156:159]
buffer_load_dwordx4 a[184:187], v4, s[8:11], s63 offen offset:2048 sc1
v_mul_f32 v125, v125, |v125|
v_mfma_f32_16x16x32_bf16 v[160:163], a[188:191], v[32:35], v[160:163]
v_mul_f32 v126, v126, |v126|
v_mul_f32 v127, v127, |v127|
v_mfma_f32_16x16x32_bf16 v[164:167], a[188:191], v[36:39], v[164:167]
v_cvt_pk_bf16_f32 v182, v124, v125
v_cvt_pk_bf16_f32 v183, v126, v127
v_mfma_f32_16x16x32_bf16 v[168:171], a[188:191], v[40:43], v[168:171]
v_pk_max_i16 v182, v182, 0
v_pk_max_i16 v183, v183, 0
v_mfma_f32_16x16x32_bf16 v[172:175], a[188:191], v[44:47], v[172:175]
s_waitcnt lgkmcnt(5)
buffer_load_dwordx4 a[188:191], v4, s[8:11], s63 offen offset:3072 sc1
ds_write_b64 v6, v[182:183] offset:27648
v_mfma_f32_16x16x32_bf16 v[144:147], a[192:195], v[48:51], v[144:147]
ds_read_b128 v[32:35], v4 offset:4096
ds_read_b128 v[36:39], v4 offset:5120
v_mfma_f32_16x16x32_bf16 v[148:151], a[192:195], v[52:55], v[148:151]
ds_read_b128 v[40:43], v4 offset:6144
ds_read_b128 v[44:47], v4 offset:7168
v_mfma_f32_16x16x32_bf16 v[152:155], a[192:195], v[56:59], v[152:155]
v_mul_f32 v128, v128, |v128|
v_mul_f32 v129, v129, |v129|
v_mfma_f32_16x16x32_bf16 v[156:159], a[192:195], v[60:63], v[156:159]
s_mov_b32 s63, 204800
buffer_load_dwordx4 a[192:195], v4, s[8:11], s63 offen sc1
v_mul_f32 v130, v130, |v130|
v_mfma_f32_16x16x32_bf16 v[160:163], a[196:199], v[48:51], v[160:163]
v_mul_f32 v131, v131, |v131|
v_cvt_pk_bf16_f32 v176, v128, v129
v_mfma_f32_16x16x32_bf16 v[164:167], a[196:199], v[52:55], v[164:167]
v_cvt_pk_bf16_f32 v177, v130, v131
v_pk_max_i16 v176, v176, 0
v_mfma_f32_16x16x32_bf16 v[168:171], a[196:199], v[56:59], v[168:171]
v_pk_max_i16 v177, v177, 0
v_mfma_f32_16x16x32_bf16 v[172:175], a[196:199], v[60:63], v[172:175]
s_waitcnt lgkmcnt(6)
buffer_load_dwordx4 a[196:199], v4, s[8:11], s63 offen offset:1024 sc1
ds_write_b64 v6, v[176:177] offset:32768
; column tiles 7..8 of the weight stream
v_mfma_f32_16x16x32_bf16 v[68:71], a[200:203], v[20:23], v[64:67]
ds_read_b128 v[48:51], v4 offset:8192
ds_read_b128 v[52:55], v4 offset:9216
v_mfma_f32_16x16x32_bf16 v[72:75], a[200:203], v[24:27], v[64:67]
ds_read_b128 v[56:59], v4 offset:10240
ds_read_b128 v[60:63], v4 offset:11264
v_mfma_f32_16x16x32_bf16 v[76:79], a[200:203], v[28:31], v[64:67]
v_mul_f32 v132, v132, |v132|
v_mul_f32 v133, v133, |v133|
v_mfma_f32_16x16x32_bf16 v[64:67], a[200:203], v[16:19], v[64:67]
buffer_load_dwordx4 a[200:203], v4, s[8:11], s63 offen offset:2048 sc1
v_mul_f32 v134, v134, |v134|
v_mfma_f32_16x16x32_bf16 v[84:87], a[204:207], v[20:23], v[80:83]
v_mul_f32 v135, v135, |v135|
v_cvt_pk_bf16_f32 v178, v132, v133
v_mfma_f32_16x16x32_bf16 v[88:91], a[204:207], v[24:27], v[80:83]
v_cvt_pk_bf16_f32 v179, v134, v135
v_pk_max_i16 v178, v178, 0
v_mfma_f32_16x16x32_bf16 v[92:95], a[204:207], v[28:31], v[80:83]
v_pk_max_i16 v179, v179, 0
v_mfma_f32_16x16x32_bf16 v[80:83], a[204:207], v[16:19], v[80:83]
s_waitcnt vmcnt(63) lgkmcnt(5)
buffer_load_dwordx4 a[204:207], v4, s[8:11], s63 offen offset:3072 sc1
ds_write_b64 v6, v[178:179] offset:33792
v_mfma_f32_16x16x32_bf16 v[64:67], a[208:211], v[32:35], v[64:67]
ds_read_b128 v[16:19], v4 offset:12288
ds_read_b128 v[20:23], v4 offset:13312
v_mfma_f32_16x16x32_bf16 v[68:71], a[208:211], v[36:39], v[68:71]
ds_read_b128 v[24:27], v4 offset:14336
ds_read_b128 v[28:31], v4 offset:15360
v_mfma_f32_16x16x32_bf16 v[72:75], a[208:211], v[40:43], v[72:75]
v_mul_f32 v136, v136, |v136|
v_mul_f32 v137, v137, |v137|
v_mfma_f32_16x16x32_bf16 v[76:79], a[208:211], v[44:47], v[76:79]
s_mov_b32 s63, 208896
buffer_load_dwordx4 a[208:211], v4, s[8:11], s63 offen sc1
v_mul_f32 v138, v138, |v138|
v_mfma_f32_16x16x32_bf16 v[80:83], a[212:215], v[32:35], v[80:83]
v_mul_f32 v139, v139, |v139|
v_cvt_pk_bf16_f32 v180, v136, v137
v_mfma_f32_16x16x32_bf16 v[84:87], a[212:215], v[36:39], v[84:87]
v_cvt_pk_bf16_f32 v181, v138, v139
v_pk_max_i16 v180, v180, 0
v_mfma_f32_16x16x32_bf16 v[88:91], a[212:215], v[40:43], v[88:91]
v_pk_max_i16 v181, v181, 0
v_mfma_f32_16x16x32_bf16 v[92:95], a[212:215], v[44:47], v[92:95]
s_waitcnt lgkmcnt(5)
buffer_load_dwordx4 a[212:215], v4, s[8:11], s63 offen offset:1024 sc1
ds_write_b64 v6, v[180:181] offset:34816
v_mfma_f32_16x16x32_bf16 v[64:67], a[216:219], v[48:51], v[64:67]
ds_read_b128 v[32:35], v4 offset:16384
ds_read_b128 v[36:39], v4 offset:17408
v_mfma_f32_16x16x32_bf16 v[68:71], a[216:219], v[52:55], v[68:71]
ds_read_b128 v[40:43], v4 offset:18432
ds_read_b128 v[44:47], v4 offset:19456
v_mfma_f32_16x16x32_bf16 v[72:75], a[216:219], v[56:59], v[72:75]
v_mul_f32 v140, v140, |v140|
v_mul_f32 v141, v141, |v141|
v_mfma_f32_16x16x32_bf16 v[76:79], a[216:219], v[60:63], v[76:79]
buffer_load_dwordx4 a[216:219], v4, s[8:11], s63 offen offset:2048 sc1
v_mul_f32 v142, v142, |v142|
v_mfma_f32_16x16x32_bf16 v[80:83], a[220:223], v[48:51], v[80:83]
v_mul_f32 v143, v143, |v143|
v_cvt_pk_bf16_f32 v182, v140, v141
v_mfma_f32_16x16x32_bf16 v[84:87], a[220:223], v[52:55], v[84:87]
v_cvt_pk_bf16_f32 v183, v142, v143
v_pk_max_i16 v182, v182, 0
v_mfma_f32_16x16x32_bf16 v[88:91], a[220:223], v[56:59], v[88:91]
v_pk_max_i16 v183, v183, 0
v_mfma_f32_16x16x32_bf16 v[92:95], a[220:223], v[60:63], v[92:95]
s_waitcnt lgkmcnt(5)
buffer_load_dwordx4 a[220:223], v4, s[8:11], s63 offen offset:3072 sc1
ds_write_b64 v6, v[182:183] offset:35840
v_mfma_f32_16x16x32_bf16 v[64:67], a[224:227], v[16:19], v[64:67]
ds_read_b128 v[48:51], v4 offset:20480
ds_read_b128 v[52:55], v4 offset:21504
v_mfma_f32_16x16x32_bf16 v[68:71], a[224:227], v[20:23], v[68:71]
ds_read_b128 v[56:59], v4 offset:22528
ds_read_b128 v[60:63], v4 offset:23552
v_mfma_f32_16x16x32_bf16 v[72:75], a[224:227], v[24:27], v[72:75]
v_mul_f32 v144, v144, |v144|
v_mul_f32 v145, v145, |v145|
v_mfma_f32_16x16x32_bf16 v[76:79], a[224:227], v[28:31], v[76:79]
s_mov_b32 s63, 212992
buffer_load_dwordx4 a[224:227], v4, s[8:11], s63 offen sc1
v_mul_f32 v146, v146, |v146|
v_mfma_f32_16x16x32_bf16 v[80:83], a[228:231], v[16:19], v[80:83]
v_mul_f32 v147, v147, |v147|
v_cvt_pk_bf16_f32 v176, v144, v145
v_mfma_f32_16x16x32_bf16 v[84:87], a[228:231], v[20:23], v[84:87]
v_cvt_pk_bf16_f32 v177, v146, v147
v_pk_max_i16 v176, v176, 0
v_mfma_f32_16x16x32_bf16 v[88:91], a[228:231], v[24:27], v[88:91]
v_pk_max_i16 v177, v177, 0
v_mfma_f32_16x16x32_bf16 v[92:95], a[228:231], v[28:31], v[92:95]
s_waitcnt lgkmcnt(5)
buffer_load_dwordx4 a[228:231], v4, s[8:11], s63 offen offset:1024 sc1
ds_write_b64 v6, v[176:177] offset:40960
v_mfma_f32_16x16x32_bf16 v[64:67], a[232:235], v[32:35], v[64:67]
ds_read_b128 v[16:19], v4 offset:24576
ds_read_b128 v[20:23], v4 offset:25600
v_mfma_f32_16x16x32_bf16 v[68:71], a[232:235], v[36:39], v[68:71]
ds_read_b128 v[24:27], v4 offset:26624
ds_read_b128 v[28:31], v4 offset:27648
v_mfma_f32_16x16x32_bf16 v[72:75], a[232:235], v[40:43], v[72:75]
v_mul_f32 v148, v148, |v148|
v_mul_f32 v149, v149, |v149|
v_mfma_f32_16x16x32_bf16 v[76:79], a[232:235], v[44:47], v[76:79]
buffer_load_dwordx4 a[232:235], v4, s[8:11], s63 offen offset:2048 sc1
v_mul_f32 v150, v150, |v150|
v_mfma_f32_16x16x32_bf16 v[80:83], a[236:239], v[32:35], v[80:83]
v_mul_f32 v151, v151, |v151|
v_cvt_pk_bf16_f32 v178, v148, v149
v_mfma_f32_16x16x32_bf16 v[84:87], a[236:239], v[36:39], v[84:87]
v_cvt_pk_bf16_f32 v179, v150, v151
v_pk_max_i16 v178, v178, 0
v_mfma_f32_16x16x32_bf16 v[88:91], a[236:239], v[40:43], v[88:91]
v_pk_max_i16 v179, v179, 0
v_mfma_f32_16x16x32_bf16 v[92:95], a[236:239], v[44:47], v[92:95]
s_waitcnt lgkmcnt(5)
buffer_load_dwordx4 a[236:239], v4, s[8:11], s63 offen offset:3072 sc1
ds_write_b64 v6, v[178:179] offset:41984
v_mfma_f32_16x16x32_bf16 v[64:67], a[240:243], v[48:51], v[64:67]
ds_read_b128 v[32:35], v4 offset:28672
ds_read_b128 v[36:39], v4 offset:29696
v_mfma_f32_16x16x32_bf16 v[68:71], a[240:243], v[52:55], v[68:71]
ds_read_b128 v[40:43], v4 offset:30720
ds_read_b128 v[44:47], v4 offset:31744
v_mfma_f32_16x16x32_bf16 v[72:75], a[240:243], v[56:59], v[72:75]
v_mul_f32 v152, v152, |v152|
v_mul_f32 v153, v153, |v153|
v_mfma_f32_16x16x32_bf16 v[76:79], a[240:243], v[60:63], v[76:79]
s_mov_b32 s63, 217088
buffer_load_dwordx4 a[240:243], v4, s[8:11], s63 offen sc1
v_mul_f32 v154, v154, |v154|
v_mfma_f32_16x16x32_bf16 v[80:83], a[244:247], v[48:51], v[80:83]
v_mul_f32 v155, v155, |v155|
v_cvt_pk_bf16_f32 v180, v152, v153
v_mfma_f32_16x16x32_bf16 v[84:87], a[244:247], v[52:55], v[84:87]
v_cvt_pk_bf16_f32 v181, v154, v155
v_pk_max_i16 v180, v180, 0
v_mfma_f32_16x16x32_bf16 v[88:91], a[244:247], v[56:59], v[88:91]
v_pk_max_i16 v181, v181, 0
v_mfma_f32_16x16x32_bf16 v[92:95], a[244:247], v[60:63], v[92:95]
s_waitcnt lgkmcnt(5)
buffer_load_dwordx4 a[244:247], v4, s[8:11], s63 offen offset:1024 sc1
ds_write_b64 v6, v[180:181] offset:43008
v_mfma_f32_16x16x32_bf16 v[64:67], a[248:251], v[16:19], v[64:67]
ds_read_b128 v[48:51], v4 offset:32768
ds_read_b128 v[52:55], v4 offset:33792
v_mfma_f32_16x16x32_bf16 v[68:71], a[248:251], v[20:23], v[68:71]
ds_read_b128 v[56:59], v4 offset:34816
ds_read_b128 v[60:63], v4 offset:35840
v_mfma_f32_16x16x32_bf16 v[72:75], a[248:251], v[24:27], v[72:75]
v_mul_f32 v156, v156, |v156|
v_mul_f32 v157, v157, |v157|
v_mfma_f32_16x16x32_bf16 v[76:79], a[248:251], v[28:31], v[76:79]
buffer_load_dwordx4 a[248:251], v4, s[8:11], s63 offen offset:2048 sc1
v_mul_f32 v158, v158, |v158|
v_mfma_f32_16x16x32_bf16 v[80:83], a[252:255], v[16:19], v[80:83]
v_mul_f32 v159, v159, |v159|
v_cvt_pk_bf16_f32 v182, v156, v157
v_mfma_f32_16x16x32_bf16 v[84:87], a[252:255], v[20:23], v[84:87]
v_cvt_pk_bf16_f32 v183, v158, v159
v_pk_max_i16 v182, v182, 0
v_mfma_f32_16x16x32_bf16 v[88:91], a[252:255], v[24:27], v[88:91]
v_pk_max_i16 v183, v183, 0
v_mfma_f32_16x16x32_bf16 v[92:95], a[252:255], v[28:31], v[92:95]
s_waitcnt vmcnt(63) lgkmcnt(5)
buffer_load_dwordx4 a[252:255], v4, s[8:11], s63 offen offset:3072 sc1
ds_write_b64 v6, v[182:183] offset:44032
v_mfma_f32_16x16x32_bf16 v[64:67], v[224:227], v[32:35], v[64:67]
ds_read_b128 v[16:19], v4 offset:36864
ds_read_b128 v[20:23], v4 offset:37888
v_mfma_f32_16x16x32_bf16 v[68:71], v[224:227], v[36:39], v[68:71]
ds_read_b128 v[24:27], v4 offset:38912
ds_read_b128 v[28:31], v4 offset:39936
v_mfma_f32_16x16x32_bf16 v[72:75], v[224:227], v[40:43], v[72:75]
v_mul_f32 v160, v160, |v160|
v_mul_f32 v161, v161, |v161|
v_mfma_f32_16x16x32_bf16 v[76:79], v[224:227], v[44:47], v[76:79]
s_mov_b32 s63, 221184
buffer_load_dwordx4 v[224:227], v4, s[8:11], s63 offen sc1
v_mul_f32 v162, v162, |v162|
v_mfma_f32_16x16x32_bf16 v[80:83], v[228:231], v[32:35], v[80:83]
v_mul_f32 v163, v163, |v163|
v_cvt_pk_bf16_f32 v176, v160, v161
v_mfma_f32_16x16x32_bf16 v[84:87], v[228:231], v[36:39], v[84:87]
v_cvt_pk_bf16_f32 v177, v162, v163
v_pk_max_i16 v176, v176, 0
v_mfma_f32_16x16x32_bf16 v[88:91], v[228:231], v[40:43], v[88:91]
v_pk_max_i16 v177, v177, 0
v_mfma_f32_16x16x32_bf16 v[92:95], v[228:231], v[44:47], v[92:95]
s_waitcnt lgkmcnt(5)
buffer_load_dwordx4 v[228:231], v4, s[8:11], s63 offen offset:1024 sc1
ds_write_b64 v6, v[176:177] offset:49152
v_mfma_f32_16x16x32_bf16 v[64:67], v[232:235], v[48:51], v[64:67]
ds_read_b128 v[32:35], v4 offset:40960
ds_read_b128 v[36:39], v4 offset:41984
v_mfma_f32_16x16x32_bf16 v[68:71], v[232:235], v[52:55], v[68:71]
ds_read_b128 v[40:43], v4 offset:43008
ds_read_b128 v[44:47], v4 offset:44032
v_mfma_f32_16x16x32_bf16 v[72:75], v[232:235], v[56:59], v[72:75]
v_mul_f32 v164, v164, |v164|
v_mul_f32 v165, v165, |v165|
v_mfma_f32_16x16x32_bf16 v[76:79], v[232:235], v[60:63], v[76:79]
buffer_load_dwordx4 v[232:235], v4, s[8:11], s63 offen offset:2048 sc1
v_mul_f32 v166, v166, |v166|
v_mfma_f32_16x16x32_bf16 v[80:83], v[236:239], v[48:51], v[80:83]
v_mul_f32 v167, v167, |v167|
v_cvt_pk_bf16_f32 v178, v164, v165
v_mfma_f32_16x16x32_bf16 v[84:87], v[236:239], v[52:55], v[84:87]
v_cvt_pk_bf16_f32 v179, v166, v167
v_pk_max_i16 v178, v178, 0
v_mfma_f32_16x16x32_bf16 v[88:91], v[236:239], v[56:59], v[88:91]
v_pk_max_i16 v179, v179, 0
v_mfma_f32_16x16x32_bf16 v[92:95], v[236:239], v[60:63], v[92:95]
s_waitcnt lgkmcnt(5)
buffer_load_dwordx4 v[236:239], v4, s[8:11], s63 offen offset:3072 sc1
ds_write_b64 v6, v[178:179] offset:50176
v_mfma_f32_16x16x32_bf16 v[64:67], v[240:243], v[16:19], v[64:67]
ds_read_b128 v[48:51], v4 offset:45056
ds_read_b128 v[52:55], v4 offset:46080
v_mfma_f32_16x16x32_bf16 v[68:71], v[240:243], v[20:23], v[68:71]
ds_read_b128 v[56:59], v4 offset:47104
ds_read_b128 v[60:63], v4 offset:48128
v_mfma_f32_16x16x32_bf16 v[72:75], v[240:243], v[24:27], v[72:75]
v_mul_f32 v168, v168, |v168|
v_mul_f32 v169, v169, |v169|
v_mfma_f32_16x16x32_bf16 v[76:79], v[240:243], v[28:31], v[76:79]
s_mov_b32 s63, 225280
buffer_load_dwordx4 v[240:243], v4, s[8:11], s63 offen sc1
v_mul_f32 v170, v170, |v170|
v_mfma_f32_16x16x32_bf16 v[80:83], v[244:247], v[16:19], v[80:83]
v_mul_f32 v171, v171, |v171|
v_cvt_pk_bf16_f32 v180, v168, v169
v_mfma_f32_16x16x32_bf16 v[84:87], v[244:247], v[20:23], v[84:87]
LW1_4_%=:
s_cbranch_execz LWEND1_%=
v_cvt_pk_bf16_f32 v181, v170, v171
v_pk_max_i16 v180, v180, 0
v_mfma_f32_16x16x32_bf16 v[88:91], v[244:247], v[24:27], v[88:91]
v_pk_max_i16 v181, v181, 0
v_mfma_f32_16x16x32_bf16 v[92:95], v[244:247], v[28:31], v[92:95]
s_waitcnt lgkmcnt(5)
buffer_load_dwordx4 v[244:247], v4, s[8:11], s63 offen offset:1024 sc1
ds_write_b64 v6, v[180:181] offset:51200
v_mfma_f32_16x16x32_bf16 v[64:67], v[248:251], v[32:35], v[64:67]
ds_read_b128 v[16:19], v5
ds_read_b128 v[20:23], v5 offset:1024
v_mfma_f32_16x16x32_bf16 v[68:71], v[248:251], v[36:39], v[68:71]
ds_read_b128 v[24:27], v5 offset:2048
ds_read_b128 v[28:31], v5 offset:3072
v_mfma_f32_16x16x32_bf16 v[72:75], v[248:251], v[40:43], v[72:75]
v_mul_f32 v172, v172, |v172|
v_mul_f32 v173, v173, |v173|
v_mfma_f32_16x16x32_bf16 v[76:79], v[248:251], v[44:47], v[76:79]
buffer_load_dwordx4 v[248:251], v4, s[8:11], s63 offen offset:2048 sc1
v_mul_f32 v174, v174, |v174|
)"
R"(v_mfma_f32_16x16x32_bf16 v[80:83], v[252:255], v[32:35], v[80:83]
v_mul_f32 v175, v175, |v175|
v_cvt_pk_bf16_f32 v182, v172, v173
v_mfma_f32_16x16x32_bf16 v[84:87], v[252:255], v[36:39], v[84:87]
v_cvt_pk_bf16_f32 v183, v174, v175
v_pk_max_i16 v182, v182, 0
v_mfma_f32_16x16x32_bf16 v[88:91], v[252:255], v[40:43], v[88:91]
v_pk_max_i16 v183, v183, 0
v_mfma_f32_16x16x32_bf16 v[92:95], v[252:255], v[44:47], v[92:95]
s_waitcnt lgkmcnt(5)
buffer_load_dwordx4 v[252:255], v4, s[8:11], s63 offen offset:3072 sc1
ds_write_b64 v6, v[182:183] offset:52224
v_mfma_f32_16x16x32_bf16 v[64:67], v[208:211], v[48:51], v[64:67]
ds_read_b128 v[32:35], v5 offset:4096
ds_read_b128 v[36:39], v5 offset:5120
v_mfma_f32_16x16x32_bf16 v[68:71], v[208:211], v[52:55], v[68:71]
ds_read_b128 v[40:43], v5 offset:6144
ds_read_b128 v[44:47], v5 offset:7168
v_mfma_f32_16x16x32_bf16 v[72:75], v[208:211], v[56:59], v[72:75]
v_mfma_f32_16x16x32_bf16 v[76:79], v[208:211], v[60:63], v[76:79]
s_mov_b32 s63, 229376
buffer_load_dwordx4 v[208:211], v4, s[8:11], s63 offen sc1
v_mfma_f32_16x16x32_bf16 v[80:83], v[212:215], v[48:51], v[80:83]
v_mfma_f32_16x16x32_bf16 v[84:87], v[212:215], v[52:55], v[84:87]
v_mfma_f32_16x16x32_bf16 v[88:91], v[212:215], v[56:59], v[88:91]
v_mfma_f32_16x16x32_bf16 v[92:95], v[212:215], v[60:63], v[92:95]
s_waitcnt lgkmcnt(5)
buffer_load_dwordx4 v[212:215], v4, s[8:11], s63 offen offset:1024 sc1
v_mfma_f32_16x16x32_bf16 v[64:67], v[216:219], v[16:19], v[64:67]
ds_read_b128 v[48:51], v5 offset:8192
ds_read_b128 v[52:55], v5 offset:9216
v_mfma_f32_16x16x32_bf16 v[68:71], v[216:219], v[20:23], v[68:71]
ds_read_b128 v[56:59], v5 offset:10240
ds_read_b128 v[60:63], v5 offset:11264
v_mfma_f32_16x16x32_bf16 v[72:75], v[216:219], v[24:27], v[72:75]
v_mfma_f32_16x16x32_bf16 v[76:79], v[216:219], v[28:31], v[76:79]
buffer_load_dwordx4 v[216:219], v4, s[8:11], s63 offen offset:2048 sc1
v_mfma_f32_16x16x32_bf16 v[80:83], v[220:223], v[16:19], v[80:83]
v_mfma_f32_16x16x32_bf16 v[84:87], v[220:223], v[20:23], v[84:87]
v_mfma_f32_16x16x32_bf16 v[88:91], v[220:223], v[24:27], v[88:91]
v_mfma_f32_16x16x32_bf16 v[92:95], v[220:223], v[28:31], v[92:95]
s_waitcnt vmcnt(63) lgkmcnt(4)
buffer_load_dwordx4 v[220:223], v4, s[8:11], s63 offen offset:3072 sc1
v_mfma_f32_16x16x32_bf16 v[64:67], a[0:3], v[32:35], v[64:67]
ds_read_b128 v[16:19], v5 offset:12288
ds_read_b128 v[20:23], v5 offset:13312
v_mfma_f32_16x16x32_bf16 v[68:71], a[0:3], v[36:39], v[68:71]
ds_read_b128 v[24:27], v5 offset:14336
ds_read_b128 v[28:31], v5 offset:15360
v_mfma_f32_16x16x32_bf16 v[72:75], a[0:3], v[40:43], v[72:75]
v_mfma_f32_16x16x32_bf16 v[76:79], a[0:3], v[44:47], v[76:79]
s_mov_b32 s63, 233472
buffer_load_dwordx4 a[0:3], v4, s[8:11], s63 offen sc1
v_mfma_f32_16x16x32_bf16 v[80:83], a[4:7], v[32:35], v[80:83]
v_mfma_f32_16x16x32_bf16 v[84:87], a[4:7], v[36:39], v[84:87]
v_mfma_f32_16x16x32_bf16 v[88:91], a[4:7], v[40:43], v[88:91]
v_mfma_f32_16x16x32_bf16 v[92:95], a[4:7], v[44:47], v[92:95]
s_waitcnt lgkmcnt(4)
buffer_load_dwordx4 a[4:7], v4, s[8:11], s63 offen offset:1024 sc1
v_mfma_f32_16x16x32_bf16 v[64:67], a[8:11], v[48:51], v[64:67]
ds_read_b128 v[32:35], v5 offset:16384
ds_read_b128 v[36:39], v5 offset:17408
v_mfma_f32_16x16x32_bf16 v[68:71], a[8:11], v[52:55], v[68:71]
ds_read_b128 v[40:43], v5 offset:18432
ds_read_b128 v[44:47], v5 offset:19456
v_mfma_f32_16x16x32_bf16 v[72:75], a[8:11], v[56:59], v[72:75]
v_mfma_f32_16x16x32_bf16 v[76:79], a[8:11], v[60:63], v[76:79]
buffer_load_dwordx4 a[8:11], v4, s[8:11], s63 offen offset:2048 sc1
v_mfma_f32_16x16x32_bf16 v[80:83], a[12:15], v[48:51], v[80:83]
v_mfma_f32_16x16x32_bf16 v[84:87], a[12:15], v[52:55], v[84:87]
v_mfma_f32_16x16x32_bf16 v[88:91], a[12:15], v[56:59], v[88:91]
v_mfma_f32_16x16x32_bf16 v[92:95], a[12:15], v[60:63], v[92:95]
s_waitcnt lgkmcnt(4)
buffer_load_dwordx4 a[12:15], v4, s[8:11], s63 offen offset:3072 sc1
v_mfma_f32_16x16x32_bf16 v[64:67], a[16:19], v[16:19], v[64:67]
ds_read_b128 v[48:51], v5 offset:20480
ds_read_b128 v[52:55], v5 offset:21504
v_mfma_f32_16x16x32_bf16 v[68:71], a[16:19], v[20:23], v[68:71]
ds_read_b128 v[56:59], v5 offset:22528
ds_read_b128 v[60:63], v5 offset:23552
v_mfma_f32_16x16x32_bf16 v[72:75], a[16:19], v[24:27], v[72:75]
v_mfma_f32_16x16x32_bf16 v[76:79], a[16:19], v[28:31], v[76:79]
s_mov_b32 s63, 237568
buffer_load_dwordx4 a[16:19], v4, s[8:11], s63 offen sc1
v_mfma_f32_16x16x32_bf16 v[80:83], a[20:23], v[16:19], v[80:83]
v_mfma_f32_16x16x32_bf16 v[84:87], a[20:23], v[20:23], v[84:87]
v_mfma_f32_16x16x32_bf16 v[88:91], a[20:23], v[24:27], v[88:91]
v_mfma_f32_16x16x32_bf16 v[92:95], a[20:23], v[28:31], v[92:95]
s_waitcnt lgkmcnt(4)
buffer_load_dwordx4 a[20:23], v4, s[8:11], s63 offen offset:1024 sc1
s_barrier
v_mfma_f32_16x16x32_bf16 v[64:67], a[24:27], v[32:35], v[64:67]
ds_read_b128 v[16:19], v5 offset:24576
ds_read_b128 v[20:23], v5 offset:25600
v_mfma_f32_16x16x32_bf16 v[68:71], a[24:27], v[36:39], v[68:71]
ds_read_b128 v[24:27], v5 offset:26624
ds_read_b128 v[28:31], v5 offset:27648
v_mfma_f32_16x16x32_bf16 v[72:75], a[24:27], v[40:43], v[72:75]
v_mfma_f32_16x16x32_bf16 v[76:79], a[24:27], v[44:47], v[76:79]
buffer_load_dwordx4 a[24:27], v4, s[8:11], s63 offen offset:2048 sc1
v_mfma_f32_16x16x32_bf16 v[80:83], a[28:31], v[32:35], v[80:83]
v_mfma_f32_16x16x32_bf16 v[84:87], a[28:31], v[36:39], v[84:87]
v_mfma_f32_16x16x32_bf16 v[88:91], a[28:31], v[40:43], v[88:91]
v_mfma_f32_16x16x32_bf16 v[92:95], a[28:31], v[44:47], v[92:95]
s_waitcnt lgkmcnt(4)
buffer_load_dwordx4 a[28:31], v4, s[8:11], s63 offen offset:3072 sc1
v_mfma_f32_16x16x32_bf16 v[64:67], a[32:35], v[48:51], v[64:67]
ds_read_b128 v[32:35], v5 offset:28672
ds_read_b128 v[36:39], v5 offset:29696
v_mfma_f32_16x16x32_bf16 v[68:71], a[32:35], v[52:55], v[68:71]
ds_read_b128 v[40:43], v5 offset:30720
ds_read_b128 v[44:47], v5 offset:31744
v_mfma_f32_16x16x32_bf16 v[72:75], a[32:35], v[56:59], v[72:75]
v_mfma_f32_16x16x32_bf16 v[76:79], a[32:35], v[60:63], v[76:79]
s_mov_b32 s63, 241664
buffer_load_dwordx4 a[32:35], v4, s[8:11], s63 offen sc1
v_mfma_f32_16x16x32_bf16 v[80:83], a[36:39], v[48:51], v[80:83]
v_mfma_f32_16x16x32_bf16 v[84:87], a[36:39], v[52:55], v[84:87]
v_mfma_f32_16x16x32_bf16 v[88:91], a[36:39], v[56:59], v[88:91]
v_mfma_f32_16x16x32_bf16 v[92:95], a[36:39], v[60:63], v[92:95]
s_waitcnt lgkmcnt(4)
buffer_load_dwordx4 a[36:39], v4, s[8:11], s63 offen offset:1024 sc1
; column tiles 9..11 of the weight stream
v_mfma_f32_16x16x32_bf16 v[96:99], a[40:43], v[16:19], 0
ds_read_b128 v[48:51], v5 offset:32768
ds_read_b128 v[52:55], v5 offset:33792
v_mfma_f32_16x16x32_bf16 v[100:103], a[40:43], v[20:23], 0
ds_read_b128 v[56:59], v5 offset:34816
ds_read_b128 v[60:63], v5 offset:35840
v_mfma_f32_16x16x32_bf16 v[104:107], a[40:43], v[24:27], 0
v_mfma_f32_16x16x32_bf16 v[108:111], a[40:43], v[28:31], 0
buffer_load_dwordx4 a[40:43], v4, s[8:11], s63 offen offset:2048 sc1
v_mfma_f32_16x16x32_bf16 v[112:115], a[44:47], v[16:19], 0
v_mul_f32 v64, v64, |v64|
v_mul_f32 v65, v65, |v65|
v_mfma_f32_16x16x32_bf16 v[116:119], a[44:47], v[20:23], 0
v_mul_f32 v66, v66, |v66|
v_mul_f32 v67, v67, |v67|
v_mfma_f32_16x16x32_bf16 v[120:123], a[44:47], v[24:27], 0
v_cvt_pk_bf16_f32 v176, v64, v65
v_cvt_pk_bf16_f32 v177, v66, v67
v_mfma_f32_16x16x32_bf16 v[124:127], a[44:47], v[28:31], 0
s_waitcnt vmcnt(63)
buffer_load_dwordx4 a[44:47], v4, s[8:11], s63 offen offset:3072 sc1
v_pk_max_i16 v176, v176, 0
v_mfma_f32_16x16x32_bf16 v[128:131], a[48:51], v[16:19], 0
v_pk_max_i16 v177, v177, 0
v_mfma_f32_16x16x32_bf16 v[132:135], a[48:51], v[20:23], 0
ds_write_b64 v6, v[176:177] offset:57344
v_mul_f32 v68, v68, |v68|
v_mfma_f32_16x16x32_bf16 v[136:139], a[48:51], v[24:27], 0
v_mul_f32 v69, v69, |v69|
v_mul_f32 v70, v70, |v70|
v_mfma_f32_16x16x32_bf16 v[140:143], a[48:51], v[28:31], 0
s_waitcnt lgkmcnt(5)
s_mov_b32 s63, 245760
buffer_load_dwordx4 a[48:51], v4, s[8:11], s63 offen sc1
v_mul_f32 v71, v71, |v71|
v_mfma_f32_16x16x32_bf16 v[96:99], a[52:55], v[32:35], v[96:99]
ds_read_b128 v[16:19], v5 offset:36864
ds_read_b128 v[20:23], v5 offset:37888
v_mfma_f32_16x16x32_bf16 v[100:103], a[52:55], v[36:39], v[100:103]
ds_read_b128 v[24:27], v5 offset:38912
ds_read_b128 v[28:31], v5 offset:39936
v_mfma_f32_16x16x32_bf16 v[104:107], a[52:55], v[40:43], v[104:107]
v_cvt_pk_bf16_f32 v178, v68, v69
v_cvt_pk_bf16_f32 v179, v70, v71
v_mfma_f32_16x16x32_bf16 v[108:111], a[52:55], v[44:47], v[108:111]
buffer_load_dwordx4 a[52:55], v4, s[8:11], s63 offen offset:1024 sc1
v_pk_max_i16 v178, v178, 0
v_mfma_f32_16x16x32_bf16 v[112:115], a[56:59], v[32:35], v[112:115]
v_pk_max_i16 v179, v179, 0
v_mfma_f32_16x16x32_bf16 v[116:119], a[56:59], v[36:39], v[116:119]
ds_write_b64 v6, v[178:179] offset:58368
v_mul_f32 v72, v72, |v72|
v_mfma_f32_16x16x32_bf16 v[120:123], a[56:59], v[40:43], v[120:123]
v_mul_f32 v73, v73, |v73|
v_mul_f32 v74, v74, |v74|
v_mfma_f32_16x16x32_bf16 v[124:127], a[56:59], v[44:47], v[124:127]
buffer_load_dwordx4 a[56:59], v4, s[8:11], s63 offen offset:2048 sc1
v_mul_f32 v75, v75, |v75|
v_mfma_f32_16x16x32_bf16 v[128:131], a[60:63], v[32:35], v[128:131]
v_cvt_pk_bf16_f32 v180, v72, v73
v_cvt_pk_bf16_f32 v181, v74, v75
v_mfma_f32_16x16x32_bf16 v[132:135], a[60:63], v[36:39], v[132:135]
v_pk_max_i16 v180, v180, 0
v_pk_max_i16 v181, v181, 0
v_mfma_f32_16x16x32_bf16 v[136:139], a[60:63], v[40:43], v[136:139]
ds_write_b64 v6, v[180:181] offset:59392
v_mul_f32 v76, v76, |v76|
v_mfma_f32_16x16x32_bf16 v[140:143], a[60:63], v[44:47], v[140:143]
s_waitcnt lgkmcnt(7)
buffer_load_dwordx4 a[60:63], v4, s[8:11], s63 offen offset:3072 sc1
v_mul_f32 v77, v77, |v77|
v_mfma_f32_16x16x32_bf16 v[96:99], a[64:67], v[48:51], v[96:99]
ds_read_b128 v[32:35], v5 offset:40960
ds_read_b128 v[36:39], v5 offset:41984
v_mfma_f32_16x16x32_bf16 v[100:103], a[64:67], v[52:55], v[100:103]
ds_read_b128 v[40:43], v5 offset:43008
ds_read_b128 v[44:47], v5 offset:44032
v_mfma_f32_16x16x32_bf16 v[104:107], a[64:67], v[56:59], v[104:107]
v_mul_f32 v78, v78, |v78|
v_mul_f32 v79, v79, |v79|
v_mfma_f32_16x16x32_bf16 v[108:111], a[64:67], v[60:63], v[108:111]
s_mov_b32 s63, 249856
buffer_load_dwordx4 a[64:67], v4, s[8:11], s63 offen sc1
v_cvt_pk_bf16_f32 v182, v76, v77
v_mfma_f32_16x16x32_bf16 v[112:115], a[68:71], v[48:51], v[112:115]
v_cvt_pk_bf16_f32 v183, v78, v79
v_pk_max_i16 v182, v182, 0
v_mfma_f32_16x16x32_bf16 v[116:119], a[68:71], v[52:55], v[116:119]
v_pk_max_i16 v183, v183, 0
v_mfma_f32_16x16x32_bf16 v[120:123], a[68:71], v[56:59], v[120:123]
ds_write_b64 v6, v[182:183] offset:60416
v_mul_f32 v80, v80, |v80|
v_mfma_f32_16x16x32_bf16 v[124:127], a[68:71], v[60:63], v[124:127]
buffer_load_dwordx4 a[68:71], v4, s[8:11], s63 offen offset:1024 sc1
v_mul_f32 v81, v81, |v81|
v_mfma_f32_16x16x32_bf16 v[128:131], a[72:75], v[48:51], v[128:131]
v_mul_f32 v82, v82, |v82|
v_mul_f32 v83, v83, |v83|
v_mfma_f32_16x16x32_bf16 v[132:135], a[72:75], v[52:55], v[132:135]
v_cvt_pk_bf16_f32 v176, v80, v81
v_cvt_pk_bf16_f32 v177, v82, v83
v_mfma_f32_16x16x32_bf16 v[136:139], a[72:75], v[56:59], v[136:139]
v_pk_max_i16 v176, v176, 0
v_pk_max_i16 v177, v177, 0
v_mfma_f32_16x16x32_bf16 v[140:143], a[72:75], v[60:63], v[140:143]
s_waitcnt lgkmcnt(7)
buffer_load_dwordx4 a[72:75], v4, s[8:11], s63 offen offset:2048 sc1
ds_write_b64 v7, v[176:177]
v_mfma_f32_16x16x32_bf16 v[96:99], a[76:79], v[16:19], v[96:99]
ds_read_b128 v[48:51], v5 offset:45056
ds_read_b128 v[52:55], v5 offset:46080
v_mfma_f32_16x16x32_bf16 v[100:103], a[76:79], v[20:23], v[100:103]
ds_read_b128 v[56:59], v5 offset:47104
ds_read_b128 v[60:63], v5 offset:48128
v_mfma_f32_16x16x32_bf16 v[104:107], a[76:79], v[24:27], v[104:107]
v_mul_f32 v84, v84, |v84|
v_mul_f32 v85, v85, |v85|
v_mfma_f32_16x16x32_bf16 v[108:111], a[76:79], v[28:31], v[108:111]
buffer_load_dwordx4 a[76:79], v4, s[8:11], s63 offen offset:3072 sc1
v_mul_f32 v86, v86, |v86|
v_mfma_f32_16x16x32_bf16 v[112:115], a[80:83], v[16:19], v[112:115]
v_mul_f32 v87, v87, |v87|
v_cvt_pk_bf16_f32 v178, v84, v85
v_mfma_f32_16x16x32_bf16 v[116:119], a[80:83], v[20:23], v[116:119]
v_cvt_pk_bf16_f32 v179, v86, v87
v_pk_max_i16 v178, v178, 0
v_mfma_f32_16x16x32_bf16 v[120:123], a[80:83], v[24:27], v[120:123]
v_pk_max_i16 v179, v179, 0
v_mfma_f32_16x16x32_bf16 v[124:127], a[80:83], v[28:31], v[124:127]
s_mov_b32 s63, 253952
buffer_load_dwordx4 a[80:83], v4, s[8:11], s63 offen sc1
ds_write_b64 v7, v[178:179] offset:1024
v_mfma_f32_16x16x32_bf16 v[128:131], a[84:87], v[16:19], v[128:131]
v_mul_f32 v88, v88, |v88|
v_mul_f32 v89, v89, |v89|
v_mfma_f32_16x16x32_bf16 v[132:135], a[84:87], v[20:23], v[132:135]
v_mul_f32 v90, v90, |v90|
v_mul_f32 v91, v91, |v91|
v_mfma_f32_16x16x32_bf16 v[136:139], a[84:87], v[24:27], v[136:139]
v_cvt_pk_bf16_f32 v180, v88, v89
v_cvt_pk_bf16_f32 v181, v90, v91
v_mfma_f32_16x16x32_bf16 v[140:143], a[84:87], v[28:31], v[140:143]
s_waitcnt lgkmcnt(7)
buffer_load_dwordx4 a[84:87], v4, s[8:11], s63 offen offset:1024 sc1
v_pk_max_i16 v180, v180, 0
v_mfma_f32_16x16x32_bf16 v[96:99], a[88:91], v[32:35], v[96:99]
ds_read_b128 v[16:19], v191
ds_read_b128 v[20:23], v191 offset:1024
v_mfma_f32_16x16x32_bf16 v[100:103], a[88:91], v[36:39], v[100:103]
ds_read_b128 v[24:27], v191 offset:2048
ds_read_b128 v[28:31], v191 offset:3072
v_mfma_f32_16x16x32_bf16 v[104:107], a[88:91], v[40:43], v[104:107]
v_pk_max_i16 v181, v181, 0
v_mfma_f32_16x16x32_bf16 v[108:111], a[88:91], v[44:47], v[108:111]
buffer_load_dwordx4 a[88:91], v4, s[8:11], s63 offen offset:2048 sc1
ds_write_b64 v7, v[180:181] offset:2048
v_mfma_f32_16x16x32_bf16 v[112:115], a[92:95], v[32:35], v[112:115]
v_mul_f32 v92, v92, |v92|
v_mul_f32 v93, v93, |v93|
v_mfma_f32_16x16x32_bf16 v[116:119], a[92:95], v[36:39], v[116:119]
v_mul_f32 v94, v94, |v94|
v_mul_f32 v95, v95, |v95|
v_mfma_f32_16x16x32_bf16 v[120:123], a[92:95], v[40:43], v[120:123]
v_cvt_pk_bf16_f32 v182, v92, v93
v_cvt_pk_bf16_f32 v183, v94, v95
v_mfma_f32_16x16x32_bf16 v[124:127], a[92:95], v[44:47], v[124:127]
s_waitcnt vmcnt(63)
buffer_load_dwordx4 a[92:95], v4, s[8:11], s63 offen offset:3072 sc1
v_pk_max_i16 v182, v182, 0
v_mfma_f32_16x16x32_bf16 v[128:131], a[96:99], v[32:35], v[128:131]
v_pk_max_i16 v183, v183, 0
v_mfma_f32_16x16x32_bf16 v[132:135], a[96:99], v[36:39], v[132:135]
ds_write_b64 v7, v[182:183] offset:3072
v_mfma_f32_16x16x32_bf16 v[136:139], a[96:99], v[40:43], v[136:139]
v_mfma_f32_16x16x32_bf16 v[140:143], a[96:99], v[44:47], v[140:143]
s_waitcnt lgkmcnt(7)
s_mov_b32 s63, 258048
buffer_load_dwordx4 a[96:99], v4, s[8:11], s63 offen sc1
v_mfma_f32_16x16x32_bf16 v[96:99], a[100:103], v[48:51], v[96:99]
ds_read_b128 v[32:35], v191 offset:4096
ds_read_b128 v[36:39], v191 offset:5120
v_mfma_f32_16x16x32_bf16 v[100:103], a[100:103], v[52:55], v[100:103]
ds_read_b128 v[40:43], v191 offset:6144
ds_read_b128 v[44:47], v191 offset:7168
v_mfma_f32_16x16x32_bf16 v[104:107], a[100:103], v[56:59], v[104:107]
v_mfma_f32_16x16x32_bf16 v[108:111], a[100:103], v[60:63], v[108:111]
buffer_load_dwordx4 a[100:103], v4, s[8:11], s63 offen offset:1024 sc1
v_mfma_f32_16x16x32_bf16 v[112:115], a[104:107], v[48:51], v[112:115]
v_mfma_f32_16x16x32_bf16 v[116:119], a[104:107], v[52:55], v[116:119]
v_mfma_f32_16x16x32_bf16 v[120:123], a[104:107], v[56:59], v[120:123]
v_mfma_f32_16x16x32_bf16 v[124:127], a[104:107], v[60:63], v[124:127]
buffer_load_dwordx4 a[104:107], v4, s[8:11], s63 offen offset:2048 sc1
v_mfma_f32_16x16x32_bf16 v[128:131], a[108:111], v[48:51], v[128:131]
v_mfma_f32_16x16x32_bf16 v[132:135], a[108:111], v[52:55], v[132:135]
v_mfma_f32_16x16x32_bf16 v[136:139], a[108:111], v[56:59], v[136:139]
v_mfma_f32_16x16x32_bf16 v[140:143], a[108:111], v[60:63], v[140:143]
s_waitcnt lgkmcnt(6)
buffer_load_dwordx4 a[108:111], v4, s[8:11], s63 offen offset:3072 sc1
v_mfma_f32_16x16x32_bf16 v[96:99], a[112:115], v[16:19], v[96:99]
ds_read_b128 v[48:51], v191 offset:8192
ds_read_b128 v[52:55], v191 offset:9216
v_mfma_f32_16x16x32_bf16 v[100:103], a[112:115], v[20:23], v[100:103]
ds_read_b128 v[56:59], v191 offset:10240
ds_read_b128 v[60:63], v191 offset:11264
v_mfma_f32_16x16x32_bf16 v[104:107], a[112:115], v[24:27], v[104:107]
v_mfma_f32_16x16x32_bf16 v[108:111], a[112:115], v[28:31], v[108:111]
s_mov_b32 s63, 262144
buffer_load_dwordx4 a[112:115], v4, s[8:11], s63 offen sc1
v_mfma_f32_16x16x32_bf16 v[112:115], a[116:119], v[16:19], v[112:115]
v_mfma_f32_16x16x32_bf16 v[116:119], a[116:119], v[20:23], v[116:119]
v_mfma_f32_16x16x32_bf16 v[120:123], a[116:119], v[24:27], v[120:123]
v_mfma_f32_16x16x32_bf16 v[124:127], a[116:119], v[28:31], v[124:127]
buffer_load_dwordx4 a[116:119], v4, s[8:11], s63 offen offset:1024 sc1
v_mfma_f32_16x16x32_bf16 v[128:131], a[120:123], v[16:19], v[128:131]
v_mfma_f32_16x16x32_bf16 v[132:135], a[120:123], v[20:23], v[132:135]
v_mfma_f32_16x16x32_bf16 v[136:139], a[120:123], v[24:27], v[136:139]
v_mfma_f32_16x16x32_bf16 v[140:143], a[120:123], v[28:31], v[140:143]
s_waitcnt lgkmcnt(4)
buffer_load_dwordx4 a[120:123], v4, s[8:11], s63 offen offset:2048 sc1
v_mfma_f32_16x16x32_bf16 v[96:99], a[124:127], v[32:35], v[96:99]
ds_read_b128 v[16:19], v191 offset:12288
ds_read_b128 v[20:23], v191 offset:13312
v_mfma_f32_16x16x32_bf16 v[100:103], a[124:127], v[36:39], v[100:103]
ds_read_b128 v[24:27], v191 offset:14336
ds_read_b128 v[28:31], v191 offset:15360
v_mfma_f32_16x16x32_bf16 v[104:107], a[124:127], v[40:43], v[104:107]
v_mfma_f32_16x16x32_bf16 v[108:111], a[124:127], v[44:47], v[108:111]
buffer_load_dwordx4 a[124:127], v4, s[8:11], s63 offen offset:3072 sc1
v_mfma_f32_16x16x32_bf16 v[112:115], a[128:131], v[32:35], v[112:115]
v_mfma_f32_16x16x32_bf16 v[116:119], a[128:131], v[36:39], v[116:119]
v_mfma_f32_16x16x32_bf16 v[120:123], a[128:131], v[40:43], v[120:123]
v_mfma_f32_16x16x32_bf16 v[124:127], a[128:131], v[44:47], v[124:127]
s_mov_b32 s63, 266240
buffer_load_dwordx4 a[128:131], v4, s[8:11], s63 offen sc1
v_mfma_f32_16x16x32_bf16 v[128:131], a[132:135], v[32:35], v[128:131]
v_mfma_f32_16x16x32_bf16 v[132:135], a[132:135], v[36:39], v[132:135]
v_mfma_f32_16x16x32_bf16 v[136:139], a[132:135], v[40:43], v[136:139]
v_mfma_f32_16x16x32_bf16 v[140:143], a[132:135], v[44:47], v[140:143]
s_waitcnt lgkmcnt(4)
buffer_load_dwordx4 a[132:135], v4, s[8:11], s63 offen offset:1024 sc1
v_mfma_f32_16x16x32_bf16 v[96:99], a[136:139], v[48:51], v[96:99]
ds_read_b128 v[32:35], v191 offset:16384
ds_read_b128 v[36:39], v191 offset:17408
v_mfma_f32_16x16x32_bf16 v[100:103], a[136:139], v[52:55], v[100:103]
ds_read_b128 v[40:43], v191 offset:18432
ds_read_b128 v[44:47], v191 offset:19456
v_mfma_f32_16x16x32_bf16 v[104:107], a[136:139], v[56:59], v[104:107]
v_mfma_f32_16x16x32_bf16 v[108:111], a[136:139], v[60:63], v[108:111]
buffer_load_dwordx4 a[136:139], v4, s[8:11], s63 offen offset:2048 sc1
v_mfma_f32_16x16x32_bf16 v[112:115], a[140:143], v[48:51], v[112:115]
v_mfma_f32_16x16x32_bf16 v[116:119], a[140:143], v[52:55], v[116:119]
v_mfma_f32_16x16x32_bf16 v[120:123], a[140:143], v[56:59], v[120:123]
v_mfma_f32_16x16x32_bf16 v[124:127], a[140:143], v[60:63], v[124:127]
s_waitcnt vmcnt(63)
buffer_load_dwordx4 a[140:143], v4, s[8:11], s63 offen offset:3072 sc1
v_mfma_f32_16x16x32_bf16 v[128:131], a[144:147], v[48:51], v[128:131]
v_mfma_f32_16x16x32_bf16 v[132:135], a[144:147], v[52:55], v[132:135]
v_mfma_f32_16x16x32_bf16 v[136:139], a[144:147], v[56:59], v[136:139]
v_mfma_f32_16x16x32_bf16 v[140:143], a[144:147], v[60:63], v[140:143]
s_waitcnt lgkmcnt(4)
s_mov_b32 s63, 270336
buffer_load_dwordx4 a[144:147], v4, s[8:11], s63 offen sc1
v_mfma_f32_16x16x32_bf16 v[96:99], a[148:151], v[16:19], v[96:99]
ds_read_b128 v[48:51], v191 offset:20480
ds_read_b128 v[52:55], v191 offset:21504
v_mfma_f32_16x16x32_bf16 v[100:103], a[148:151], v[20:23], v[100:103]
ds_read_b128 v[56:59], v191 offset:22528
ds_read_b128 v[60:63], v191 offset:23552
v_mfma_f32_16x16x32_bf16 v[104:107], a[148:151], v[24:27], v[104:107]
v_mfma_f32_16x16x32_bf16 v[108:111], a[148:151], v[28:31], v[108:111]
buffer_load_dwordx4 a[148:151], v4, s[8:11], s63 offen offset:1024 sc1
v_mfma_f32_16x16x32_bf16 v[112:115], a[152:155], v[16:19], v[112:115]
v_mfma_f32_16x16x32_bf16 v[116:119], a[152:155], v[20:23], v[116:119]
v_mfma_f32_16x16x32_bf16 v[120:123], a[152:155], v[24:27], v[120:123]
v_mfma_f32_16x16x32_bf16 v[124:127], a[152:155], v[28:31], v[124:127]
buffer_load_dwordx4 a[152:155], v4, s[8:11], s63 offen offset:2048 sc1
v_mfma_f32_16x16x32_bf16 v[128:131], a[156:159], v[16:19], v[128:131]
)"
R"(v_mfma_f32_16x16x32_bf16 v[132:135], a[156:159], v[20:23], v[132:135]
v_mfma_f32_16x16x32_bf16 v[136:139], a[156:159], v[24:27], v[136:139]
v_mfma_f32_16x16x32_bf16 v[140:143], a[156:159], v[28:31], v[140:143]
s_waitcnt lgkmcnt(4)
buffer_load_dwordx4 a[156:159], v4, s[8:11], s63 offen offset:3072 sc1
v_mfma_f32_16x16x32_bf16 v[96:99], a[160:163], v[32:35], v[96:99]
ds_read_b128 v[16:19], v191 offset:24576
ds_read_b128 v[20:23], v191 offset:25600
v_mfma_f32_16x16x32_bf16 v[100:103], a[160:163], v[36:39], v[100:103]
ds_read_b128 v[24:27], v191 offset:26624
ds_read_b128 v[28:31], v191 offset:27648
v_mfma_f32_16x16x32_bf16 v[104:107], a[160:163], v[40:43], v[104:107]
v_mfma_f32_16x16x32_bf16 v[108:111], a[160:163], v[44:47], v[108:111]
s_mov_b32 s63, 274432
buffer_load_dwordx4 a[160:163], v4, s[8:11], s63 offen sc1
v_mfma_f32_16x16x32_bf16 v[112:115], a[164:167], v[32:35], v[112:115]
v_mfma_f32_16x16x32_bf16 v[116:119], a[164:167], v[36:39], v[116:119]
v_mfma_f32_16x16x32_bf16 v[120:123], a[164:167], v[40:43], v[120:123]
v_mfma_f32_16x16x32_bf16 v[124:127], a[164:167], v[44:47], v[124:127]
buffer_load_dwordx4 a[164:167], v4, s[8:11], s63 offen offset:1024 sc1
v_mfma_f32_16x16x32_bf16 v[128:131], a[168:171], v[32:35], v[128:131]
v_mfma_f32_16x16x32_bf16 v[132:135], a[168:171], v[36:39], v[132:135]
v_mfma_f32_16x16x32_bf16 v[136:139], a[168:171], v[40:43], v[136:139]
v_mfma_f32_16x16x32_bf16 v[140:143], a[168:171], v[44:47], v[140:143]
s_waitcnt lgkmcnt(4)
buffer_load_dwordx4 a[168:171], v4, s[8:11], s63 offen offset:2048 sc1
v_mfma_f32_16x16x32_bf16 v[96:99], a[172:175], v[48:51], v[96:99]
ds_read_b128 v[32:35], v191 offset:28672
ds_read_b128 v[36:39], v191 offset:29696
v_mfma_f32_16x16x32_bf16 v[100:103], a[172:175], v[52:55], v[100:103]
ds_read_b128 v[40:43], v191 offset:30720
ds_read_b128 v[44:47], v191 offset:31744
v_mfma_f32_16x16x32_bf16 v[104:107], a[172:175], v[56:59], v[104:107]
v_mfma_f32_16x16x32_bf16 v[108:111], a[172:175], v[60:63], v[108:111]
buffer_load_dwordx4 a[172:175], v4, s[8:11], s63 offen offset:3072 sc1
v_mfma_f32_16x16x32_bf16 v[112:115], a[176:179], v[48:51], v[112:115]
v_mfma_f32_16x16x32_bf16 v[116:119], a[176:179], v[52:55], v[116:119]
v_mfma_f32_16x16x32_bf16 v[120:123], a[176:179], v[56:59], v[120:123]
v_mfma_f32_16x16x32_bf16 v[124:127], a[176:179], v[60:63], v[124:127]
s_mov_b32 s63, 278528
buffer_load_dwordx4 a[176:179], v4, s[8:11], s63 offen sc1
v_mfma_f32_16x16x32_bf16 v[128:131], a[180:183], v[48:51], v[128:131]
v_mfma_f32_16x16x32_bf16 v[132:135], a[180:183], v[52:55], v[132:135]
v_mfma_f32_16x16x32_bf16 v[136:139], a[180:183], v[56:59], v[136:139]
v_mfma_f32_16x16x32_bf16 v[140:143], a[180:183], v[60:63], v[140:143]
s_waitcnt lgkmcnt(4)
buffer_load_dwordx4 a[180:183], v4, s[8:11], s63 offen offset:1024 sc1
s_barrier
v_mfma_f32_16x16x32_bf16 v[96:99], a[184:187], v[16:19], v[96:99]
ds_read_b128 v[48:51], v191 offset:32768
ds_read_b128 v[52:55], v191 offset:33792
v_mfma_f32_16x16x32_bf16 v[100:103], a[184:187], v[20:23], v[100:103]
ds_read_b128 v[56:59], v191 offset:34816
ds_read_b128 v[60:63], v191 offset:35840
v_mfma_f32_16x16x32_bf16 v[104:107], a[184:187], v[24:27], v[104:107]
v_mfma_f32_16x16x32_bf16 v[108:111], a[184:187], v[28:31], v[108:111]
global_load_dwordx2 v[176:177], v8, s[56:57] sc1
global_load_dwordx2 v[178:179], v8, s[56:57] offset:1024 sc1
global_load_dwordx2 v[180:181], v8, s[56:57] offset:2048 sc1
global_load_dwordx2 v[182:183], v8, s[56:57] offset:3072 sc1
global_load_dwordx2 v[200:201], v9, s[56:57] sc1
global_load_dwordx2 v[204:205], v190, s[48:49]
global_load_dwordx2 v[202:203], v190, s[28:29] offset:1024
buffer_load_dwordx4 a[184:187], v4, s[8:11], s63 offen offset:2048 sc1
v_mfma_f32_16x16x32_bf16 v[112:115], a[188:191], v[16:19], v[112:115]
v_mfma_f32_16x16x32_bf16 v[116:119], a[188:191], v[20:23], v[116:119]
v_mfma_f32_16x16x32_bf16 v[120:123], a[188:191], v[24:27], v[120:123]
v_mfma_f32_16x16x32_bf16 v[124:127], a[188:191], v[28:31], v[124:127]
s_waitcnt vmcnt(63)
buffer_load_dwordx4 a[188:191], v4, s[8:11], s63 offen offset:3072 sc1
v_mfma_f32_16x16x32_bf16 v[128:131], a[192:195], v[16:19], v[128:131]
v_mfma_f32_16x16x32_bf16 v[132:135], a[192:195], v[20:23], v[132:135]
v_mfma_f32_16x16x32_bf16 v[136:139], a[192:195], v[24:27], v[136:139]
v_mfma_f32_16x16x32_bf16 v[140:143], a[192:195], v[28:31], v[140:143]
s_waitcnt lgkmcnt(4)
s_mov_b32 s63, 282624
buffer_load_dwordx4 a[192:195], v4, s[8:11], s63 offen sc1
v_mfma_f32_16x16x32_bf16 v[96:99], a[196:199], v[32:35], v[96:99]
ds_read_b128 v[16:19], v191 offset:36864
ds_read_b128 v[20:23], v191 offset:37888
v_mfma_f32_16x16x32_bf16 v[100:103], a[196:199], v[36:39], v[100:103]
ds_read_b128 v[24:27], v191 offset:38912
ds_read_b128 v[28:31], v191 offset:39936
v_mfma_f32_16x16x32_bf16 v[104:107], a[196:199], v[40:43], v[104:107]
v_mfma_f32_16x16x32_bf16 v[108:111], a[196:199], v[44:47], v[108:111]
buffer_load_dwordx4 a[196:199], v4, s[8:11], s63 offen offset:1024 sc1
v_mfma_f32_16x16x32_bf16 v[112:115], a[200:203], v[32:35], v[112:115]
v_mfma_f32_16x16x32_bf16 v[116:119], a[200:203], v[36:39], v[116:119]
v_mfma_f32_16x16x32_bf16 v[120:123], a[200:203], v[40:43], v[120:123]
v_mfma_f32_16x16x32_bf16 v[124:127], a[200:203], v[44:47], v[124:127]
buffer_load_dwordx4 a[200:203], v4, s[8:11], s63 offen offset:2048 sc1
v_mfma_f32_16x16x32_bf16 v[128:131], a[204:207], v[32:35], v[128:131]
v_mfma_f32_16x16x32_bf16 v[132:135], a[204:207], v[36:39], v[132:135]
v_mfma_f32_16x16x32_bf16 v[136:139], a[204:207], v[40:43], v[136:139]
v_mfma_f32_16x16x32_bf16 v[140:143], a[204:207], v[44:47], v[140:143]
s_waitcnt lgkmcnt(4)
buffer_load_dwordx4 a[204:207], v4, s[8:11], s63 offen offset:3072 sc1
v_mfma_f32_16x16x32_bf16 v[96:99], a[208:211], v[48:51], v[96:99]
ds_read_b128 v[32:35], v191 offset:40960
ds_read_b128 v[36:39], v191 offset:41984
v_mfma_f32_16x16x32_bf16 v[100:103], a[208:211], v[52:55], v[100:103]
ds_read_b128 v[40:43], v191 offset:43008
ds_read_b128 v[44:47], v191 offset:44032
v_mfma_f32_16x16x32_bf16 v[104:107], a[208:211], v[56:59], v[104:107]
v_mfma_f32_16x16x32_bf16 v[108:111], a[208:211], v[60:63], v[108:111]
s_mov_b32 s63, 286720
buffer_load_dwordx4 a[208:211], v4, s[8:11], s63 offen sc1
v_mfma_f32_16x16x32_bf16 v[112:115], a[212:215], v[48:51], v[112:115]
LW1_5_%=:
s_cbranch_execz LWEND1_%=
v_mfma_f32_16x16x32_bf16 v[116:119], a[212:215], v[52:55], v[116:119]
v_mfma_f32_16x16x32_bf16 v[120:123], a[212:215], v[56:59], v[120:123]
v_mfma_f32_16x16x32_bf16 v[124:127], a[212:215], v[60:63], v[124:127]
buffer_load_dwordx4 a[212:215], v4, s[8:11], s63 offen offset:1024 sc1
v_mfma_f32_16x16x32_bf16 v[128:131], a[216:219], v[48:51], v[128:131]
v_mfma_f32_16x16x32_bf16 v[132:135], a[216:219], v[52:55], v[132:135]
v_mfma_f32_16x16x32_bf16 v[136:139], a[216:219], v[56:59], v[136:139]
v_mfma_f32_16x16x32_bf16 v[140:143], a[216:219], v[60:63], v[140:143]
s_waitcnt lgkmcnt(4)
buffer_load_dwordx4 a[216:219], v4, s[8:11], s63 offen offset:2048 sc1
v_mfma_f32_16x16x32_bf16 v[96:99], a[220:223], v[16:19], v[96:99]
ds_read_b128 v[48:51], v191 offset:45056
ds_read_b128 v[52:55], v191 offset:46080
v_mfma_f32_16x16x32_bf16 v[100:103], a[220:223], v[20:23], v[100:103]
ds_read_b128 v[56:59], v191 offset:47104
ds_read_b128 v[60:63], v191 offset:48128
v_mfma_f32_16x16x32_bf16 v[104:107], a[220:223], v[24:27], v[104:107]
v_mfma_f32_16x16x32_bf16 v[108:111], a[220:223], v[28:31], v[108:111]
buffer_load_dwordx4 a[220:223], v4, s[8:11], s63 offen offset:3072 sc1
v_mfma_f32_16x16x32_bf16 v[112:115], a[224:227], v[16:19], v[112:115]
v_mfma_f32_16x16x32_bf16 v[116:119], a[224:227], v[20:23], v[116:119]
v_mfma_f32_16x16x32_bf16 v[120:123], a[224:227], v[24:27], v[120:123]
v_mfma_f32_16x16x32_bf16 v[124:127], a[224:227], v[28:31], v[124:127]
s_mov_b32 s63, 290816
buffer_load_dwordx4 a[224:227], v4, s[8:11], s63 offen sc1
v_mfma_f32_16x16x32_bf16 v[128:131], a[228:231], v[16:19], v[128:131]
v_mfma_f32_16x16x32_bf16 v[132:135], a[228:231], v[20:23], v[132:135]
v_mfma_f32_16x16x32_bf16 v[136:139], a[228:231], v[24:27], v[136:139]
v_mfma_f32_16x16x32_bf16 v[140:143], a[228:231], v[28:31], v[140:143]
s_waitcnt lgkmcnt(4)
buffer_load_dwordx4 a[228:231], v4, s[8:11], s63 offen offset:1024 sc1
v_mfma_f32_16x16x32_bf16 v[96:99], a[232:235], v[32:35], v[96:99]
ds_read_b128 v[16:19], v5 offset:24576
ds_read_b128 v[20:23], v5 offset:25600
v_mfma_f32_16x16x32_bf16 v[100:103], a[232:235], v[36:39], v[100:103]
ds_read_b128 v[24:27], v5 offset:26624
ds_read_b128 v[28:31], v5 offset:27648
v_mfma_f32_16x16x32_bf16 v[104:107], a[232:235], v[40:43], v[104:107]
v_mfma_f32_16x16x32_bf16 v[108:111], a[232:235], v[44:47], v[108:111]
buffer_load_dwordx4 a[232:235], v4, s[8:11], s63 offen offset:2048 sc1
s_waitcnt vmcnt(14)
v_lshlrev_b32 v196, 16, v204
v_mfma_f32_16x16x32_bf16 v[112:115], a[236:239], v[32:35], v[112:115]
v_and_b32 v197, 0xffff0000, v204
v_lshlrev_b32 v198, 16, v205
v_mfma_f32_16x16x32_bf16 v[116:119], a[236:239], v[36:39], v[116:119]
v_and_b32 v199, 0xffff0000, v205
v_mfma_f32_16x16x32_bf16 v[120:123], a[236:239], v[40:43], v[120:123]
s_nop 0
v_mfma_f32_16x16x32_bf16 v[80:83], v[186:189], v[176:179], v[196:199]
v_mfma_f32_16x16x32_bf16 v[124:127], a[236:239], v[44:47], v[124:127]
buffer_load_dwordx4 a[236:239], v4, s[8:11], s63 offen offset:3072 sc1
v_mfma_f32_16x16x32_bf16 v[84:87], v[184:187], v[176:179], v[196:199]
v_mfma_f32_16x16x32_bf16 v[128:131], a[240:243], v[32:35], v[128:131]
v_mfma_f32_16x16x32_bf16 v[88:91], v[186:189], v[180:183], v[196:199]
v_mfma_f32_16x16x32_bf16 v[132:135], a[240:243], v[36:39], v[132:135]
v_mfma_f32_16x16x32_bf16 v[92:95], v[184:187], v[180:183], v[196:199]
v_mfma_f32_16x16x32_bf16 v[136:139], a[240:243], v[40:43], v[136:139]
v_mfma_f32_16x16x32_bf16 v[140:143], a[240:243], v[44:47], v[140:143]
s_waitcnt lgkmcnt(4)
s_mov_b32 s63, 294912
buffer_load_dwordx4 a[240:243], v4, s[8:11], s63 offen sc1
v_mfma_f32_16x16x32_bf16 v[96:99], a[244:247], v[48:51], v[96:99]
ds_read_b128 v[32:35], v5 offset:28672
ds_read_b128 v[36:39], v5 offset:29696
v_mfma_f32_16x16x32_bf16 v[100:103], a[244:247], v[52:55], v[100:103]
ds_read_b128 v[40:43], v5 offset:30720
ds_read_b128 v[44:47], v5 offset:31744
v_mfma_f32_16x16x32_bf16 v[104:107], a[244:247], v[56:59], v[104:107]
v_mfma_f32_16x16x32_bf16 v[108:111], a[244:247], v[60:63], v[108:111]
buffer_load_dwordx4 a[244:247], v4, s[8:11], s63 offen offset:1024 sc1
v_mfma_f32_16x16x32_bf16 v[112:115], a[248:251], v[48:51], v[112:115]
v_mfma_f32_16x16x32_bf16 v[116:119], a[248:251], v[52:55], v[116:119]
v_mfma_f32_16x16x32_bf16 v[120:123], a[248:251], v[56:59], v[120:123]
v_mfma_f32_16x16x32_bf16 v[124:127], a[248:251], v[60:63], v[124:127]
buffer_load_dwordx4 a[248:251], v4, s[8:11], s63 offen offset:2048 sc1
v_mfma_f32_16x16x32_bf16 v[128:131], a[252:255], v[48:51], v[128:131]
v_mfma_f32_16x16x32_bf16 v[132:135], a[252:255], v[52:55], v[132:135]
v_mfma_f32_16x16x32_bf16 v[136:139], a[252:255], v[56:59], v[136:139]
v_mfma_f32_16x16x32_bf16 v[140:143], a[252:255], v[60:63], v[140:143]
s_waitcnt lgkmcnt(4)
buffer_load_dwordx4 a[252:255], v4, s[8:11], s63 offen offset:3072 sc1
; column tiles 12..14 of the weight stream
v_mfma_f32_16x16x32_bf16 v[144:147], v[224:227], v[16:19], 0
ds_read_b128 v[48:51], v5 offset:32768
ds_read_b128 v[52:55], v5 offset:33792
v_mfma_f32_16x16x32_bf16 v[148:151], v[224:227], v[20:23], 0
ds_read_b128 v[56:59], v5 offset:34816
ds_read_b128 v[60:63], v5 offset:35840
v_mfma_f32_16x16x32_bf16 v[152:155], v[224:227], v[24:27], 0
v_mfma_f32_16x16x32_bf16 v[156:159], v[224:227], v[28:31], 0
s_mov_b32 s63, 299008
buffer_load_dwordx4 v[224:227], v4, s[8:11], s63 offen sc1
v_mfma_f32_16x16x32_bf16 v[160:163], v[228:231], v[16:19], 0
v_cvt_pk_bf16_f32 v176, v96, v97
v_cvt_pk_bf16_f32 v177, v98, v99
v_mfma_f32_16x16x32_bf16 v[164:167], v[228:231], v[20:23], 0
v_cvt_pk_bf16_f32 v178, v100, v101
v_cvt_pk_bf16_f32 v179, v102, v103
v_mfma_f32_16x16x32_bf16 v[168:171], v[228:231], v[24:27], 0
v_min_u32 v176, -2, v176
v_min_u32 v177, -2, v177
v_mfma_f32_16x16x32_bf16 v[172:175], v[228:231], v[28:31], 0
buffer_load_dwordx4 v[228:231], v4, s[8:11], s63 offen offset:1024 sc1
v_min_u32 v178, -2, v178
v_mfma_f32_16x16x32_bf16 v[64:67], v[232:235], v[16:19], 0
v_min_u32 v179, -2, v179
v_mfma_f32_16x16x32_bf16 v[68:71], v[232:235], v[20:23], 0
global_store_dwordx4 v4, v[176:179], s[6:7] sc1
v_mfma_f32_16x16x32_bf16 v[72:75], v[232:235], v[24:27], 0
v_mfma_f32_16x16x32_bf16 v[76:79], v[232:235], v[28:31], 0
s_waitcnt lgkmcnt(4)
buffer_load_dwordx4 v[232:235], v4, s[8:11], s63 offen offset:2048 sc1
v_mfma_f32_16x16x32_bf16 v[144:147], v[236:239], v[32:35], v[144:147]
ds_read_b128 v[16:19], v5 offset:36864
ds_read_b128 v[20:23], v5 offset:37888
v_mfma_f32_16x16x32_bf16 v[148:151], v[236:239], v[36:39], v[148:151]
ds_read_b128 v[24:27], v5 offset:38912
ds_read_b128 v[28:31], v5 offset:39936
v_mfma_f32_16x16x32_bf16 v[152:155], v[236:239], v[40:43], v[152:155]
v_mfma_f32_16x16x32_bf16 v[156:159], v[236:239], v[44:47], v[156:159]
buffer_load_dwordx4 v[236:239], v4, s[8:11], s63 offen offset:3072 sc1
v_mfma_f32_16x16x32_bf16 v[160:163], v[240:243], v[32:35], v[160:163]
v_mfma_f32_16x16x32_bf16 v[164:167], v[240:243], v[36:39], v[164:167]
v_mfma_f32_16x16x32_bf16 v[168:171], v[240:243], v[40:43], v[168:171]
v_mfma_f32_16x16x32_bf16 v[172:175], v[240:243], v[44:47], v[172:175]
s_mov_b32 s63, 303104
buffer_load_dwordx4 v[240:243], v4, s[8:11], s63 offen sc1
v_mfma_f32_16x16x32_bf16 v[64:67], v[244:247], v[32:35], v[64:67]
v_cvt_pk_bf16_f32 v180, v104, v105
v_cvt_pk_bf16_f32 v181, v106, v107
v_mfma_f32_16x16x32_bf16 v[68:71], v[244:247], v[36:39], v[68:71]
v_cvt_pk_bf16_f32 v182, v108, v109
v_cvt_pk_bf16_f32 v183, v110, v111
v_mfma_f32_16x16x32_bf16 v[72:75], v[244:247], v[40:43], v[72:75]
v_min_u32 v180, -2, v180
v_min_u32 v181, -2, v181
v_mfma_f32_16x16x32_bf16 v[76:79], v[244:247], v[44:47], v[76:79]
s_waitcnt lgkmcnt(4)
buffer_load_dwordx4 v[244:247], v4, s[8:11], s63 offen offset:1024 sc1
v_min_u32 v182, -2, v182
v_mfma_f32_16x16x32_bf16 v[144:147], v[248:251], v[48:51], v[144:147]
ds_read_b128 v[32:35], v5 offset:40960
ds_read_b128 v[36:39], v5 offset:41984
v_mfma_f32_16x16x32_bf16 v[148:151], v[248:251], v[52:55], v[148:151]
ds_read_b128 v[40:43], v5 offset:43008
ds_read_b128 v[44:47], v5 offset:44032
v_mfma_f32_16x16x32_bf16 v[152:155], v[248:251], v[56:59], v[152:155]
v_min_u32 v183, -2, v183
v_mfma_f32_16x16x32_bf16 v[156:159], v[248:251], v[60:63], v[156:159]
buffer_load_dwordx4 v[248:251], v4, s[8:11], s63 offen offset:2048 sc1
global_store_dwordx4 v4, v[180:183], s[6:7] offset:1024 sc1
v_mfma_f32_16x16x32_bf16 v[160:163], v[252:255], v[48:51], v[160:163]
s_add_u32 s6, s6, 2097152
s_addc_u32 s7, s7, 0
v_mfma_f32_16x16x32_bf16 v[164:167], v[252:255], v[52:55], v[164:167]
v_mfma_f32_16x16x32_bf16 v[168:171], v[252:255], v[56:59], v[168:171]
v_mfma_f32_16x16x32_bf16 v[172:175], v[252:255], v[60:63], v[172:175]
buffer_load_dwordx4 v[252:255], v4, s[8:11], s63 offen offset:3072 sc1
v_mfma_f32_16x16x32_bf16 v[64:67], v[208:211], v[48:51], v[64:67]
v_mfma_f32_16x16x32_bf16 v[68:71], v[208:211], v[52:55], v[68:71]
v_mfma_f32_16x16x32_bf16 v[72:75], v[208:211], v[56:59], v[72:75]
v_mfma_f32_16x16x32_bf16 v[76:79], v[208:211], v[60:63], v[76:79]
s_waitcnt lgkmcnt(4)
s_mov_b32 s63, 307200
buffer_load_dwordx4 v[208:211], v4, s[8:11], s63 offen sc1
v_mfma_f32_16x16x32_bf16 v[144:147], v[212:215], v[16:19], v[144:147]
ds_read_b128 v[48:51], v5 offset:45056
ds_read_b128 v[52:55], v5 offset:46080
v_mfma_f32_16x16x32_bf16 v[148:151], v[212:215], v[20:23], v[148:151]
ds_read_b128 v[56:59], v5 offset:47104
ds_read_b128 v[60:63], v5 offset:48128
v_mfma_f32_16x16x32_bf16 v[152:155], v[212:215], v[24:27], v[152:155]
v_mfma_f32_16x16x32_bf16 v[156:159], v[212:215], v[28:31], v[156:159]
buffer_load_dwordx4 v[212:215], v4, s[8:11], s63 offen offset:1024 sc1
v_cvt_pk_bf16_f32 v176, v112, v113
v_mfma_f32_16x16x32_bf16 v[160:163], v[216:219], v[16:19], v[160:163]
v_cvt_pk_bf16_f32 v177, v114, v115
v_cvt_pk_bf16_f32 v178, v116, v117
v_mfma_f32_16x16x32_bf16 v[164:167], v[216:219], v[20:23], v[164:167]
v_cvt_pk_bf16_f32 v179, v118, v119
v_min_u32 v176, -2, v176
v_mfma_f32_16x16x32_bf16 v[168:171], v[216:219], v[24:27], v[168:171]
v_min_u32 v177, -2, v177
v_min_u32 v178, -2, v178
v_mfma_f32_16x16x32_bf16 v[172:175], v[216:219], v[28:31], v[172:175]
buffer_load_dwordx4 v[216:219], v4, s[8:11], s63 offen offset:2048 sc1
v_min_u32 v179, -2, v179
v_mfma_f32_16x16x32_bf16 v[64:67], v[220:223], v[16:19], v[64:67]
global_store_dwordx4 v4, v[176:179], s[6:7] sc1
v_mfma_f32_16x16x32_bf16 v[68:71], v[220:223], v[20:23], v[68:71]
v_mfma_f32_16x16x32_bf16 v[72:75], v[220:223], v[24:27], v[72:75]
v_mfma_f32_16x16x32_bf16 v[76:79], v[220:223], v[28:31], v[76:79]
s_waitcnt lgkmcnt(4)
buffer_load_dwordx4 v[220:223], v4, s[8:11], s63 offen offset:3072 sc1
v_mfma_f32_16x16x32_bf16 v[144:147], a[0:3], v[32:35], v[144:147]
ds_read_b128 v[16:19], v191
ds_read_b128 v[20:23], v191 offset:1024
v_mfma_f32_16x16x32_bf16 v[148:151], a[0:3], v[36:39], v[148:151]
ds_read_b128 v[24:27], v191 offset:2048
ds_read_b128 v[28:31], v191 offset:3072
v_mfma_f32_16x16x32_bf16 v[152:155], a[0:3], v[40:43], v[152:155]
v_mfma_f32_16x16x32_bf16 v[156:159], a[0:3], v[44:47], v[156:159]
s_mov_b32 s63, 311296
buffer_load_dwordx4 a[0:3], v4, s[8:11], s63 offen sc1
v_mfma_f32_16x16x32_bf16 v[160:163], a[4:7], v[32:35], v[160:163]
v_mfma_f32_16x16x32_bf16 v[164:167], a[4:7], v[36:39], v[164:167]
v_mfma_f32_16x16x32_bf16 v[168:171], a[4:7], v[40:43], v[168:171]
v_mfma_f32_16x16x32_bf16 v[172:175], a[4:7], v[44:47], v[172:175]
buffer_load_dwordx4 a[4:7], v4, s[8:11], s63 offen offset:1024 sc1
v_cvt_pk_bf16_f32 v180, v120, v121
v_mfma_f32_16x16x32_bf16 v[64:67], a[8:11], v[32:35], v[64:67]
v_cvt_pk_bf16_f32 v181, v122, v123
v_cvt_pk_bf16_f32 v182, v124, v125
v_mfma_f32_16x16x32_bf16 v[68:71], a[8:11], v[36:39], v[68:71]
v_cvt_pk_bf16_f32 v183, v126, v127
v_min_u32 v180, -2, v180
v_mfma_f32_16x16x32_bf16 v[72:75], a[8:11], v[40:43], v[72:75]
v_min_u32 v181, -2, v181
v_min_u32 v182, -2, v182
v_mfma_f32_16x16x32_bf16 v[76:79], a[8:11], v[44:47], v[76:79]
s_waitcnt lgkmcnt(4)
buffer_load_dwordx4 a[8:11], v4, s[8:11], s63 offen offset:2048 sc1
v_min_u32 v183, -2, v183
v_mfma_f32_16x16x32_bf16 v[144:147], a[12:15], v[48:51], v[144:147]
ds_read_b128 v[32:35], v191 offset:4096
ds_read_b128 v[36:39], v191 offset:5120
v_mfma_f32_16x16x32_bf16 v[148:151], a[12:15], v[52:55], v[148:151]
ds_read_b128 v[40:43], v191 offset:6144
ds_read_b128 v[44:47], v191 offset:7168
v_mfma_f32_16x16x32_bf16 v[152:155], a[12:15], v[56:59], v[152:155]
global_store_dwordx4 v4, v[180:183], s[6:7] offset:1024 sc1
s_add_u32 s6, s6, 2097152
s_addc_u32 s7, s7, 0
v_mfma_f32_16x16x32_bf16 v[156:159], a[12:15], v[60:63], v[156:159]
buffer_load_dwordx4 a[12:15], v4, s[8:11], s63 offen offset:3072 sc1
v_mfma_f32_16x16x32_bf16 v[160:163], a[16:19], v[48:51], v[160:163]
v_mfma_f32_16x16x32_bf16 v[164:167], a[16:19], v[52:55], v[164:167]
v_mfma_f32_16x16x32_bf16 v[168:171], a[16:19], v[56:59], v[168:171]
v_mfma_f32_16x16x32_bf16 v[172:175], a[16:19], v[60:63], v[172:175]
s_mov_b32 s63, 315392
buffer_load_dwordx4 a[16:19], v4, s[8:11], s63 offen sc1
v_mfma_f32_16x16x32_bf16 v[64:67], a[20:23], v[48:51], v[64:67]
v_mfma_f32_16x16x32_bf16 v[68:71], a[20:23], v[52:55], v[68:71]
v_mfma_f32_16x16x32_bf16 v[72:75], a[20:23], v[56:59], v[72:75]
v_mfma_f32_16x16x32_bf16 v[76:79], a[20:23], v[60:63], v[76:79]
s_waitcnt lgkmcnt(4)
buffer_load_dwordx4 a[20:23], v4, s[8:11], s63 offen offset:1024 sc1
v_mfma_f32_16x16x32_bf16 v[144:147], a[24:27], v[16:19], v[144:147]
ds_read_b128 v[48:51], v191 offset:8192
ds_read_b128 v[52:55], v191 offset:9216
v_mfma_f32_16x16x32_bf16 v[148:151], a[24:27], v[20:23], v[148:151]
ds_read_b128 v[56:59], v191 offset:10240
ds_read_b128 v[60:63], v191 offset:11264
v_mfma_f32_16x16x32_bf16 v[152:155], a[24:27], v[24:27], v[152:155]
v_cvt_pk_bf16_f32 v176, v128, v129
v_cvt_pk_bf16_f32 v177, v130, v131
v_mfma_f32_16x16x32_bf16 v[156:159], a[24:27], v[28:31], v[156:159]
buffer_load_dwordx4 a[24:27], v4, s[8:11], s63 offen offset:2048 sc1
v_cvt_pk_bf16_f32 v178, v132, v133
v_mfma_f32_16x16x32_bf16 v[160:163], a[28:31], v[16:19], v[160:163]
v_cvt_pk_bf16_f32 v179, v134, v135
v_min_u32 v176, -2, v176
v_mfma_f32_16x16x32_bf16 v[164:167], a[28:31], v[20:23], v[164:167]
v_min_u32 v177, -2, v177
v_min_u32 v178, -2, v178
v_mfma_f32_16x16x32_bf16 v[168:171], a[28:31], v[24:27], v[168:171]
v_min_u32 v179, -2, v179
v_mfma_f32_16x16x32_bf16 v[172:175], a[28:31], v[28:31], v[172:175]
buffer_load_dwordx4 a[28:31], v4, s[8:11], s63 offen offset:3072 sc1
global_store_dwordx4 v4, v[176:179], s[6:7] sc1
v_mfma_f32_16x16x32_bf16 v[64:67], a[32:35], v[16:19], v[64:67]
v_mfma_f32_16x16x32_bf16 v[68:71], a[32:35], v[20:23], v[68:71]
v_mfma_f32_16x16x32_bf16 v[72:75], a[32:35], v[24:27], v[72:75]
v_mfma_f32_16x16x32_bf16 v[76:79], a[32:35], v[28:31], v[76:79]
s_waitcnt lgkmcnt(4)
s_mov_b32 s63, 319488
buffer_load_dwordx4 a[32:35], v4, s[8:11], s63 offen sc1
v_mfma_f32_16x16x32_bf16 v[144:147], a[36:39], v[32:35], v[144:147]
ds_read_b128 v[16:19], v191 offset:12288
ds_read_b128 v[20:23], v191 offset:13312
v_mfma_f32_16x16x32_bf16 v[148:151], a[36:39], v[36:39], v[148:151]
ds_read_b128 v[24:27], v191 offset:14336
ds_read_b128 v[28:31], v191 offset:15360
v_mfma_f32_16x16x32_bf16 v[152:155], a[36:39], v[40:43], v[152:155]
v_mfma_f32_16x16x32_bf16 v[156:159], a[36:39], v[44:47], v[156:159]
buffer_load_dwordx4 a[36:39], v4, s[8:11], s63 offen offset:1024 sc1
v_mfma_f32_16x16x32_bf16 v[160:163], a[40:43], v[32:35], v[160:163]
v_mfma_f32_16x16x32_bf16 v[164:167], a[40:43], v[36:39], v[164:167]
v_mfma_f32_16x16x32_bf16 v[168:171], a[40:43], v[40:43], v[168:171]
)"
R"(v_mfma_f32_16x16x32_bf16 v[172:175], a[40:43], v[44:47], v[172:175]
buffer_load_dwordx4 a[40:43], v4, s[8:11], s63 offen offset:2048 sc1
v_cvt_pk_bf16_f32 v180, v136, v137
v_mfma_f32_16x16x32_bf16 v[64:67], a[44:47], v[32:35], v[64:67]
v_cvt_pk_bf16_f32 v181, v138, v139
v_cvt_pk_bf16_f32 v182, v140, v141
v_mfma_f32_16x16x32_bf16 v[68:71], a[44:47], v[36:39], v[68:71]
v_cvt_pk_bf16_f32 v183, v142, v143
v_min_u32 v180, -2, v180
v_mfma_f32_16x16x32_bf16 v[72:75], a[44:47], v[40:43], v[72:75]
v_min_u32 v181, -2, v181
v_min_u32 v182, -2, v182
v_mfma_f32_16x16x32_bf16 v[76:79], a[44:47], v[44:47], v[76:79]
s_waitcnt lgkmcnt(4)
buffer_load_dwordx4 a[44:47], v4, s[8:11], s63 offen offset:3072 sc1
v_min_u32 v183, -2, v183
v_mfma_f32_16x16x32_bf16 v[144:147], a[48:51], v[48:51], v[144:147]
ds_read_b128 v[32:35], v191 offset:16384
ds_read_b128 v[36:39], v191 offset:17408
v_mfma_f32_16x16x32_bf16 v[148:151], a[48:51], v[52:55], v[148:151]
ds_read_b128 v[40:43], v191 offset:18432
ds_read_b128 v[44:47], v191 offset:19456
v_mfma_f32_16x16x32_bf16 v[152:155], a[48:51], v[56:59], v[152:155]
global_store_dwordx4 v4, v[180:183], s[6:7] offset:1024 sc1
s_add_u32 s6, s6, 2097152
s_addc_u32 s7, s7, 0
v_mfma_f32_16x16x32_bf16 v[156:159], a[48:51], v[60:63], v[156:159]
s_mov_b32 s63, 323584
buffer_load_dwordx4 a[48:51], v4, s[8:11], s63 offen sc1
v_mfma_f32_16x16x32_bf16 v[160:163], a[52:55], v[48:51], v[160:163]
v_mfma_f32_16x16x32_bf16 v[164:167], a[52:55], v[52:55], v[164:167]
v_mfma_f32_16x16x32_bf16 v[168:171], a[52:55], v[56:59], v[168:171]
v_mfma_f32_16x16x32_bf16 v[172:175], a[52:55], v[60:63], v[172:175]
buffer_load_dwordx4 a[52:55], v4, s[8:11], s63 offen offset:1024 sc1
v_mfma_f32_16x16x32_bf16 v[64:67], a[56:59], v[48:51], v[64:67]
v_mfma_f32_16x16x32_bf16 v[68:71], a[56:59], v[52:55], v[68:71]
v_mfma_f32_16x16x32_bf16 v[72:75], a[56:59], v[56:59], v[72:75]
v_mfma_f32_16x16x32_bf16 v[76:79], a[56:59], v[60:63], v[76:79]
s_waitcnt lgkmcnt(4)
buffer_load_dwordx4 a[56:59], v4, s[8:11], s63 offen offset:2048 sc1
v_mfma_f32_16x16x32_bf16 v[144:147], a[60:63], v[16:19], v[144:147]
ds_read_b128 v[48:51], v191 offset:20480
ds_read_b128 v[52:55], v191 offset:21504
v_mfma_f32_16x16x32_bf16 v[148:151], a[60:63], v[20:23], v[148:151]
ds_read_b128 v[56:59], v191 offset:22528
ds_read_b128 v[60:63], v191 offset:23552
v_mfma_f32_16x16x32_bf16 v[152:155], a[60:63], v[24:27], v[152:155]
v_mfma_f32_16x16x32_bf16 v[156:159], a[60:63], v[28:31], v[156:159]
buffer_load_dwordx4 a[60:63], v4, s[8:11], s63 offen offset:3072 sc1
v_mfma_f32_16x16x32_bf16 v[160:163], a[64:67], v[16:19], v[160:163]
v_mfma_f32_16x16x32_bf16 v[164:167], a[64:67], v[20:23], v[164:167]
v_mfma_f32_16x16x32_bf16 v[168:171], a[64:67], v[24:27], v[168:171]
v_mfma_f32_16x16x32_bf16 v[172:175], a[64:67], v[28:31], v[172:175]
s_mov_b32 s63, 327680
buffer_load_dwordx4 a[64:67], v4, s[8:11], s63 offen sc1
v_mfma_f32_16x16x32_bf16 v[64:67], a[68:71], v[16:19], v[64:67]
v_mfma_f32_16x16x32_bf16 v[68:71], a[68:71], v[20:23], v[68:71]
v_mfma_f32_16x16x32_bf16 v[72:75], a[68:71], v[24:27], v[72:75]
v_mfma_f32_16x16x32_bf16 v[76:79], a[68:71], v[28:31], v[76:79]
s_waitcnt lgkmcnt(4)
buffer_load_dwordx4 a[68:71], v4, s[8:11], s63 offen offset:1024 sc1
v_mfma_f32_16x16x32_bf16 v[144:147], a[72:75], v[32:35], v[144:147]
ds_read_b128 v[16:19], v191 offset:24576
ds_read_b128 v[20:23], v191 offset:25600
v_mfma_f32_16x16x32_bf16 v[148:151], a[72:75], v[36:39], v[148:151]
ds_read_b128 v[24:27], v191 offset:26624
ds_read_b128 v[28:31], v191 offset:27648
v_mfma_f32_16x16x32_bf16 v[152:155], a[72:75], v[40:43], v[152:155]
v_mfma_f32_16x16x32_bf16 v[156:159], a[72:75], v[44:47], v[156:159]
buffer_load_dwordx4 a[72:75], v4, s[8:11], s63 offen offset:2048 sc1
v_mfma_f32_16x16x32_bf16 v[160:163], a[76:79], v[32:35], v[160:163]
v_mfma_f32_16x16x32_bf16 v[164:167], a[76:79], v[36:39], v[164:167]
v_mfma_f32_16x16x32_bf16 v[168:171], a[76:79], v[40:43], v[168:171]
v_mfma_f32_16x16x32_bf16 v[172:175], a[76:79], v[44:47], v[172:175]
buffer_load_dwordx4 a[76:79], v4, s[8:11], s63 offen offset:3072 sc1
v_mfma_f32_16x16x32_bf16 v[64:67], a[80:83], v[32:35], v[64:67]
v_mfma_f32_16x16x32_bf16 v[68:71], a[80:83], v[36:39], v[68:71]
v_mfma_f32_16x16x32_bf16 v[72:75], a[80:83], v[40:43], v[72:75]
v_mfma_f32_16x16x32_bf16 v[76:79], a[80:83], v[44:47], v[76:79]
s_waitcnt lgkmcnt(4)
v_mfma_f32_16x16x32_bf16 v[144:147], a[84:87], v[48:51], v[144:147]
ds_read_b128 v[32:35], v191 offset:28672
ds_read_b128 v[36:39], v191 offset:29696
v_mfma_f32_16x16x32_bf16 v[148:151], a[84:87], v[52:55], v[148:151]
ds_read_b128 v[40:43], v191 offset:30720
ds_read_b128 v[44:47], v191 offset:31744
v_mfma_f32_16x16x32_bf16 v[152:155], a[84:87], v[56:59], v[152:155]
v_mfma_f32_16x16x32_bf16 v[156:159], a[84:87], v[60:63], v[156:159]
v_mfma_f32_16x16x32_bf16 v[160:163], a[88:91], v[48:51], v[160:163]
v_mfma_f32_16x16x32_bf16 v[164:167], a[88:91], v[52:55], v[164:167]
v_mfma_f32_16x16x32_bf16 v[168:171], a[88:91], v[56:59], v[168:171]
v_mfma_f32_16x16x32_bf16 v[172:175], a[88:91], v[60:63], v[172:175]
v_mfma_f32_16x16x32_bf16 v[64:67], a[92:95], v[48:51], v[64:67]
v_mfma_f32_16x16x32_bf16 v[68:71], a[92:95], v[52:55], v[68:71]
v_mfma_f32_16x16x32_bf16 v[72:75], a[92:95], v[56:59], v[72:75]
v_mfma_f32_16x16x32_bf16 v[76:79], a[92:95], v[60:63], v[76:79]
s_waitcnt lgkmcnt(4)
v_mfma_f32_16x16x32_bf16 v[144:147], a[96:99], v[16:19], v[144:147]
ds_read_b128 v[48:51], v191 offset:32768
ds_read_b128 v[52:55], v191 offset:33792
v_mfma_f32_16x16x32_bf16 v[148:151], a[96:99], v[20:23], v[148:151]
ds_read_b128 v[56:59], v191 offset:34816
ds_read_b128 v[60:63], v191 offset:35840
v_mfma_f32_16x16x32_bf16 v[152:155], a[96:99], v[24:27], v[152:155]
v_mfma_f32_16x16x32_bf16 v[156:159], a[96:99], v[28:31], v[156:159]
v_mfma_f32_16x16x32_bf16 v[160:163], a[100:103], v[16:19], v[160:163]
v_mfma_f32_16x16x32_bf16 v[164:167], a[100:103], v[20:23], v[164:167]
v_mfma_f32_16x16x32_bf16 v[168:171], a[100:103], v[24:27], v[168:171]
v_mfma_f32_16x16x32_bf16 v[172:175], a[100:103], v[28:31], v[172:175]
v_mfma_f32_16x16x32_bf16 v[64:67], a[104:107], v[16:19], v[64:67]
v_mfma_f32_16x16x32_bf16 v[68:71], a[104:107], v[20:23], v[68:71]
v_mfma_f32_16x16x32_bf16 v[72:75], a[104:107], v[24:27], v[72:75]
v_mfma_f32_16x16x32_bf16 v[76:79], a[104:107], v[28:31], v[76:79]
s_waitcnt lgkmcnt(4)
v_mfma_f32_16x16x32_bf16 v[144:147], a[108:111], v[32:35], v[144:147]
ds_read_b128 v[16:19], v191 offset:36864
ds_read_b128 v[20:23], v191 offset:37888
v_mfma_f32_16x16x32_bf16 v[148:151], a[108:111], v[36:39], v[148:151]
ds_read_b128 v[24:27], v191 offset:38912
ds_read_b128 v[28:31], v191 offset:39936
v_mfma_f32_16x16x32_bf16 v[152:155], a[108:111], v[40:43], v[152:155]
v_mfma_f32_16x16x32_bf16 v[156:159], a[108:111], v[44:47], v[156:159]
v_mfma_f32_16x16x32_bf16 v[160:163], a[112:115], v[32:35], v[160:163]
v_mfma_f32_16x16x32_bf16 v[164:167], a[112:115], v[36:39], v[164:167]
v_mfma_f32_16x16x32_bf16 v[168:171], a[112:115], v[40:43], v[168:171]
v_mfma_f32_16x16x32_bf16 v[172:175], a[112:115], v[44:47], v[172:175]
v_mfma_f32_16x16x32_bf16 v[64:67], a[116:119], v[32:35], v[64:67]
v_mfma_f32_16x16x32_bf16 v[68:71], a[116:119], v[36:39], v[68:71]
v_mfma_f32_16x16x32_bf16 v[72:75], a[116:119], v[40:43], v[72:75]
v_mfma_f32_16x16x32_bf16 v[76:79], a[116:119], v[44:47], v[76:79]
s_waitcnt lgkmcnt(4)
v_mfma_f32_16x16x32_bf16 v[144:147], a[120:123], v[48:51], v[144:147]
ds_read_b128 v[32:35], v191 offset:40960
ds_read_b128 v[36:39], v191 offset:41984
v_mfma_f32_16x16x32_bf16 v[148:151], a[120:123], v[52:55], v[148:151]
ds_read_b128 v[40:43], v191 offset:43008
ds_read_b128 v[44:47], v191 offset:44032
v_mfma_f32_16x16x32_bf16 v[152:155], a[120:123], v[56:59], v[152:155]
v_mfma_f32_16x16x32_bf16 v[156:159], a[120:123], v[60:63], v[156:159]
s_mov_b32 s96, 0
buffer_load_dwordx2 v[128:129], v194, s[52:55], s96 offen sc0 sc1
s_mov_b32 s96, 16384
buffer_load_dwordx2 v[130:131], v194, s[52:55], s96 offen sc0 sc1
s_mov_b32 s96, 32768
buffer_load_dwordx2 v[132:133], v194, s[52:55], s96 offen sc0 sc1
s_mov_b32 s96, 49152
buffer_load_dwordx2 v[134:135], v194, s[52:55], s96 offen sc0 sc1
v_mfma_f32_16x16x32_bf16 v[160:163], a[124:127], v[48:51], v[160:163]
v_mfma_f32_16x16x32_bf16 v[164:167], a[124:127], v[52:55], v[164:167]
v_mfma_f32_16x16x32_bf16 v[168:171], a[124:127], v[56:59], v[168:171]
v_mfma_f32_16x16x32_bf16 v[172:175], a[124:127], v[60:63], v[172:175]
v_mfma_f32_16x16x32_bf16 v[64:67], a[128:131], v[48:51], v[64:67]
v_mfma_f32_16x16x32_bf16 v[68:71], a[128:131], v[52:55], v[68:71]
v_mfma_f32_16x16x32_bf16 v[72:75], a[128:131], v[56:59], v[72:75]
v_mfma_f32_16x16x32_bf16 v[76:79], a[128:131], v[60:63], v[76:79]
s_waitcnt lgkmcnt(4)
v_mfma_f32_16x16x32_bf16 v[144:147], a[132:135], v[16:19], v[144:147]
ds_read_b128 v[48:51], v191 offset:45056
ds_read_b128 v[52:55], v191 offset:46080
v_mfma_f32_16x16x32_bf16 v[148:151], a[132:135], v[20:23], v[148:151]
ds_read_b128 v[56:59], v191 offset:47104
ds_read_b128 v[60:63], v191 offset:48128
v_mfma_f32_16x16x32_bf16 v[152:155], a[132:135], v[24:27], v[152:155]
v_mfma_f32_16x16x32_bf16 v[156:159], a[132:135], v[28:31], v[156:159]
v_mfma_f32_16x16x32_bf16 v[160:163], a[136:139], v[16:19], v[160:163]
v_mfma_f32_16x16x32_bf16 v[164:167], a[136:139], v[20:23], v[164:167]
v_mfma_f32_16x16x32_bf16 v[168:171], a[136:139], v[24:27], v[168:171]
v_mfma_f32_16x16x32_bf16 v[172:175], a[136:139], v[28:31], v[172:175]
v_mfma_f32_16x16x32_bf16 v[64:67], a[140:143], v[16:19], v[64:67]
v_mfma_f32_16x16x32_bf16 v[68:71], a[140:143], v[20:23], v[68:71]
v_mfma_f32_16x16x32_bf16 v[72:75], a[140:143], v[24:27], v[72:75]
v_mfma_f32_16x16x32_bf16 v[76:79], a[140:143], v[28:31], v[76:79]
s_waitcnt lgkmcnt(4)
v_mfma_f32_16x16x32_bf16 v[144:147], a[144:147], v[32:35], v[144:147]
ds_read_b128 v[16:19], v5 offset:24576
ds_read_b128 v[20:23], v5 offset:25600
v_mfma_f32_16x16x32_bf16 v[148:151], a[144:147], v[36:39], v[148:151]
ds_read_b128 v[24:27], v5 offset:26624
ds_read_b128 v[28:31], v5 offset:27648
v_mfma_f32_16x16x32_bf16 v[152:155], a[144:147], v[40:43], v[152:155]
v_mfma_f32_16x16x32_bf16 v[156:159], a[144:147], v[44:47], v[156:159]
v_mfma_f32_16x16x32_bf16 v[160:163], a[148:151], v[32:35], v[160:163]
v_mfma_f32_16x16x32_bf16 v[164:167], a[148:151], v[36:39], v[164:167]
v_mfma_f32_16x16x32_bf16 v[168:171], a[148:151], v[40:43], v[168:171]
v_mfma_f32_16x16x32_bf16 v[172:175], a[148:151], v[44:47], v[172:175]
v_mfma_f32_16x16x32_bf16 v[64:67], a[152:155], v[32:35], v[64:67]
v_mfma_f32_16x16x32_bf16 v[68:71], a[152:155], v[36:39], v[68:71]
v_mfma_f32_16x16x32_bf16 v[72:75], a[152:155], v[40:43], v[72:75]
v_mfma_f32_16x16x32_bf16 v[76:79], a[152:155], v[44:47], v[76:79]
s_waitcnt lgkmcnt(4)
v_mfma_f32_16x16x32_bf16 v[144:147], a[156:159], v[48:51], v[144:147]
ds_read_b128 v[32:35], v5 offset:28672
ds_read_b128 v[36:39], v5 offset:29696
v_mfma_f32_16x16x32_bf16 v[148:151], a[156:159], v[52:55], v[148:151]
ds_read_b128 v[40:43], v5 offset:30720
ds_read_b128 v[44:47], v5 offset:31744
v_mfma_f32_16x16x32_bf16 v[152:155], a[156:159], v[56:59], v[152:155]
v_mfma_f32_16x16x32_bf16 v[156:159], a[156:159], v[60:63], v[156:159]
s_sub_u32 s96, s93, 1
s_and_b32 s96, s96, 7
s_lshl_b32 s96, s96, 13
s_add_u32 s96, s96, 2097152
buffer_load_dwordx4 v[136:139], v4, s[52:55], s96 offen sc0 sc1
buffer_load_dwordx4 v[140:143], v4, s[52:55], s96 offen offset:1024 sc0 sc1
v_mfma_f32_16x16x32_bf16 v[160:163], a[160:163], v[48:51], v[160:163]
v_mfma_f32_16x16x32_bf16 v[164:167], a[160:163], v[52:55], v[164:167]
v_mfma_f32_16x16x32_bf16 v[168:171], a[160:163], v[56:59], v[168:171]
v_mfma_f32_16x16x32_bf16 v[172:175], a[160:163], v[60:63], v[172:175]
v_mfma_f32_16x16x32_bf16 v[64:67], a[164:167], v[48:51], v[64:67]
v_mfma_f32_16x16x32_bf16 v[68:71], a[164:167], v[52:55], v[68:71]
v_mfma_f32_16x16x32_bf16 v[72:75], a[164:167], v[56:59], v[72:75]
v_mfma_f32_16x16x32_bf16 v[76:79], a[164:167], v[60:63], v[76:79]
s_waitcnt lgkmcnt(4)
; column tiles 15..15 of the weight stream
v_mfma_f32_16x16x32_bf16 v[96:99], a[168:171], v[16:19], 0
ds_read_b128 v[48:51], v5 offset:32768
v_mfma_f32_16x16x32_bf16 v[100:103], a[168:171], v[20:23], 0
ds_read_b128 v[52:55], v5 offset:33792
v_mfma_f32_16x16x32_bf16 v[104:107], a[168:171], v[24:27], 0
ds_read_b128 v[56:59], v5 offset:34816
v_cvt_pk_bf16_f32 v176, v144, v145
v_mfma_f32_16x16x32_bf16 v[108:111], a[168:171], v[28:31], 0
s_waitcnt lgkmcnt(6)
ds_read_b128 v[60:63], v5 offset:35840
v_cvt_pk_bf16_f32 v177, v146, v147
v_mfma_f32_16x16x32_bf16 v[96:99], a[172:175], v[32:35], v[96:99]
s_waitcnt lgkmcnt(6)
ds_read_b128 v[16:19], v5 offset:36864
v_cvt_pk_bf16_f32 v178, v148, v149
v_mfma_f32_16x16x32_bf16 v[100:103], a[172:175], v[36:39], v[100:103]
s_waitcnt lgkmcnt(6)
ds_read_b128 v[20:23], v5 offset:37888
v_cvt_pk_bf16_f32 v179, v150, v151
v_mfma_f32_16x16x32_bf16 v[104:107], a[172:175], v[40:43], v[104:107]
s_waitcnt lgkmcnt(6)
ds_read_b128 v[24:27], v5 offset:38912
v_min_u32 v176, -2, v176
v_mfma_f32_16x16x32_bf16 v[108:111], a[172:175], v[44:47], v[108:111]
s_waitcnt lgkmcnt(6)
ds_read_b128 v[28:31], v5 offset:39936
v_min_u32 v177, -2, v177
v_mfma_f32_16x16x32_bf16 v[96:99], a[176:179], v[48:51], v[96:99]
s_waitcnt lgkmcnt(6)
ds_read_b128 v[32:35], v5 offset:40960
v_min_u32 v178, -2, v178
v_mfma_f32_16x16x32_bf16 v[100:103], a[176:179], v[52:55], v[100:103]
s_waitcnt lgkmcnt(6)
ds_read_b128 v[36:39], v5 offset:41984
v_min_u32 v179, -2, v179
v_mfma_f32_16x16x32_bf16 v[104:107], a[176:179], v[56:59], v[104:107]
s_waitcnt lgkmcnt(6)
ds_read_b128 v[40:43], v5 offset:43008
global_store_dwordx4 v4, v[176:179], s[6:7] sc1
v_mfma_f32_16x16x32_bf16 v[108:111], a[176:179], v[60:63], v[108:111]
s_waitcnt lgkmcnt(6)
ds_read_b128 v[44:47], v5 offset:44032
v_cvt_pk_bf16_f32 v180, v152, v153
v_mfma_f32_16x16x32_bf16 v[96:99], a[180:183], v[16:19], v[96:99]
LW1_6_%=:
s_cbranch_execz LWEND1_%=
s_waitcnt lgkmcnt(6)
ds_read_b128 v[48:51], v5 offset:45056
v_cvt_pk_bf16_f32 v181, v154, v155
v_mfma_f32_16x16x32_bf16 v[100:103], a[180:183], v[20:23], v[100:103]
s_waitcnt lgkmcnt(6)
ds_read_b128 v[52:55], v5 offset:46080
v_cvt_pk_bf16_f32 v182, v156, v157
v_mfma_f32_16x16x32_bf16 v[104:107], a[180:183], v[24:27], v[104:107]
s_waitcnt lgkmcnt(6)
ds_read_b128 v[56:59], v5 offset:47104
v_cvt_pk_bf16_f32 v183, v158, v159
v_mfma_f32_16x16x32_bf16 v[108:111], a[180:183], v[28:31], v[108:111]
s_waitcnt vmcnt(62) lgkmcnt(6)
ds_read_b128 v[60:63], v5 offset:48128
v_min_u32 v180, -2, v180
v_mfma_f32_16x16x32_bf16 v[96:99], a[184:187], v[32:35], v[96:99]
s_waitcnt lgkmcnt(6)
ds_read_b128 v[16:19], v191
v_min_u32 v181, -2, v181
v_mfma_f32_16x16x32_bf16 v[100:103], a[184:187], v[36:39], v[100:103]
s_waitcnt lgkmcnt(6)
ds_read_b128 v[20:23], v191 offset:1024
v_min_u32 v182, -2, v182
v_mfma_f32_16x16x32_bf16 v[104:107], a[184:187], v[40:43], v[104:107]
s_waitcnt lgkmcnt(6)
ds_read_b128 v[24:27], v191 offset:2048
v_min_u32 v183, -2, v183
v_mfma_f32_16x16x32_bf16 v[108:111], a[184:187], v[44:47], v[108:111]
s_waitcnt vmcnt(61) lgkmcnt(6)
ds_read_b128 v[28:31], v191 offset:3072
global_store_dwordx4 v4, v[180:183], s[6:7] offset:1024 sc1
v_mfma_f32_16x16x32_bf16 v[96:99], a[188:191], v[48:51], v[96:99]
s_waitcnt lgkmcnt(6)
ds_read_b128 v[32:35], v191 offset:4096
s_add_u32 s6, s6, 2097152
s_addc_u32 s7, s7, 0
v_mfma_f32_16x16x32_bf16 v[100:103], a[188:191], v[52:55], v[100:103]
s_waitcnt lgkmcnt(6)
ds_read_b128 v[36:39], v191 offset:5120
v_cvt_pk_bf16_f32 v176, v160, v161
v_mfma_f32_16x16x32_bf16 v[104:107], a[188:191], v[56:59], v[104:107]
s_waitcnt lgkmcnt(6)
ds_read_b128 v[40:43], v191 offset:6144
v_cvt_pk_bf16_f32 v177, v162, v163
v_mfma_f32_16x16x32_bf16 v[108:111], a[188:191], v[60:63], v[108:111]
s_waitcnt vmcnt(61) lgkmcnt(6)
ds_read_b128 v[44:47], v191 offset:7168
v_cvt_pk_bf16_f32 v178, v164, v165
v_mfma_f32_16x16x32_bf16 v[96:99], a[192:195], v[16:19], v[96:99]
s_waitcnt lgkmcnt(6)
ds_read_b128 v[48:51], v191 offset:8192
v_cvt_pk_bf16_f32 v179, v166, v167
v_mfma_f32_16x16x32_bf16 v[100:103], a[192:195], v[20:23], v[100:103]
s_waitcnt lgkmcnt(6)
ds_read_b128 v[52:55], v191 offset:9216
v_min_u32 v176, -2, v176
v_mfma_f32_16x16x32_bf16 v[104:107], a[192:195], v[24:27], v[104:107]
s_waitcnt lgkmcnt(6)
ds_read_b128 v[56:59], v191 offset:10240
v_min_u32 v177, -2, v177
v_mfma_f32_16x16x32_bf16 v[108:111], a[192:195], v[28:31], v[108:111]
s_waitcnt vmcnt(60) lgkmcnt(6)
s_sub_u32 s96, s93, 2
s_and_b32 s96, s96, 7
s_lshl_b32 s96, s96, 13
s_add_u32 s96, s96, 4194304
buffer_load_dwordx4 v[144:147], v4, s[52:55], s96 offen sc0 sc1
buffer_load_dwordx4 v[148:151], v4, s[52:55], s96 offen offset:1024 sc0 sc1
ds_read_b128 v[60:63], v191 offset:11264
v_mfma_f32_16x16x32_bf16 v[96:99], a[196:199], v[32:35], v[96:99]
s_waitcnt lgkmcnt(6)
ds_read_b128 v[16:19], v191 offset:12288
v_min_u32 v178, -2, v178
v_mfma_f32_16x16x32_bf16 v[100:103], a[196:199], v[36:39], v[100:103]
s_waitcnt lgkmcnt(6)
ds_read_b128 v[20:23], v191 offset:13312
v_min_u32 v179, -2, v179
v_mfma_f32_16x16x32_bf16 v[104:107], a[196:199], v[40:43], v[104:107]
s_waitcnt lgkmcnt(6)
ds_read_b128 v[24:27], v191 offset:14336
global_store_dwordx4 v4, v[176:179], s[6:7] sc1
v_mfma_f32_16x16x32_bf16 v[108:111], a[196:199], v[44:47], v[108:111]
s_waitcnt vmcnt(62) lgkmcnt(6)
ds_read_b128 v[28:31], v191 offset:15360
v_cvt_pk_bf16_f32 v180, v168, v169
v_mfma_f32_16x16x32_bf16 v[96:99], a[200:203], v[48:51], v[96:99]
s_waitcnt lgkmcnt(6)
ds_read_b128 v[32:35], v191 offset:16384
v_cvt_pk_bf16_f32 v181, v170, v171
v_mfma_f32_16x16x32_bf16 v[100:103], a[200:203], v[52:55], v[100:103]
s_waitcnt lgkmcnt(6)
ds_read_b128 v[36:39], v191 offset:17408
v_cvt_pk_bf16_f32 v182, v172, v173
v_mfma_f32_16x16x32_bf16 v[104:107], a[200:203], v[56:59], v[104:107]
s_waitcnt lgkmcnt(6)
ds_read_b128 v[40:43], v191 offset:18432
v_cvt_pk_bf16_f32 v183, v174, v175
v_mfma_f32_16x16x32_bf16 v[108:111], a[200:203], v[60:63], v[108:111]
s_waitcnt vmcnt(61) lgkmcnt(6)
ds_read_b128 v[44:47], v191 offset:19456
v_min_u32 v180, -2, v180
v_mfma_f32_16x16x32_bf16 v[96:99], a[204:207], v[16:19], v[96:99]
s_waitcnt lgkmcnt(6)
ds_read_b128 v[48:51], v191 offset:20480
v_min_u32 v181, -2, v181
v_mfma_f32_16x16x32_bf16 v[100:103], a[204:207], v[20:23], v[100:103]
s_waitcnt lgkmcnt(6)
ds_read_b128 v[52:55], v191 offset:21504
v_min_u32 v182, -2, v182
v_mfma_f32_16x16x32_bf16 v[104:107], a[204:207], v[24:27], v[104:107]
s_waitcnt lgkmcnt(6)
ds_read_b128 v[56:59], v191 offset:22528
v_min_u32 v183, -2, v183
v_mfma_f32_16x16x32_bf16 v[108:111], a[204:207], v[28:31], v[108:111]
s_waitcnt vmcnt(60) lgkmcnt(6)
ds_read_b128 v[60:63], v191 offset:23552
global_store_dwordx4 v4, v[180:183], s[6:7] offset:1024 sc1
v_mfma_f32_16x16x32_bf16 v[96:99], a[208:211], v[32:35], v[96:99]
s_waitcnt lgkmcnt(6)
ds_read_b128 v[16:19], v191 offset:24576
s_add_u32 s6, s6, 2097152
s_addc_u32 s7, s7, 0
v_mfma_f32_16x16x32_bf16 v[100:103], a[208:211], v[36:39], v[100:103]
s_waitcnt lgkmcnt(6)
ds_read_b128 v[20:23], v191 offset:25600
v_cvt_pk_bf16_f32 v176, v64, v65
v_mfma_f32_16x16x32_bf16 v[104:107], a[208:211], v[40:43], v[104:107]
s_waitcnt lgkmcnt(6)
ds_read_b128 v[24:27], v191 offset:26624
v_cvt_pk_bf16_f32 v177, v66, v67
v_mfma_f32_16x16x32_bf16 v[108:111], a[208:211], v[44:47], v[108:111]
s_waitcnt vmcnt(60) lgkmcnt(6)
ds_read_b128 v[28:31], v191 offset:27648
v_cvt_pk_bf16_f32 v178, v68, v69
v_mfma_f32_16x16x32_bf16 v[96:99], a[212:215], v[48:51], v[96:99]
s_waitcnt lgkmcnt(6)
ds_read_b128 v[32:35], v191 offset:28672
v_cvt_pk_bf16_f32 v179, v70, v71
v_mfma_f32_16x16x32_bf16 v[100:103], a[212:215], v[52:55], v[100:103]
s_waitcnt lgkmcnt(6)
)"
R"(ds_read_b128 v[36:39], v191 offset:29696
v_min_u32 v176, -2, v176
v_mfma_f32_16x16x32_bf16 v[104:107], a[212:215], v[56:59], v[104:107]
s_waitcnt lgkmcnt(6)
ds_read_b128 v[40:43], v191 offset:30720
v_min_u32 v177, -2, v177
v_mfma_f32_16x16x32_bf16 v[108:111], a[212:215], v[60:63], v[108:111]
s_waitcnt vmcnt(59) lgkmcnt(6)
ds_read_b128 v[44:47], v191 offset:31744
v_min_u32 v178, -2, v178
v_mfma_f32_16x16x32_bf16 v[96:99], a[216:219], v[16:19], v[96:99]
s_waitcnt lgkmcnt(6)
ds_read_b128 v[48:51], v191 offset:32768
v_min_u32 v179, -2, v179
v_mfma_f32_16x16x32_bf16 v[100:103], a[216:219], v[20:23], v[100:103]
s_waitcnt lgkmcnt(6)
ds_read_b128 v[52:55], v191 offset:33792
global_store_dwordx4 v4, v[176:179], s[6:7] sc1
v_mfma_f32_16x16x32_bf16 v[104:107], a[216:219], v[24:27], v[104:107]
s_waitcnt lgkmcnt(6)
ds_read_b128 v[56:59], v191 offset:34816
v_cvt_pk_bf16_f32 v180, v72, v73
v_mfma_f32_16x16x32_bf16 v[108:111], a[216:219], v[28:31], v[108:111]
s_waitcnt vmcnt(59) lgkmcnt(6)
ds_read_b128 v[60:63], v191 offset:35840
v_cvt_pk_bf16_f32 v181, v74, v75
v_mfma_f32_16x16x32_bf16 v[96:99], a[220:223], v[32:35], v[96:99]
s_waitcnt lgkmcnt(6)
ds_read_b128 v[16:19], v191 offset:36864
v_cvt_pk_bf16_f32 v182, v76, v77
v_mfma_f32_16x16x32_bf16 v[100:103], a[220:223], v[36:39], v[100:103]
s_waitcnt lgkmcnt(6)
ds_read_b128 v[20:23], v191 offset:37888
v_cvt_pk_bf16_f32 v183, v78, v79
v_mfma_f32_16x16x32_bf16 v[104:107], a[220:223], v[40:43], v[104:107]
s_waitcnt lgkmcnt(6)
ds_read_b128 v[24:27], v191 offset:38912
v_min_u32 v180, -2, v180
v_mfma_f32_16x16x32_bf16 v[108:111], a[220:223], v[44:47], v[108:111]
s_waitcnt vmcnt(58) lgkmcnt(6)
ds_read_b128 v[28:31], v191 offset:39936
v_min_u32 v181, -2, v181
v_mfma_f32_16x16x32_bf16 v[96:99], a[224:227], v[48:51], v[96:99]
s_waitcnt lgkmcnt(6)
ds_read_b128 v[32:35], v191 offset:40960
v_min_u32 v182, -2, v182
v_mfma_f32_16x16x32_bf16 v[100:103], a[224:227], v[52:55], v[100:103]
s_waitcnt lgkmcnt(6)
ds_read_b128 v[36:39], v191 offset:41984
v_min_u32 v183, -2, v183
v_mfma_f32_16x16x32_bf16 v[104:107], a[224:227], v[56:59], v[104:107]
s_waitcnt lgkmcnt(6)
ds_read_b128 v[40:43], v191 offset:43008
global_store_dwordx4 v4, v[180:183], s[6:7] offset:1024 sc1
v_mfma_f32_16x16x32_bf16 v[108:111], a[224:227], v[60:63], v[108:111]
s_waitcnt vmcnt(58) lgkmcnt(6)
ds_read_b128 v[44:47], v191 offset:44032
s_add_u32 s6, s6, 2097152
s_addc_u32 s7, s7, 0
v_mfma_f32_16x16x32_bf16 v[96:99], a[228:231], v[16:19], v[96:99]
s_waitcnt lgkmcnt(6)
ds_read_b128 v[48:51], v191 offset:45056
s_waitcnt vmcnt(13)
v_max_u32 v12, v128, v129
v_mfma_f32_16x16x32_bf16 v[100:103], a[228:231], v[20:23], v[100:103]
s_waitcnt lgkmcnt(6)
ds_read_b128 v[52:55], v191 offset:46080
s_waitcnt vmcnt(12)
v_max3_u32 v12, v12, v130, v131
v_mfma_f32_16x16x32_bf16 v[104:107], a[228:231], v[24:27], v[104:107]
s_waitcnt lgkmcnt(6)
ds_read_b128 v[56:59], v191 offset:47104
s_waitcnt vmcnt(11)
v_max3_u32 v12, v12, v132, v133
v_mfma_f32_16x16x32_bf16 v[108:111], a[228:231], v[28:31], v[108:111]
s_waitcnt lgkmcnt(6)
ds_read_b128 v[60:63], v191 offset:48128
s_waitcnt vmcnt(10)
v_max3_u32 v12, v12, v134, v135
v_mfma_f32_16x16x32_bf16 v[96:99], a[232:235], v[32:35], v[96:99]
s_waitcnt lgkmcnt(6)
ds_read_b128 v[16:19], v5 offset:24576
v_cmp_eq_u32 vcc, -1, v12
s_nop 0
s_cmp_eq_u64 vcc, 0
s_cbranch_scc0 Lx_66_%=
Lr_67_%=:
v_mfma_f32_16x16x32_bf16 v[100:103], a[232:235], v[36:39], v[100:103]
s_waitcnt lgkmcnt(6)
v_mfma_f32_16x16x32_bf16 v[196:199], v[186:189], v[128:131], 0
ds_read_b128 v[20:23], v5 offset:25600
v_mfma_f32_16x16x32_bf16 v[104:107], a[232:235], v[40:43], v[104:107]
s_waitcnt lgkmcnt(6)
v_mfma_f32_16x16x32_bf16 v[196:199], v[184:187], v[128:131], v[196:199]
ds_read_b128 v[24:27], v5 offset:26624
v_mfma_f32_16x16x32_bf16 v[108:111], a[232:235], v[44:47], v[108:111]
s_waitcnt lgkmcnt(6)
v_mfma_f32_16x16x32_bf16 v[196:199], v[186:189], v[132:135], v[196:199]
ds_read_b128 v[28:31], v5 offset:27648
v_mfma_f32_16x16x32_bf16 v[96:99], a[236:239], v[48:51], v[96:99]
s_waitcnt lgkmcnt(6)
v_mfma_f32_16x16x32_bf16 v[196:199], v[184:187], v[132:135], v[196:199]
ds_read_b128 v[32:35], v5 offset:28672
v_mfma_f32_16x16x32_bf16 v[100:103], a[236:239], v[52:55], v[100:103]
s_waitcnt lgkmcnt(6)
ds_read_b128 v[36:39], v5 offset:29696
v_mfma_f32_16x16x32_bf16 v[104:107], a[236:239], v[56:59], v[104:107]
s_waitcnt lgkmcnt(6)
ds_read_b128 v[40:43], v5 offset:30720
v_mfma_f32_16x16x32_bf16 v[108:111], a[236:239], v[60:63], v[108:111]
s_waitcnt lgkmcnt(6)
ds_read_b128 v[44:47], v5 offset:31744
; column tiles 16..16 of the weight stream
v_mfma_f32_16x16x32_bf16 v[112:115], a[240:243], v[16:19], 0
s_waitcnt lgkmcnt(6)
ds_read_b128 v[48:51], v5 offset:32768
v_add_f32 v196, v196, v196 row_ror:8 row_mask:0xf bank_mask:0xf
v_mfma_f32_16x16x32_bf16 v[116:119], a[240:243], v[20:23], 0
s_waitcnt lgkmcnt(6)
ds_read_b128 v[52:55], v5 offset:33792
v_add_f32 v197, v197, v197 row_ror:8 row_mask:0xf bank_mask:0xf
v_mfma_f32_16x16x32_bf16 v[120:123], a[240:243], v[24:27], 0
s_waitcnt lgkmcnt(6)
ds_read_b128 v[56:59], v5 offset:34816
v_cvt_pk_bf16_f32 v176, v96, v97
v_mfma_f32_16x16x32_bf16 v[124:127], a[240:243], v[28:31], 0
s_waitcnt lgkmcnt(6)
ds_read_b128 v[60:63], v5 offset:35840
v_cvt_pk_bf16_f32 v177, v98, v99
v_mfma_f32_16x16x32_bf16 v[112:115], a[244:247], v[32:35], v[112:115]
s_waitcnt lgkmcnt(6)
ds_read_b128 v[16:19], v5 offset:36864
v_cvt_pk_bf16_f32 v178, v100, v101
v_mfma_f32_16x16x32_bf16 v[116:119], a[244:247], v[36:39], v[116:119]
s_waitcnt lgkmcnt(6)
ds_read_b128 v[20:23], v5 offset:37888
v_cvt_pk_bf16_f32 v179, v102, v103
v_mfma_f32_16x16x32_bf16 v[120:123], a[244:247], v[40:43], v[120:123]
s_waitcnt lgkmcnt(6)
ds_read_b128 v[24:27], v5 offset:38912
v_min_u32 v176, -2, v176
v_mfma_f32_16x16x32_bf16 v[124:127], a[244:247], v[44:47], v[124:127]
s_waitcnt lgkmcnt(6)
ds_read_b128 v[28:31], v5 offset:39936
v_min_u32 v177, -2, v177
v_mfma_f32_16x16x32_bf16 v[112:115], a[248:251], v[48:51], v[112:115]
s_waitcnt lgkmcnt(6)
ds_read_b128 v[32:35], v5 offset:40960
v_min_u32 v178, -2, v178
v_mfma_f32_16x16x32_bf16 v[116:119], a[248:251], v[52:55], v[116:119]
s_waitcnt lgkmcnt(6)
ds_read_b128 v[36:39], v5 offset:41984
v_min_u32 v179, -2, v179
v_mfma_f32_16x16x32_bf16 v[120:123], a[248:251], v[56:59], v[120:123]
s_waitcnt lgkmcnt(6)
ds_read_b128 v[40:43], v5 offset:43008
global_store_dwordx4 v4, v[176:179], s[6:7] sc1
v_mfma_f32_16x16x32_bf16 v[124:127], a[248:251], v[60:63], v[124:127]
s_waitcnt lgkmcnt(6)
ds_read_b128 v[44:47], v5 offset:44032
v_add_f32 v198, v198, v198 row_ror:8 row_mask:0xf bank_mask:0xf
v_mfma_f32_16x16x32_bf16 v[112:115], a[252:255], v[16:19], v[112:115]
s_waitcnt lgkmcnt(6)
ds_read_b128 v[48:51], v5 offset:45056
v_add_f32 v199, v199, v199 row_ror:8 row_mask:0xf bank_mask:0xf
v_mfma_f32_16x16x32_bf16 v[116:119], a[252:255], v[20:23], v[116:119]
s_waitcnt lgkmcnt(6)
v_mfma_f32_16x16x32_bf16 v[196:199], v[186:189], v[200:203], v[196:199]
ds_read_b128 v[52:55], v5 offset:46080
v_mfma_f32_16x16x32_bf16 v[120:123], a[252:255], v[24:27], v[120:123]
s_waitcnt lgkmcnt(6)
v_mfma_f32_16x16x32_bf16 v[196:199], v[184:187], v[200:203], v[196:199]
ds_read_b128 v[56:59], v5 offset:47104
v_mfma_f32_16x16x32_bf16 v[124:127], a[252:255], v[28:31], v[124:127]
s_waitcnt lgkmcnt(6)
ds_read_b128 v[60:63], v5 offset:48128
v_mfma_f32_16x16x32_bf16 v[112:115], v[224:227], v[32:35], v[112:115]
s_waitcnt lgkmcnt(6)
ds_read_b128 v[16:19], v191
v_mfma_f32_16x16x32_bf16 v[116:119], v[224:227], v[36:39], v[116:119]
s_waitcnt lgkmcnt(6)
ds_read_b128 v[20:23], v191 offset:1024
v_mfma_f32_16x16x32_bf16 v[120:123], v[224:227], v[40:43], v[120:123]
s_waitcnt lgkmcnt(6)
ds_read_b128 v[24:27], v191 offset:2048
v_cvt_pk_bf16_f32 v196, v196, v197
v_mfma_f32_16x16x32_bf16 v[124:127], v[224:227], v[44:47], v[124:127]
s_waitcnt lgkmcnt(6)
ds_read_b128 v[28:31], v191 offset:3072
v_cvt_pk_bf16_f32 v180, v104, v105
v_mfma_f32_16x16x32_bf16 v[112:115], v[228:231], v[48:51], v[112:115]
s_waitcnt lgkmcnt(6)
ds_read_b128 v[32:35], v191 offset:4096
v_cvt_pk_bf16_f32 v181, v106, v107
v_mfma_f32_16x16x32_bf16 v[116:119], v[228:231], v[52:55], v[116:119]
s_waitcnt lgkmcnt(6)
ds_read_b128 v[36:39], v191 offset:5120
v_cvt_pk_bf16_f32 v182, v108, v109
v_mfma_f32_16x16x32_bf16 v[120:123], v[228:231], v[56:59], v[120:123]
s_waitcnt lgkmcnt(6)
ds_read_b128 v[40:43], v191 offset:6144
v_cvt_pk_bf16_f32 v183, v110, v111
v_mfma_f32_16x16x32_bf16 v[124:127], v[228:231], v[60:63], v[124:127]
s_waitcnt lgkmcnt(6)
ds_read_b128 v[44:47], v191 offset:7168
v_min_u32 v180, -2, v180
v_mfma_f32_16x16x32_bf16 v[112:115], v[232:235], v[16:19], v[112:115]
s_waitcnt lgkmcnt(6)
ds_read_b128 v[48:51], v191 offset:8192
v_min_u32 v181, -2, v181
v_mfma_f32_16x16x32_bf16 v[116:119], v[232:235], v[20:23], v[116:119]
s_waitcnt lgkmcnt(6)
ds_read_b128 v[52:55], v191 offset:9216
v_min_u32 v182, -2, v182
v_mfma_f32_16x16x32_bf16 v[120:123], v[232:235], v[24:27], v[120:123]
s_waitcnt lgkmcnt(6)
ds_read_b128 v[56:59], v191 offset:10240
v_min_u32 v183, -2, v183
v_mfma_f32_16x16x32_bf16 v[124:127], v[232:235], v[28:31], v[124:127]
s_waitcnt lgkmcnt(6)
ds_read_b128 v[60:63], v191 offset:11264
global_store_dwordx4 v4, v[180:183], s[6:7] offset:1024 sc1
v_mfma_f32_16x16x32_bf16 v[112:115], v[236:239], v[32:35], v[112:115]
s_waitcnt lgkmcnt(6)
ds_read_b128 v[16:19], v191 offset:12288
s_add_u32 s6, s6, 2097152
s_addc_u32 s7, s7, 0
v_mfma_f32_16x16x32_bf16 v[116:119], v[236:239], v[36:39], v[116:119]
s_waitcnt lgkmcnt(6)
ds_read_b128 v[20:23], v191 offset:13312
v_cvt_pk_bf16_f32 v197, v198, v199
v_mfma_f32_16x16x32_bf16 v[120:123], v[236:239], v[40:43], v[120:123]
s_waitcnt lgkmcnt(6)
ds_read_b128 v[24:27], v191 offset:14336
s_mov_b32 exec_lo, 0x00ff00ff
s_mov_b32 exec_hi, 0x00ff00ff
global_store_dwordx2 v10, v[196:197], s[30:31]
s_mov_b64 exec, -1
v_mfma_f32_16x16x32_bf16 v[124:127], v[236:239], v[44:47], v[124:127]
s_waitcnt lgkmcnt(6)
ds_read_b128 v[28:31], v191 offset:15360
s_waitcnt vmcnt(12)
v_max_u32 v12, v136, v137
v_mfma_f32_16x16x32_bf16 v[112:115], v[240:243], v[48:51], v[112:115]
s_waitcnt lgkmcnt(6)
ds_read_b128 v[32:35], v191 offset:16384
v_max3_u32 v12, v12, v138, v139
v_mfma_f32_16x16x32_bf16 v[116:119], v[240:243], v[52:55], v[116:119]
s_waitcnt lgkmcnt(6)
ds_read_b128 v[36:39], v191 offset:17408
s_waitcnt vmcnt(11)
v_max3_u32 v12, v12, v140, v141
v_mfma_f32_16x16x32_bf16 v[120:123], v[240:243], v[56:59], v[120:123]
s_waitcnt lgkmcnt(6)
ds_read_b128 v[40:43], v191 offset:18432
v_max3_u32 v12, v12, v142, v143
v_mfma_f32_16x16x32_bf16 v[124:127], v[240:243], v[60:63], v[124:127]
s_waitcnt lgkmcnt(6)
ds_read_b128 v[44:47], v191 offset:19456
v_cmp_eq_u32 vcc, -1, v12
s_nop 0
s_cmp_eq_u64 vcc, 0
s_cbranch_scc0 Lx_70_%=
Lr_71_%=:
v_mfma_f32_16x16x32_bf16 v[112:115], v[244:247], v[16:19], v[112:115]
s_waitcnt lgkmcnt(6)
v_mfma_f32_16x16x32_bf16 v[80:83], v[186:189], v[136:139], v[80:83]
ds_read_b128 v[48:51], v191 offset:20480
v_mfma_f32_16x16x32_bf16 v[116:119], v[244:247], v[20:23], v[116:119]
s_waitcnt lgkmcnt(6)
v_mfma_f32_16x16x32_bf16 v[84:87], v[184:187], v[136:139], v[84:87]
ds_read_b128 v[52:55], v191 offset:21504
v_mfma_f32_16x16x32_bf16 v[120:123], v[244:247], v[24:27], v[120:123]
s_waitcnt lgkmcnt(6)
v_mfma_f32_16x16x32_bf16 v[88:91], v[186:189], v[140:143], v[88:91]
ds_read_b128 v[56:59], v191 offset:22528
v_mfma_f32_16x16x32_bf16 v[124:127], v[244:247], v[28:31], v[124:127]
s_waitcnt lgkmcnt(6)
v_mfma_f32_16x16x32_bf16 v[92:95], v[184:187], v[140:143], v[92:95]
ds_read_b128 v[60:63], v191 offset:23552
s_waitcnt vmcnt(8)
v_max_u32 v12, v144, v145
v_mfma_f32_16x16x32_bf16 v[112:115], v[248:251], v[32:35], v[112:115]
s_waitcnt lgkmcnt(6)
ds_read_b128 v[16:19], v191 offset:24576
v_max3_u32 v12, v12, v146, v147
v_mfma_f32_16x16x32_bf16 v[116:119], v[248:251], v[36:39], v[116:119]
s_waitcnt lgkmcnt(6)
ds_read_b128 v[20:23], v191 offset:25600
s_waitcnt vmcnt(7)
v_max3_u32 v12, v12, v148, v149
v_mfma_f32_16x16x32_bf16 v[120:123], v[248:251], v[40:43], v[120:123]
s_waitcnt lgkmcnt(6)
ds_read_b128 v[24:27], v191 offset:26624
v_max3_u32 v12, v12, v150, v151
v_mfma_f32_16x16x32_bf16 v[124:127], v[248:251], v[44:47], v[124:127]
s_waitcnt lgkmcnt(6)
ds_read_b128 v[28:31], v191 offset:27648
v_cmp_eq_u32 vcc, -1, v12
s_nop 0
s_cmp_eq_u64 vcc, 0
s_cbranch_scc0 Lx_74_%=
Lr_75_%=:
v_mfma_f32_16x16x32_bf16 v[112:115], v[252:255], v[48:51], v[112:115]
s_waitcnt lgkmcnt(6)
v_mfma_f32_16x16x32_bf16 v[80:83], v[186:189], v[144:147], v[80:83]
ds_read_b128 v[32:35], v191 offset:28672
v_mfma_f32_16x16x32_bf16 v[116:119], v[252:255], v[52:55], v[116:119]
s_waitcnt lgkmcnt(6)
v_mfma_f32_16x16x32_bf16 v[84:87], v[184:187], v[144:147], v[84:87]
ds_read_b128 v[36:39], v191 offset:29696
v_mfma_f32_16x16x32_bf16 v[120:123], v[252:255], v[56:59], v[120:123]
s_waitcnt lgkmcnt(6)
v_mfma_f32_16x16x32_bf16 v[88:91], v[186:189], v[148:151], v[88:91]
ds_read_b128 v[40:43], v191 offset:30720
v_mfma_f32_16x16x32_bf16 v[124:127], v[252:255], v[60:63], v[124:127]
s_waitcnt lgkmcnt(6)
v_mfma_f32_16x16x32_bf16 v[92:95], v[184:187], v[148:151], v[92:95]
ds_read_b128 v[44:47], v191 offset:31744
v_mfma_f32_16x16x32_bf16 v[112:115], v[208:211], v[16:19], v[112:115]
s_waitcnt lgkmcnt(6)
ds_read_b128 v[48:51], v191 offset:32768
v_mfma_f32_16x16x32_bf16 v[116:119], v[208:211], v[20:23], v[116:119]
s_waitcnt lgkmcnt(6)
ds_read_b128 v[52:55], v191 offset:33792
v_mfma_f32_16x16x32_bf16 v[120:123], v[208:211], v[24:27], v[120:123]
s_waitcnt lgkmcnt(6)
ds_read_b128 v[56:59], v191 offset:34816
v_mfma_f32_16x16x32_bf16 v[124:127], v[208:211], v[28:31], v[124:127]
s_waitcnt lgkmcnt(6)
ds_read_b128 v[60:63], v191 offset:35840
v_mfma_f32_16x16x32_bf16 v[112:115], v[212:215], v[32:35], v[112:115]
s_waitcnt lgkmcnt(6)
ds_read_b128 v[16:19], v191 offset:36864
v_mfma_f32_16x16x32_bf16 v[116:119], v[212:215], v[36:39], v[116:119]
s_waitcnt lgkmcnt(6)
ds_read_b128 v[20:23], v191 offset:37888
v_mfma_f32_16x16x32_bf16 v[120:123], v[212:215], v[40:43], v[120:123]
s_waitcnt lgkmcnt(6)
ds_read_b128 v[24:27], v191 offset:38912
v_mfma_f32_16x16x32_bf16 v[124:127], v[212:215], v[44:47], v[124:127]
s_waitcnt lgkmcnt(6)
ds_read_b128 v[28:31], v191 offset:39936
v_mfma_f32_16x16x32_bf16 v[112:115], v[216:219], v[48:51], v[112:115]
s_waitcnt lgkmcnt(6)
ds_read_b128 v[32:35], v191 offset:40960
v_mfma_f32_16x16x32_bf16 v[116:119], v[216:219], v[52:55], v[116:119]
s_waitcnt lgkmcnt(6)
ds_read_b128 v[36:39], v191 offset:41984
v_mfma_f32_16x16x32_bf16 v[120:123], v[216:219], v[56:59], v[120:123]
s_waitcnt lgkmcnt(6)
ds_read_b128 v[40:43], v191 offset:43008
v_mfma_f32_16x16x32_bf16 v[124:127], v[216:219], v[60:63], v[124:127]
s_waitcnt lgkmcnt(6)
ds_read_b128 v[44:47], v191 offset:44032
v_mfma_f32_16x16x32_bf16 v[112:115], v[220:223], v[16:19], v[112:115]
s_waitcnt lgkmcnt(6)
ds_read_b128 v[48:51], v191 offset:45056
v_mfma_f32_16x16x32_bf16 v[116:119], v[220:223], v[20:23], v[116:119]
s_waitcnt lgkmcnt(6)
ds_read_b128 v[52:55], v191 offset:46080
v_mfma_f32_16x16x32_bf16 v[120:123], v[220:223], v[24:27], v[120:123]
s_waitcnt lgkmcnt(6)
ds_read_b128 v[56:59], v191 offset:47104
v_mfma_f32_16x16x32_bf16 v[124:127], v[220:223], v[28:31], v[124:127]
s_waitcnt lgkmcnt(6)
ds_read_b128 v[60:63], v191 offset:48128
v_mfma_f32_16x16x32_bf16 v[112:115], a[0:3], v[32:35], v[112:115]
s_waitcnt lgkmcnt(6)
ds_read_b128 v[16:19], v5 offset:24576
v_mfma_f32_16x16x32_bf16 v[116:119], a[0:3], v[36:39], v[116:119]
s_waitcnt lgkmcnt(6)
ds_read_b128 v[20:23], v5 offset:25600
v_mfma_f32_16x16x32_bf16 v[120:123], a[0:3], v[40:43], v[120:123]
s_waitcnt lgkmcnt(6)
ds_read_b128 v[24:27], v5 offset:26624
v_mfma_f32_16x16x32_bf16 v[124:127], a[0:3], v[44:47], v[124:127]
s_waitcnt lgkmcnt(6)
ds_read_b128 v[28:31], v5 offset:27648
v_mfma_f32_16x16x32_bf16 v[112:115], a[4:7], v[48:51], v[112:115]
s_waitcnt lgkmcnt(6)
ds_read_b128 v[32:35], v5 offset:28672
v_mfma_f32_16x16x32_bf16 v[116:119], a[4:7], v[52:55], v[116:119]
s_waitcnt lgkmcnt(6)
ds_read_b128 v[36:39], v5 offset:29696
v_mfma_f32_16x16x32_bf16 v[120:123], a[4:7], v[56:59], v[120:123]
s_waitcnt lgkmcnt(6)
ds_read_b128 v[40:43], v5 offset:30720
v_mfma_f32_16x16x32_bf16 v[124:127], a[4:7], v[60:63], v[124:127]
s_waitcnt lgkmcnt(6)
ds_read_b128 v[44:47], v5 offset:31744
; column tiles 17..17 of the weight stream
v_mfma_f32_16x16x32_bf16 v[80:83], a[8:11], v[16:19], v[80:83]
s_waitcnt lgkmcnt(6)
ds_read_b128 v[48:51], v5 offset:32768
v_mfma_f32_16x16x32_bf16 v[84:87], a[8:11], v[20:23], v[84:87]
s_waitcnt lgkmcnt(6)
ds_read_b128 v[52:55], v5 offset:33792
v_mfma_f32_16x16x32_bf16 v[88:91], a[8:11], v[24:27], v[88:91]
s_waitcnt lgkmcnt(6)
)"
R"(ds_read_b128 v[56:59], v5 offset:34816
v_cvt_pk_bf16_f32 v176, v112, v113
v_mfma_f32_16x16x32_bf16 v[92:95], a[8:11], v[28:31], v[92:95]
s_waitcnt lgkmcnt(6)
ds_read_b128 v[60:63], v5 offset:35840
v_cvt_pk_bf16_f32 v177, v114, v115
v_mfma_f32_16x16x32_bf16 v[80:83], a[12:15], v[32:35], v[80:83]
s_waitcnt lgkmcnt(6)
ds_read_b128 v[16:19], v5 offset:36864
v_cvt_pk_bf16_f32 v178, v116, v117
v_mfma_f32_16x16x32_bf16 v[84:87], a[12:15], v[36:39], v[84:87]
s_waitcnt lgkmcnt(6)
ds_read_b128 v[20:23], v5 offset:37888
v_cvt_pk_bf16_f32 v179, v118, v119
v_mfma_f32_16x16x32_bf16 v[88:91], a[12:15], v[40:43], v[88:91]
s_waitcnt lgkmcnt(6)
ds_read_b128 v[24:27], v5 offset:38912
v_min_u32 v176, -2, v176
v_mfma_f32_16x16x32_bf16 v[92:95], a[12:15], v[44:47], v[92:95]
s_waitcnt lgkmcnt(6)
ds_read_b128 v[28:31], v5 offset:39936
v_min_u32 v177, -2, v177
v_mfma_f32_16x16x32_bf16 v[80:83], a[16:19], v[48:51], v[80:83]
s_waitcnt lgkmcnt(6)
ds_read_b128 v[32:35], v5 offset:40960
v_min_u32 v178, -2, v178
v_mfma_f32_16x16x32_bf16 v[84:87], a[16:19], v[52:55], v[84:87]
s_waitcnt lgkmcnt(6)
ds_read_b128 v[36:39], v5 offset:41984
v_min_u32 v179, -2, v179
v_mfma_f32_16x16x32_bf16 v[88:91], a[16:19], v[56:59], v[88:91]
s_waitcnt lgkmcnt(6)
ds_read_b128 v[40:43], v5 offset:43008
global_store_dwordx4 v4, v[176:179], s[6:7] sc1
v_mfma_f32_16x16x32_bf16 v[92:95], a[16:19], v[60:63], v[92:95]
s_waitcnt lgkmcnt(6)
ds_read_b128 v[44:47], v5 offset:44032
v_cvt_pk_bf16_f32 v180, v120, v121
v_mfma_f32_16x16x32_bf16 v[80:83], a[20:23], v[16:19], v[80:83]
s_waitcnt lgkmcnt(6)
ds_read_b128 v[48:51], v5 offset:45056
v_cvt_pk_bf16_f32 v181, v122, v123
v_mfma_f32_16x16x32_bf16 v[84:87], a[20:23], v[20:23], v[84:87]
s_waitcnt lgkmcnt(6)
ds_read_b128 v[52:55], v5 offset:46080
v_cvt_pk_bf16_f32 v182, v124, v125
v_mfma_f32_16x16x32_bf16 v[88:91], a[20:23], v[24:27], v[88:91]
s_waitcnt lgkmcnt(6)
ds_read_b128 v[56:59], v5 offset:47104
v_cvt_pk_bf16_f32 v183, v126, v127
v_mfma_f32_16x16x32_bf16 v[92:95], a[20:23], v[28:31], v[92:95]
s_waitcnt lgkmcnt(6)
ds_read_b128 v[60:63], v5 offset:48128
v_min_u32 v180, -2, v180
v_mfma_f32_16x16x32_bf16 v[80:83], a[24:27], v[32:35], v[80:83]
s_waitcnt lgkmcnt(6)
ds_read_b128 v[16:19], v191
v_min_u32 v181, -2, v181
v_mfma_f32_16x16x32_bf16 v[84:87], a[24:27], v[36:39], v[84:87]
s_waitcnt lgkmcnt(6)
ds_read_b128 v[20:23], v191 offset:1024
v_min_u32 v182, -2, v182
v_mfma_f32_16x16x32_bf16 v[88:91], a[24:27], v[40:43], v[88:91]
s_waitcnt lgkmcnt(6)
ds_read_b128 v[24:27], v191 offset:2048
v_min_u32 v183, -2, v183
v_mfma_f32_16x16x32_bf16 v[92:95], a[24:27], v[44:47], v[92:95]
s_waitcnt lgkmcnt(6)
s_sub_u32 s96, s93, 3
s_and_b32 s96, s96, 7
s_lshl_b32 s96, s96, 13
s_add_u32 s96, s96, 6291456
buffer_load_dwordx4 v[152:155], v4, s[52:55], s96 offen sc0 sc1
buffer_load_dwordx4 v[156:159], v4, s[52:55], s96 offen offset:1024 sc0 sc1
ds_read_b128 v[28:31], v191 offset:3072
v_mfma_f32_16x16x32_bf16 v[80:83], a[28:31], v[48:51], v[80:83]
s_waitcnt lgkmcnt(6)
ds_read_b128 v[32:35], v191 offset:4096
global_store_dwordx4 v4, v[180:183], s[6:7] offset:1024 sc1
v_mfma_f32_16x16x32_bf16 v[84:87], a[28:31], v[52:55], v[84:87]
s_waitcnt lgkmcnt(6)
ds_read_b128 v[36:39], v191 offset:5120
s_add_u32 s6, s6, 2097152
s_addc_u32 s7, s7, 0
v_mfma_f32_16x16x32_bf16 v[88:91], a[28:31], v[56:59], v[88:91]
s_waitcnt lgkmcnt(6)
ds_read_b128 v[40:43], v191 offset:6144
v_mfma_f32_16x16x32_bf16 v[92:95], a[28:31], v[60:63], v[92:95]
LW1_7_%=:
s_cbranch_execz LWEND1_%=
s_waitcnt lgkmcnt(6)
ds_read_b128 v[44:47], v191 offset:7168
v_mfma_f32_16x16x32_bf16 v[80:83], a[32:35], v[16:19], v[80:83]
s_waitcnt lgkmcnt(6)
ds_read_b128 v[48:51], v191 offset:8192
v_mfma_f32_16x16x32_bf16 v[84:87], a[32:35], v[20:23], v[84:87]
s_waitcnt lgkmcnt(6)
ds_read_b128 v[52:55], v191 offset:9216
v_mfma_f32_16x16x32_bf16 v[88:91], a[32:35], v[24:27], v[88:91]
s_waitcnt lgkmcnt(6)
ds_read_b128 v[56:59], v191 offset:10240
v_mfma_f32_16x16x32_bf16 v[92:95], a[32:35], v[28:31], v[92:95]
s_waitcnt lgkmcnt(6)
ds_read_b128 v[60:63], v191 offset:11264
v_mfma_f32_16x16x32_bf16 v[80:83], a[36:39], v[32:35], v[80:83]
s_waitcnt lgkmcnt(6)
ds_read_b128 v[16:19], v191 offset:12288
v_mfma_f32_16x16x32_bf16 v[84:87], a[36:39], v[36:39], v[84:87]
s_waitcnt lgkmcnt(6)
ds_read_b128 v[20:23], v191 offset:13312
v_mfma_f32_16x16x32_bf16 v[88:91], a[36:39], v[40:43], v[88:91]
s_waitcnt lgkmcnt(6)
ds_read_b128 v[24:27], v191 offset:14336
v_mfma_f32_16x16x32_bf16 v[92:95], a[36:39], v[44:47], v[92:95]
s_waitcnt lgkmcnt(6)
ds_read_b128 v[28:31], v191 offset:15360
v_mfma_f32_16x16x32_bf16 v[80:83], a[40:43], v[48:51], v[80:83]
s_waitcnt lgkmcnt(6)
ds_read_b128 v[32:35], v191 offset:16384
v_mfma_f32_16x16x32_bf16 v[84:87], a[40:43], v[52:55], v[84:87]
s_waitcnt lgkmcnt(6)
ds_read_b128 v[36:39], v191 offset:17408
v_mfma_f32_16x16x32_bf16 v[88:91], a[40:43], v[56:59], v[88:91]
s_waitcnt lgkmcnt(6)
ds_read_b128 v[40:43], v191 offset:18432
v_mfma_f32_16x16x32_bf16 v[92:95], a[40:43], v[60:63], v[92:95]
s_waitcnt lgkmcnt(6)
ds_read_b128 v[44:47], v191 offset:19456
v_mfma_f32_16x16x32_bf16 v[80:83], a[44:47], v[16:19], v[80:83]
s_waitcnt lgkmcnt(6)
ds_read_b128 v[48:51], v191 offset:20480
v_mfma_f32_16x16x32_bf16 v[84:87], a[44:47], v[20:23], v[84:87]
s_waitcnt lgkmcnt(6)
ds_read_b128 v[52:55], v191 offset:21504
v_mfma_f32_16x16x32_bf16 v[88:91], a[44:47], v[24:27], v[88:91]
s_waitcnt lgkmcnt(6)
ds_read_b128 v[56:59], v191 offset:22528
v_mfma_f32_16x16x32_bf16 v[92:95], a[44:47], v[28:31], v[92:95]
s_waitcnt lgkmcnt(6)
s_sub_u32 s96, s93, 4
s_and_b32 s96, s96, 7
s_lshl_b32 s96, s96, 13
s_add_u32 s96, s96, 8388608
buffer_load_dwordx4 v[128:131], v4, s[52:55], s96 offen sc0 sc1
buffer_load_dwordx4 v[132:135], v4, s[52:55], s96 offen offset:1024 sc0 sc1
ds_read_b128 v[60:63], v191 offset:23552
v_mfma_f32_16x16x32_bf16 v[80:83], a[48:51], v[32:35], v[80:83]
s_waitcnt lgkmcnt(6)
ds_read_b128 v[16:19], v191 offset:24576
v_mfma_f32_16x16x32_bf16 v[84:87], a[48:51], v[36:39], v[84:87]
s_waitcnt lgkmcnt(6)
ds_read_b128 v[20:23], v191 offset:25600
v_mfma_f32_16x16x32_bf16 v[88:91], a[48:51], v[40:43], v[88:91]
s_waitcnt lgkmcnt(6)
ds_read_b128 v[24:27], v191 offset:26624
v_mfma_f32_16x16x32_bf16 v[92:95], a[48:51], v[44:47], v[92:95]
s_waitcnt lgkmcnt(6)
ds_read_b128 v[28:31], v191 offset:27648
v_mfma_f32_16x16x32_bf16 v[80:83], a[52:55], v[48:51], v[80:83]
s_waitcnt lgkmcnt(6)
ds_read_b128 v[32:35], v191 offset:28672
v_mfma_f32_16x16x32_bf16 v[84:87], a[52:55], v[52:55], v[84:87]
s_waitcnt lgkmcnt(6)
ds_read_b128 v[36:39], v191 offset:29696
v_mfma_f32_16x16x32_bf16 v[88:91], a[52:55], v[56:59], v[88:91]
s_waitcnt lgkmcnt(6)
ds_read_b128 v[40:43], v191 offset:30720
v_mfma_f32_16x16x32_bf16 v[92:95], a[52:55], v[60:63], v[92:95]
s_waitcnt lgkmcnt(6)
ds_read_b128 v[44:47], v191 offset:31744
v_mfma_f32_16x16x32_bf16 v[80:83], a[56:59], v[16:19], v[80:83]
s_waitcnt lgkmcnt(6)
ds_read_b128 v[48:51], v191 offset:32768
v_mfma_f32_16x16x32_bf16 v[84:87], a[56:59], v[20:23], v[84:87]
s_waitcnt lgkmcnt(6)
ds_read_b128 v[52:55], v191 offset:33792
v_mfma_f32_16x16x32_bf16 v[88:91], a[56:59], v[24:27], v[88:91]
s_waitcnt lgkmcnt(6)
ds_read_b128 v[56:59], v191 offset:34816
v_mfma_f32_16x16x32_bf16 v[92:95], a[56:59], v[28:31], v[92:95]
s_waitcnt lgkmcnt(6)
ds_read_b128 v[60:63], v191 offset:35840
v_mfma_f32_16x16x32_bf16 v[80:83], a[60:63], v[32:35], v[80:83]
s_waitcnt lgkmcnt(6)
ds_read_b128 v[16:19], v191 offset:36864
v_mfma_f32_16x16x32_bf16 v[84:87], a[60:63], v[36:39], v[84:87]
s_waitcnt lgkmcnt(6)
ds_read_b128 v[20:23], v191 offset:37888
v_mfma_f32_16x16x32_bf16 v[88:91], a[60:63], v[40:43], v[88:91]
s_waitcnt lgkmcnt(6)
ds_read_b128 v[24:27], v191 offset:38912
v_mfma_f32_16x16x32_bf16 v[92:95], a[60:63], v[44:47], v[92:95]
s_waitcnt lgkmcnt(6)
ds_read_b128 v[28:31], v191 offset:39936
v_mfma_f32_16x16x32_bf16 v[80:83], a[64:67], v[48:51], v[80:83]
s_waitcnt lgkmcnt(6)
ds_read_b128 v[32:35], v191 offset:40960
v_mfma_f32_16x16x32_bf16 v[84:87], a[64:67], v[52:55], v[84:87]
s_waitcnt lgkmcnt(6)
ds_read_b128 v[36:39], v191 offset:41984
v_mfma_f32_16x16x32_bf16 v[88:91], a[64:67], v[56:59], v[88:91]
s_waitcnt lgkmcnt(6)
ds_read_b128 v[40:43], v191 offset:43008
v_mfma_f32_16x16x32_bf16 v[92:95], a[64:67], v[60:63], v[92:95]
s_waitcnt lgkmcnt(6)
s_sub_u32 s96, s93, 5
s_and_b32 s96, s96, 7
s_lshl_b32 s96, s96, 13
s_add_u32 s96, s96, 10485760
buffer_load_dwordx4 v[136:139], v4, s[52:55], s96 offen sc0 sc1
buffer_load_dwordx4 v[140:143], v4, s[52:55], s96 offen offset:1024 sc0 sc1
ds_read_b128 v[44:47], v191 offset:44032
v_mfma_f32_16x16x32_bf16 v[80:83], a[68:71], v[16:19], v[80:83]
s_waitcnt lgkmcnt(6)
ds_read_b128 v[48:51], v191 offset:45056
v_mfma_f32_16x16x32_bf16 v[84:87], a[68:71], v[20:23], v[84:87]
s_waitcnt lgkmcnt(6)
ds_read_b128 v[52:55], v191 offset:46080
v_mfma_f32_16x16x32_bf16 v[88:91], a[68:71], v[24:27], v[88:91]
s_waitcnt lgkmcnt(6)
ds_read_b128 v[56:59], v191 offset:47104
v_mfma_f32_16x16x32_bf16 v[92:95], a[68:71], v[28:31], v[92:95]
s_waitcnt lgkmcnt(6)
ds_read_b128 v[60:63], v191 offset:48128
v_mfma_f32_16x16x32_bf16 v[80:83], a[72:75], v[32:35], v[80:83]
s_waitcnt lgkmcnt(6)
v_mfma_f32_16x16x32_bf16 v[84:87], a[72:75], v[36:39], v[84:87]
s_waitcnt lgkmcnt(5)
v_mfma_f32_16x16x32_bf16 v[88:91], a[72:75], v[40:43], v[88:91]
s_waitcnt lgkmcnt(4)
v_mfma_f32_16x16x32_bf16 v[92:95], a[72:75], v[44:47], v[92:95]
s_waitcnt lgkmcnt(3)
v_mfma_f32_16x16x32_bf16 v[80:83], a[76:79], v[48:51], v[80:83]
s_waitcnt lgkmcnt(2)
v_mfma_f32_16x16x32_bf16 v[84:87], a[76:79], v[52:55], v[84:87]
s_waitcnt lgkmcnt(1)
v_mfma_f32_16x16x32_bf16 v[88:91], a[76:79], v[56:59], v[88:91]
s_waitcnt lgkmcnt(0)
v_mfma_f32_16x16x32_bf16 v[92:95], a[76:79], v[60:63], v[92:95]
; the last partial sums, then the output tile leaves through LDS as whole 128-B rows
s_sub_u32 s96, s93, 6
s_and_b32 s96, s96, 7
s_lshl_b32 s96, s96, 13
s_add_u32 s96, s96, 12582912
buffer_load_dwordx4 v[144:147], v4, s[52:55], s96 offen sc0 sc1
buffer_load_dwordx4 v[148:151], v4, s[52:55], s96 offen offset:1024 sc0 sc1
s_sub_u32 s96, s93, 7
s_and_b32 s96, s96, 7
s_lshl_b32 s96, s96, 13
s_add_u32 s96, s96, 14680064
buffer_load_dwordx4 v[112:115], v4, s[52:55], s96 offen sc0 sc1
buffer_load_dwordx4 v[116:119], v4, s[52:55], s96 offen offset:1024 sc0 sc1
s_waitcnt vmcnt(10)
v_max_u32 v12, v152, v153
v_max3_u32 v12, v12, v154, v155
s_waitcnt vmcnt(9)
v_max3_u32 v12, v12, v156, v157
v_max3_u32 v12, v12, v158, v159
v_cmp_eq_u32 vcc, -1, v12
s_nop 0
s_cmp_eq_u64 vcc, 0
s_cbranch_scc1 Lcd_76_%=
s_mov_b32 s97, 32768
Lcp_77_%=:
s_sub_u32 s96, s93, 3
s_and_b32 s96, s96, 7
s_lshl_b32 s96, s96, 13
s_add_u32 s96, s96, 6291456
buffer_load_dwordx4 v[152:155], v4, s[52:55], s96 offen sc0 sc1
buffer_load_dwordx4 v[156:159], v4, s[52:55], s96 offen offset:1024 sc0 sc1
s_waitcnt vmcnt(1)
v_max_u32 v12, v152, v153
v_max3_u32 v12, v12, v154, v155
s_waitcnt vmcnt(0)
v_max3_u32 v12, v12, v156, v157
v_max3_u32 v12, v12, v158, v159
v_cmp_eq_u32 vcc, -1, v12
s_nop 0
s_cmp_eq_u64 vcc, 0
s_cbranch_scc1 Lcd_76_%=
s_sub_u32 s97, s97, 1
s_cmp_lg_u32 s97, 0
s_cbranch_scc1 Lcp_77_%=
Lcd_76_%=:
v_mfma_f32_16x16x32_bf16 v[80:83], v[186:189], v[152:155], v[80:83]
v_mfma_f32_16x16x32_bf16 v[84:87], v[184:187], v[152:155], v[84:87]
v_mfma_f32_16x16x32_bf16 v[88:91], v[186:189], v[156:159], v[88:91]
v_mfma_f32_16x16x32_bf16 v[92:95], v[184:187], v[156:159], v[92:95]
s_waitcnt vmcnt(7)
v_max_u32 v12, v128, v129
v_max3_u32 v12, v12, v130, v131
s_waitcnt vmcnt(6)
v_max3_u32 v12, v12, v132, v133
v_max3_u32 v12, v12, v134, v135
v_cmp_eq_u32 vcc, -1, v12
s_nop 0
s_cmp_eq_u64 vcc, 0
s_cbranch_scc1 Lcd_78_%=
s_mov_b32 s97, 32768
Lcp_79_%=:
s_sub_u32 s96, s93, 4
s_and_b32 s96, s96, 7
s_lshl_b32 s96, s96, 13
s_add_u32 s96, s96, 8388608
buffer_load_dwordx4 v[128:131], v4, s[52:55], s96 offen sc0 sc1
buffer_load_dwordx4 v[132:135], v4, s[52:55], s96 offen offset:1024 sc0 sc1
s_waitcnt vmcnt(1)
v_max_u32 v12, v128, v129
v_max3_u32 v12, v12, v130, v131
s_waitcnt vmcnt(0)
v_max3_u32 v12, v12, v132, v133
v_max3_u32 v12, v12, v134, v135
v_cmp_eq_u32 vcc, -1, v12
s_nop 0
s_cmp_eq_u64 vcc, 0
s_cbranch_scc1 Lcd_78_%=
s_sub_u32 s97, s97, 1
s_cmp_lg_u32 s97, 0
s_cbranch_scc1 Lcp_79_%=
Lcd_78_%=:
v_mfma_f32_16x16x32_bf16 v[80:83], v[186:189], v[128:131], v[80:83]
v_mfma_f32_16x16x32_bf16 v[84:87], v[184:187], v[128:131], v[84:87]
v_mfma_f32_16x16x32_bf16 v[88:91], v[186:189], v[132:135], v[88:91]
v_mfma_f32_16x16x32_bf16 v[92:95], v[184:187], v[132:135], v[92:95]
s_waitcnt vmcnt(5)
v_max_u32 v12, v136, v137
v_max3_u32 v12, v12, v138, v139
s_waitcnt vmcnt(4)
v_max3_u32 v12, v12, v140, v141
v_max3_u32 v12, v12, v142, v143
v_cmp_eq_u32 vcc, -1, v12
s_nop 0
s_cmp_eq_u64 vcc, 0
s_cbranch_scc1 Lcd_80_%=
s_mov_b32 s97, 32768
Lcp_81_%=:
s_sub_u32 s96, s93, 5
s_and_b32 s96, s96, 7
s_lshl_b32 s96, s96, 13
s_add_u32 s96, s96, 10485760
buffer_load_dwordx4 v[136:139], v4, s[52:55], s96 offen sc0 sc1
buffer_load_dwordx4 v[140:143], v4, s[52:55], s96 offen offset:1024 sc0 sc1
s_waitcnt vmcnt(1)
v_max_u32 v12, v136, v137
v_max3_u32 v12, v12, v138, v139
s_waitcnt vmcnt(0)
v_max3_u32 v12, v12, v140, v141
v_max3_u32 v12, v12, v142, v143
v_cmp_eq_u32 vcc, -1, v12
s_nop 0
s_cmp_eq_u64 vcc, 0
s_cbranch_scc1 Lcd_80_%=
s_sub_u32 s97, s97, 1
s_cmp_lg_u32 s97, 0
s_cbranch_scc1 Lcp_81_%=
Lcd_80_%=:
v_mfma_f32_16x16x32_bf16 v[80:83], v[186:189], v[136:139], v[80:83]
v_mfma_f32_16x16x32_bf16 v[84:87], v[184:187], v[136:139], v[84:87]
v_mfma_f32_16x16x32_bf16 v[88:91], v[186:189], v[140:143], v[88:91]
v_mfma_f32_16x16x32_bf16 v[92:95], v[184:187], v[140:143], v[92:95]
s_waitcnt vmcnt(3)
v_max_u32 v12, v144, v145
v_max3_u32 v12, v12, v146, v147
s_waitcnt vmcnt(2)
v_max3_u32 v12, v12, v148, v149
v_max3_u32 v12, v12, v150, v151
v_cmp_eq_u32 vcc, -1, v12
s_nop 0
s_cmp_eq_u64 vcc, 0
s_cbranch_scc1 Lcd_82_%=
s_mov_b32 s97, 32768
Lcp_83_%=:
s_sub_u32 s96, s93, 6
s_and_b32 s96, s96, 7
s_lshl_b32 s96, s96, 13
s_add_u32 s96, s96, 12582912
buffer_load_dwordx4 v[144:147], v4, s[52:55], s96 offen sc0 sc1
buffer_load_dwordx4 v[148:151], v4, s[52:55], s96 offen offset:1024 sc0 sc1
s_waitcnt vmcnt(1)
v_max_u32 v12, v144, v145
v_max3_u32 v12, v12, v146, v147
s_waitcnt vmcnt(0)
v_max3_u32 v12, v12, v148, v149
v_max3_u32 v12, v12, v150, v151
v_cmp_eq_u32 vcc, -1, v12
s_nop 0
s_cmp_eq_u64 vcc, 0
s_cbranch_scc1 Lcd_82_%=
s_sub_u32 s97, s97, 1
s_cmp_lg_u32 s97, 0
s_cbranch_scc1 Lcp_83_%=
Lcd_82_%=:
v_mfma_f32_16x16x32_bf16 v[80:83], v[186:189], v[144:147], v[80:83]
v_mfma_f32_16x16x32_bf16 v[84:87], v[184:187], v[144:147], v[84:87]
v_mfma_f32_16x16x32_bf16 v[88:91], v[186:189], v[148:151], v[88:91]
v_mfma_f32_16x16x32_bf16 v[92:95], v[184:187], v[148:151], v[92:95]
s_waitcnt vmcnt(1)
v_max_u32 v12, v112, v113
v_max3_u32 v12, v12, v114, v115
s_waitcnt vmcnt(0)
)"
R"(v_max3_u32 v12, v12, v116, v117
v_max3_u32 v12, v12, v118, v119
v_cmp_eq_u32 vcc, -1, v12
s_nop 0
s_cmp_eq_u64 vcc, 0
s_cbranch_scc1 Lcd_84_%=
s_mov_b32 s97, 32768
Lcp_85_%=:
s_sub_u32 s96, s93, 7
s_and_b32 s96, s96, 7
s_lshl_b32 s96, s96, 13
s_add_u32 s96, s96, 14680064
buffer_load_dwordx4 v[112:115], v4, s[52:55], s96 offen sc0 sc1
buffer_load_dwordx4 v[116:119], v4, s[52:55], s96 offen offset:1024 sc0 sc1
s_waitcnt vmcnt(1)
v_max_u32 v12, v112, v113
v_max3_u32 v12, v12, v114, v115
s_waitcnt vmcnt(0)
v_max3_u32 v12, v12, v116, v117
v_max3_u32 v12, v12, v118, v119
v_cmp_eq_u32 vcc, -1, v12
s_nop 0
s_cmp_eq_u64 vcc, 0
s_cbranch_scc1 Lcd_84_%=
s_sub_u32 s97, s97, 1
s_cmp_lg_u32 s97, 0
s_cbranch_scc1 Lcp_85_%=
Lcd_84_%=:
v_mfma_f32_16x16x32_bf16 v[80:83], v[186:189], v[112:115], v[80:83]
v_mfma_f32_16x16x32_bf16 v[84:87], v[184:187], v[112:115], v[84:87]
v_mfma_f32_16x16x32_bf16 v[88:91], v[186:189], v[116:119], v[88:91]
v_mfma_f32_16x16x32_bf16 v[92:95], v[184:187], v[116:119], v[92:95]
s_nop 4
v_cvt_pk_bf16_f32 v176, v80, v81
v_cvt_pk_bf16_f32 v177, v82, v83
ds_write_b64 v206, v[176:177]
v_cvt_pk_bf16_f32 v178, v84, v85
v_cvt_pk_bf16_f32 v179, v86, v87
ds_write_b64 v206, v[178:179] offset:2304
v_cvt_pk_bf16_f32 v180, v88, v89
v_cvt_pk_bf16_f32 v181, v90, v91
ds_write_b64 v206, v[180:181] offset:4608
v_cvt_pk_bf16_f32 v182, v92, v93
v_cvt_pk_bf16_f32 v183, v94, v95
ds_write_b64 v206, v[182:183] offset:6912
s_waitcnt lgkmcnt(0)
s_barrier
ds_read_b128 v[176:179], v192
ds_read_b128 v[180:183], v192 offset:1152
s_waitcnt lgkmcnt(1)
global_store_dwordx4 v193, v[176:179], s[60:61]
s_waitcnt lgkmcnt(0)
global_store_dwordx4 v193, v[180:183], s[4:5]
s_endpgm
; ===== rare paths
Lx_20_%=:
s_mov_b32 s97, 32768
Lpf_18_%=:
global_load_dwordx4 v[176:179], v13, s[50:51] sc0 sc1
global_load_dwordx4 v[180:183], v13, s[50:51] offset:16 sc0 sc1
s_waitcnt vmcnt(1)
v_xor_b32 v176, s40, v176
v_xor_b32 v177, s41, v177
v_xor_b32 v178, s40, v178
v_xor_b32 v179, s41, v179
s_waitcnt vmcnt(0)
v_xor_b32 v180, s40, v180
v_xor_b32 v181, s41, v181
v_xor_b32 v182, s40, v182
v_xor_b32 v183, s41, v183
v_or_b32 v163, v176, v177
v_or3_b32 v163, v163, v178, v179
v_or3_b32 v163, v163, v180, v181
v_or3_b32 v163, v163, v182, v183
v_cmp_ne_u32 vcc, 0, v163
s_nop 0
s_cmp_eq_u64 vcc, 0
s_cbranch_scc1 Lr_21_%=
s_sub_u32 s97, s97, 1
s_cmp_lg_u32 s97, 0
s_cbranch_scc1 Lpf_18_%=
s_branch Lr_21_%=
Lx_22_%=:
s_nop 15
s_nop 3
v_mov_b32 v166, v4
v_mov_b32 v167, v7
v_mov_b32 v168, v10
v_mov_b32 v169, 0xff800000
s_mov_b32 s96, 16
s_mov_b32 s98, 2432
Lfbl_24_%=:
buffer_load_dwordx2 v[132:133], v168, s[52:55], 0 offen sc1
buffer_load_dwordx4 v[120:123], v166, s[52:55], 0 offen sc1
s_waitcnt vmcnt(1)
v_mfma_f32_32x32x8_bf16 v[16:31], v[132:133], v[144:145], 0
s_waitcnt vmcnt(0)
v_mfma_f32_32x32x16_bf16 v[16:31], v[120:123], v[136:139], v[16:31]
s_nop 11
v_max3_f32 v169, v16, v17, v169
v_max3_f32 v169, v169, v18, v19
v_max3_f32 v169, v169, v20, v21
v_max3_f32 v169, v169, v22, v23
v_max3_f32 v169, v169, v24, v25
v_max3_f32 v169, v169, v26, v27
v_max3_f32 v169, v169, v28, v29
v_max3_f32 v169, v169, v30, v31
buffer_load_dwordx4 v[120:123], v166, s[52:55], 0 offen offset:1024 sc1
v_mfma_f32_32x32x8_bf16 v[16:31], v[132:133], v[146:147], 0
s_waitcnt vmcnt(0)
v_mfma_f32_32x32x16_bf16 v[16:31], v[120:123], v[136:139], v[16:31]
s_nop 11
v_max3_f32 v169, v16, v17, v169
v_max3_f32 v169, v169, v18, v19
v_max3_f32 v169, v169, v20, v21
v_max3_f32 v169, v169, v22, v23
v_max3_f32 v169, v169, v24, v25
v_max3_f32 v169, v169, v26, v27
v_max3_f32 v169, v169, v28, v29
v_max3_f32 v169, v169, v30, v31
buffer_load_dwordx4 v[120:123], v166, s[52:55], 0 offen offset:2048 sc1
v_mfma_f32_32x32x8_bf16 v[16:31], v[132:133], v[148:149], 0
s_waitcnt vmcnt(0)
v_mfma_f32_32x32x16_bf16 v[16:31], v[120:123], v[136:139], v[16:31]
s_nop 11
v_max3_f32 v169, v16, v17, v169
v_max3_f32 v169, v169, v18, v19
v_max3_f32 v169, v169, v20, v21
v_max3_f32 v169, v169, v22, v23
v_max3_f32 v169, v169, v24, v25
v_max3_f32 v169, v169, v26, v27
v_max3_f32 v169, v169, v28, v29
v_max3_f32 v169, v169, v30, v31
buffer_load_dwordx4 v[120:123], v166, s[52:55], 0 offen offset:3072 sc1
v_mfma_f32_32x32x8_bf16 v[16:31], v[132:133], v[150:151], 0
s_waitcnt vmcnt(0)
v_mfma_f32_32x32x16_bf16 v[16:31], v[120:123], v[136:139], v[16:31]
s_nop 11
v_max3_f32 v169, v16, v17, v169
v_max3_f32 v169, v169, v18, v19
v_max3_f32 v169, v169, v20, v21
v_max3_f32 v169, v169, v22, v23
v_max3_f32 v169, v169, v24, v25
v_max3_f32 v169, v169, v26, v27
v_max3_f32 v169, v169, v28, v29
v_max3_f32 v169, v169, v30, v31
s_and_b32 s97, s96, 1
s_cmp_eq_u32 s97, 0
s_mov_b32 s97, 14848
s_cselect_b32 s97, 4096, s97
v_add_u32 v166, s97, v166
s_mov_b32 s97, 14080
s_cselect_b32 s97, 4864, s97
v_add_u32 v167, s97, v167
s_mov_b32 s97, 18432
s_cselect_b32 s97, 512, s97
v_add_u32 v168, s97, v168
s_nop 15
s_nop 3
s_sub_u32 s96, s96, 1
s_cmp_lg_u32 s96, 0
s_cbranch_scc1 Lfbl_24_%=
v_mov_b32 v170, v169
s_nop 1
v_permlane32_swap_b32 v169, v170
v_max_f32 v169, v169, v170
v_pk_mul_f32 v[48:49], v[168:169], -1.0 op_sel:[1,0] op_sel_hi:[1,0]
v_pk_mul_f32 v[50:51], v[168:169], -1.0 op_sel:[1,0] op_sel_hi:[1,0]
v_pk_mul_f32 v[52:53], v[168:169], -1.0 op_sel:[1,0] op_sel_hi:[1,0]
v_pk_mul_f32 v[54:55], v[168:169], -1.0 op_sel:[1,0] op_sel_hi:[1,0]
v_pk_mul_f32 v[56:57], v[168:169], -1.0 op_sel:[1,0] op_sel_hi:[1,0]
v_pk_mul_f32 v[58:59], v[168:169], -1.0 op_sel:[1,0] op_sel_hi:[1,0]
v_pk_mul_f32 v[60:61], v[168:169], -1.0 op_sel:[1,0] op_sel_hi:[1,0]
v_pk_mul_f32 v[62:63], v[168:169], -1.0 op_sel:[1,0] op_sel_hi:[1,0]
v_mov_b32 v166, v4
v_mov_b32 v167, v7
v_mov_b32 v168, v10
v_mov_b32 v80, 0
v_mov_b32 v81, 0
v_mov_b32 v82, 0
v_mov_b32 v83, 0
v_mov_b32 v84, 0
v_mov_b32 v85, 0
v_mov_b32 v86, 0
v_mov_b32 v87, 0
v_mov_b32 v88, 0
v_mov_b32 v89, 0
v_mov_b32 v90, 0
v_mov_b32 v91, 0
v_mov_b32 v92, 0
v_mov_b32 v93, 0
v_mov_b32 v94, 0
v_mov_b32 v95, 0
s_mov_b32 s96, 16
s_mov_b32 s98, 2432
Lfbl_25_%=:
buffer_load_dwordx2 v[132:133], v168, s[52:55], 0 offen sc1
buffer_load_dwordx4 v[120:123], v166, s[52:55], 0 offen sc1
buffer_load_dwordx4 v[124:127], v167, s[52:55], 0 offen sc1
buffer_load_dwordx4 v[128:131], v167, s[52:55], 0 offen offset:608 sc1
s_waitcnt vmcnt(3)
v_mfma_f32_32x32x8_bf16 v[16:31], v[132:133], v[144:145], v[48:63]
s_waitcnt vmcnt(2)
v_mfma_f32_32x32x16_bf16 v[16:31], v[120:123], v[136:139], v[16:31]
s_nop 11
v_exp_f32 v16, v16
v_exp_f32 v17, v17
v_exp_f32 v18, v18
v_exp_f32 v19, v19
v_exp_f32 v20, v20
v_exp_f32 v21, v21
v_exp_f32 v22, v22
v_exp_f32 v23, v23
v_exp_f32 v24, v24
v_exp_f32 v25, v25
v_exp_f32 v26, v26
v_exp_f32 v27, v27
v_exp_f32 v28, v28
v_exp_f32 v29, v29
v_exp_f32 v30, v30
v_exp_f32 v31, v31
v_cvt_pk_bf16_f32 v112, v16, v17
v_cvt_pk_bf16_f32 v113, v18, v19
v_cvt_pk_bf16_f32 v114, v20, v21
v_cvt_pk_bf16_f32 v115, v22, v23
v_cvt_pk_bf16_f32 v116, v24, v25
v_cvt_pk_bf16_f32 v117, v26, v27
v_cvt_pk_bf16_f32 v118, v28, v29
v_cvt_pk_bf16_f32 v119, v30, v31
s_waitcnt vmcnt(1)
v_mfma_f32_32x32x16_bf16 v[80:95], v[124:127], v[112:115], v[80:95]
s_waitcnt vmcnt(0)
v_mfma_f32_32x32x16_bf16 v[80:95], v[128:131], v[116:119], v[80:95]
buffer_load_dwordx4 v[120:123], v166, s[52:55], 0 offen offset:1024 sc1
buffer_load_dwordx4 v[124:127], v167, s[52:55], 0 offen offset:1216 sc1
buffer_load_dwordx4 v[128:131], v167, s[52:55], 0 offen offset:1824 sc1
v_mfma_f32_32x32x8_bf16 v[16:31], v[132:133], v[146:147], v[48:63]
s_waitcnt vmcnt(2)
v_mfma_f32_32x32x16_bf16 v[16:31], v[120:123], v[136:139], v[16:31]
s_nop 11
v_exp_f32 v16, v16
v_exp_f32 v17, v17
v_exp_f32 v18, v18
v_exp_f32 v19, v19
v_exp_f32 v20, v20
v_exp_f32 v21, v21
v_exp_f32 v22, v22
v_exp_f32 v23, v23
v_exp_f32 v24, v24
v_exp_f32 v25, v25
v_exp_f32 v26, v26
v_exp_f32 v27, v27
v_exp_f32 v28, v28
v_exp_f32 v29, v29
v_exp_f32 v30, v30
v_exp_f32 v31, v31
v_cvt_pk_bf16_f32 v112, v16, v17
v_cvt_pk_bf16_f32 v113, v18, v19
v_cvt_pk_bf16_f32 v114, v20, v21
v_cvt_pk_bf16_f32 v115, v22, v23
v_cvt_pk_bf16_f32 v116, v24, v25
v_cvt_pk_bf16_f32 v117, v26, v27
v_cvt_pk_bf16_f32 v118, v28, v29
v_cvt_pk_bf16_f32 v119, v30, v31
s_waitcnt vmcnt(1)
v_mfma_f32_32x32x16_bf16 v[80:95], v[124:127], v[112:115], v[80:95]
s_waitcnt vmcnt(0)
v_mfma_f32_32x32x16_bf16 v[80:95], v[128:131], v[116:119], v[80:95]
buffer_load_dwordx4 v[120:123], v166, s[52:55], 0 offen offset:2048 sc1
buffer_load_dwordx4 v[124:127], v167, s[52:55], s98 offen sc1
buffer_load_dwordx4 v[128:131], v167, s[52:55], s98 offen offset:608 sc1
v_mfma_f32_32x32x8_bf16 v[16:31], v[132:133], v[148:149], v[48:63]
s_waitcnt vmcnt(2)
v_mfma_f32_32x32x16_bf16 v[16:31], v[120:123], v[136:139], v[16:31]
s_nop 11
v_exp_f32 v16, v16
v_exp_f32 v17, v17
v_exp_f32 v18, v18
v_exp_f32 v19, v19
v_exp_f32 v20, v20
v_exp_f32 v21, v21
v_exp_f32 v22, v22
v_exp_f32 v23, v23
v_exp_f32 v24, v24
v_exp_f32 v25, v25
v_exp_f32 v26, v26
v_exp_f32 v27, v27
v_exp_f32 v28, v28
v_exp_f32 v29, v29
v_exp_f32 v30, v30
v_exp_f32 v31, v31
v_cvt_pk_bf16_f32 v112, v16, v17
v_cvt_pk_bf16_f32 v113, v18, v19
v_cvt_pk_bf16_f32 v114, v20, v21
v_cvt_pk_bf16_f32 v115, v22, v23
v_cvt_pk_bf16_f32 v116, v24, v25
v_cvt_pk_bf16_f32 v117, v26, v27
v_cvt_pk_bf16_f32 v118, v28, v29
v_cvt_pk_bf16_f32 v119, v30, v31
s_waitcnt vmcnt(1)
v_mfma_f32_32x32x16_bf16 v[80:95], v[124:127], v[112:115], v[80:95]
s_waitcnt vmcnt(0)
v_mfma_f32_32x32x16_bf16 v[80:95], v[128:131], v[116:119], v[80:95]
buffer_load_dwordx4 v[120:123], v166, s[52:55], 0 offen offset:3072 sc1
buffer_load_dwordx4 v[124:127], v167, s[52:55], s98 offen offset:1216 sc1
buffer_load_dwordx4 v[128:131], v167, s[52:55], s98 offen offset:1824 sc1
v_mfma_f32_32x32x8_bf16 v[16:31], v[132:133], v[150:151], v[48:63]
s_waitcnt vmcnt(2)
v_mfma_f32_32x32x16_bf16 v[16:31], v[120:123], v[136:139], v[16:31]
s_nop 11
v_exp_f32 v16, v16
v_exp_f32 v17, v17
v_exp_f32 v18, v18
v_exp_f32 v19, v19
v_exp_f32 v20, v20
v_exp_f32 v21, v21
v_exp_f32 v22, v22
v_exp_f32 v23, v23
v_exp_f32 v24, v24
v_exp_f32 v25, v25
v_exp_f32 v26, v26
v_exp_f32 v27, v27
v_exp_f32 v28, v28
v_exp_f32 v29, v29
v_exp_f32 v30, v30
v_exp_f32 v31, v31
v_cvt_pk_bf16_f32 v112, v16, v17
v_cvt_pk_bf16_f32 v113, v18, v19
v_cvt_pk_bf16_f32 v114, v20, v21
v_cvt_pk_bf16_f32 v115, v22, v23
v_cvt_pk_bf16_f32 v116, v24, v25
v_cvt_pk_bf16_f32 v117, v26, v27
v_cvt_pk_bf16_f32 v118, v28, v29
v_cvt_pk_bf16_f32 v119, v30, v31
s_waitcnt vmcnt(1)
v_mfma_f32_32x32x16_bf16 v[80:95], v[124:127], v[112:115], v[80:95]
s_waitcnt vmcnt(0)
v_mfma_f32_32x32x16_bf16 v[80:95], v[128:131], v[116:119], v[80:95]
s_and_b32 s97, s96, 1
s_cmp_eq_u32 s97, 0
s_mov_b32 s97, 14848
s_cselect_b32 s97, 4096, s97
v_add_u32 v166, s97, v166
s_mov_b32 s97, 14080
s_cselect_b32 s97, 4864, s97
v_add_u32 v167, s97, v167
s_mov_b32 s97, 18432
s_cselect_b32 s97, 512, s97
v_add_u32 v168, s97, v168
s_nop 15
s_nop 3
s_sub_u32 s96, s96, 1
s_cmp_lg_u32 s96, 0
s_cbranch_scc1 Lfbl_25_%=
s_branch Lr_23_%=
Lx_26_%=:
s_waitcnt lgkmcnt(0)
s_nop 15
s_nop 3
v_mov_b32 v166, v4
v_mov_b32 v167, v7
v_mov_b32 v168, v10
v_mov_b32 v169, 0xff800000
s_mov_b32 s96, 16
s_mov_b32 s98, 2432
Lfbl_28_%=:
buffer_load_dwordx2 v[132:133], v168, s[52:55], 0 offen sc1
buffer_load_dwordx4 v[120:123], v166, s[52:55], 0 offen sc1
s_waitcnt vmcnt(1)
v_mfma_f32_32x32x8_bf16 v[16:31], v[132:133], v[152:153], 0
s_waitcnt vmcnt(0)
v_mfma_f32_32x32x16_bf16 v[16:31], v[120:123], v[140:143], v[16:31]
s_nop 11
v_max3_f32 v169, v16, v17, v169
v_max3_f32 v169, v169, v18, v19
v_max3_f32 v169, v169, v20, v21
v_max3_f32 v169, v169, v22, v23
v_max3_f32 v169, v169, v24, v25
v_max3_f32 v169, v169, v26, v27
v_max3_f32 v169, v169, v28, v29
v_max3_f32 v169, v169, v30, v31
buffer_load_dwordx4 v[120:123], v166, s[52:55], 0 offen offset:1024 sc1
v_mfma_f32_32x32x8_bf16 v[16:31], v[132:133], v[154:155], 0
s_waitcnt vmcnt(0)
v_mfma_f32_32x32x16_bf16 v[16:31], v[120:123], v[140:143], v[16:31]
s_nop 11
v_max3_f32 v169, v16, v17, v169
v_max3_f32 v169, v169, v18, v19
v_max3_f32 v169, v169, v20, v21
v_max3_f32 v169, v169, v22, v23
v_max3_f32 v169, v169, v24, v25
v_max3_f32 v169, v169, v26, v27
v_max3_f32 v169, v169, v28, v29
v_max3_f32 v169, v169, v30, v31
buffer_load_dwordx4 v[120:123], v166, s[52:55], 0 offen offset:2048 sc1
v_mfma_f32_32x32x8_bf16 v[16:31], v[132:133], v[156:157], 0
s_waitcnt vmcnt(0)
v_mfma_f32_32x32x16_bf16 v[16:31], v[120:123], v[140:143], v[16:31]
s_nop 11
v_max3_f32 v169, v16, v17, v169
)"
R"(v_max3_f32 v169, v169, v18, v19
v_max3_f32 v169, v169, v20, v21
v_max3_f32 v169, v169, v22, v23
v_max3_f32 v169, v169, v24, v25
v_max3_f32 v169, v169, v26, v27
v_max3_f32 v169, v169, v28, v29
v_max3_f32 v169, v169, v30, v31
buffer_load_dwordx4 v[120:123], v166, s[52:55], 0 offen offset:3072 sc1
v_mfma_f32_32x32x8_bf16 v[16:31], v[132:133], v[158:159], 0
s_waitcnt vmcnt(0)
v_mfma_f32_32x32x16_bf16 v[16:31], v[120:123], v[140:143], v[16:31]
s_nop 11
v_max3_f32 v169, v16, v17, v169
v_max3_f32 v169, v169, v18, v19
v_max3_f32 v169, v169, v20, v21
v_max3_f32 v169, v169, v22, v23
v_max3_f32 v169, v169, v24, v25
v_max3_f32 v169, v169, v26, v27
v_max3_f32 v169, v169, v28, v29
v_max3_f32 v169, v169, v30, v31
s_and_b32 s97, s96, 1
s_cmp_eq_u32 s97, 0
s_mov_b32 s97, 14848
s_cselect_b32 s97, 4096, s97
v_add_u32 v166, s97, v166
s_mov_b32 s97, 14080
s_cselect_b32 s97, 4864, s97
v_add_u32 v167, s97, v167
s_mov_b32 s97, 18432
s_cselect_b32 s97, 512, s97
v_add_u32 v168, s97, v168
s_nop 15
s_nop 3
s_sub_u32 s96, s96, 1
s_cmp_lg_u32 s96, 0
s_cbranch_scc1 Lfbl_28_%=
v_mov_b32 v170, v169
s_nop 1
v_permlane32_swap_b32 v169, v170
v_max_f32 v169, v169, v170
v_pk_mul_f32 v[64:65], v[168:169], -1.0 op_sel:[1,0] op_sel_hi:[1,0]
v_pk_mul_f32 v[66:67], v[168:169], -1.0 op_sel:[1,0] op_sel_hi:[1,0]
v_pk_mul_f32 v[68:69], v[168:169], -1.0 op_sel:[1,0] op_sel_hi:[1,0]
v_pk_mul_f32 v[70:71], v[168:169], -1.0 op_sel:[1,0] op_sel_hi:[1,0]
v_pk_mul_f32 v[72:73], v[168:169], -1.0 op_sel:[1,0] op_sel_hi:[1,0]
v_pk_mul_f32 v[74:75], v[168:169], -1.0 op_sel:[1,0] op_sel_hi:[1,0]
v_pk_mul_f32 v[76:77], v[168:169], -1.0 op_sel:[1,0] op_sel_hi:[1,0]
v_pk_mul_f32 v[78:79], v[168:169], -1.0 op_sel:[1,0] op_sel_hi:[1,0]
v_mov_b32 v166, v4
v_mov_b32 v167, v7
v_mov_b32 v168, v10
v_mov_b32 v96, 0
v_mov_b32 v97, 0
v_mov_b32 v98, 0
v_mov_b32 v99, 0
v_mov_b32 v100, 0
v_mov_b32 v101, 0
v_mov_b32 v102, 0
v_mov_b32 v103, 0
v_mov_b32 v104, 0
v_mov_b32 v105, 0
v_mov_b32 v106, 0
v_mov_b32 v107, 0
v_mov_b32 v108, 0
v_mov_b32 v109, 0
v_mov_b32 v110, 0
v_mov_b32 v111, 0
s_mov_b32 s96, 16
s_mov_b32 s98, 2432
Lfbl_29_%=:
buffer_load_dwordx2 v[132:133], v168, s[52:55], 0 offen sc1
buffer_load_dwordx4 v[120:123], v166, s[52:55], 0 offen sc1
buffer_load_dwordx4 v[124:127], v167, s[52:55], 0 offen sc1
buffer_load_dwordx4 v[128:131], v167, s[52:55], 0 offen offset:608 sc1
s_waitcnt vmcnt(3)
v_mfma_f32_32x32x8_bf16 v[16:31], v[132:133], v[152:153], v[64:79]
s_waitcnt vmcnt(2)
v_mfma_f32_32x32x16_bf16 v[16:31], v[120:123], v[140:143], v[16:31]
s_nop 11
v_exp_f32 v16, v16
v_exp_f32 v17, v17
v_exp_f32 v18, v18
v_exp_f32 v19, v19
v_exp_f32 v20, v20
v_exp_f32 v21, v21
v_exp_f32 v22, v22
v_exp_f32 v23, v23
v_exp_f32 v24, v24
v_exp_f32 v25, v25
v_exp_f32 v26, v26
v_exp_f32 v27, v27
v_exp_f32 v28, v28
v_exp_f32 v29, v29
v_exp_f32 v30, v30
v_exp_f32 v31, v31
v_cvt_pk_bf16_f32 v112, v16, v17
v_cvt_pk_bf16_f32 v113, v18, v19
v_cvt_pk_bf16_f32 v114, v20, v21
v_cvt_pk_bf16_f32 v115, v22, v23
v_cvt_pk_bf16_f32 v116, v24, v25
v_cvt_pk_bf16_f32 v117, v26, v27
v_cvt_pk_bf16_f32 v118, v28, v29
v_cvt_pk_bf16_f32 v119, v30, v31
s_waitcnt vmcnt(1)
v_mfma_f32_32x32x16_bf16 v[96:111], v[124:127], v[112:115], v[96:111]
s_waitcnt vmcnt(0)
v_mfma_f32_32x32x16_bf16 v[96:111], v[128:131], v[116:119], v[96:111]
buffer_load_dwordx4 v[120:123], v166, s[52:55], 0 offen offset:1024 sc1
buffer_load_dwordx4 v[124:127], v167, s[52:55], 0 offen offset:1216 sc1
buffer_load_dwordx4 v[128:131], v167, s[52:55], 0 offen offset:1824 sc1
v_mfma_f32_32x32x8_bf16 v[16:31], v[132:133], v[154:155], v[64:79]
s_waitcnt vmcnt(2)
v_mfma_f32_32x32x16_bf16 v[16:31], v[120:123], v[140:143], v[16:31]
s_nop 11
v_exp_f32 v16, v16
v_exp_f32 v17, v17
v_exp_f32 v18, v18
v_exp_f32 v19, v19
v_exp_f32 v20, v20
v_exp_f32 v21, v21
v_exp_f32 v22, v22
v_exp_f32 v23, v23
v_exp_f32 v24, v24
v_exp_f32 v25, v25
v_exp_f32 v26, v26
v_exp_f32 v27, v27
v_exp_f32 v28, v28
v_exp_f32 v29, v29
v_exp_f32 v30, v30
v_exp_f32 v31, v31
v_cvt_pk_bf16_f32 v112, v16, v17
v_cvt_pk_bf16_f32 v113, v18, v19
v_cvt_pk_bf16_f32 v114, v20, v21
v_cvt_pk_bf16_f32 v115, v22, v23
v_cvt_pk_bf16_f32 v116, v24, v25
v_cvt_pk_bf16_f32 v117, v26, v27
v_cvt_pk_bf16_f32 v118, v28, v29
v_cvt_pk_bf16_f32 v119, v30, v31
s_waitcnt vmcnt(1)
v_mfma_f32_32x32x16_bf16 v[96:111], v[124:127], v[112:115], v[96:111]
s_waitcnt vmcnt(0)
v_mfma_f32_32x32x16_bf16 v[96:111], v[128:131], v[116:119], v[96:111]
buffer_load_dwordx4 v[120:123], v166, s[52:55], 0 offen offset:2048 sc1
buffer_load_dwordx4 v[124:127], v167, s[52:55], s98 offen sc1
buffer_load_dwordx4 v[128:131], v167, s[52:55], s98 offen offset:608 sc1
v_mfma_f32_32x32x8_bf16 v[16:31], v[132:133], v[156:157], v[64:79]
s_waitcnt vmcnt(2)
v_mfma_f32_32x32x16_bf16 v[16:31], v[120:123], v[140:143], v[16:31]
s_nop 11
v_exp_f32 v16, v16
v_exp_f32 v17, v17
v_exp_f32 v18, v18
v_exp_f32 v19, v19
v_exp_f32 v20, v20
v_exp_f32 v21, v21
v_exp_f32 v22, v22
v_exp_f32 v23, v23
v_exp_f32 v24, v24
v_exp_f32 v25, v25
v_exp_f32 v26, v26
v_exp_f32 v27, v27
v_exp_f32 v28, v28
v_exp_f32 v29, v29
v_exp_f32 v30, v30
v_exp_f32 v31, v31
v_cvt_pk_bf16_f32 v112, v16, v17
v_cvt_pk_bf16_f32 v113, v18, v19
v_cvt_pk_bf16_f32 v114, v20, v21
v_cvt_pk_bf16_f32 v115, v22, v23
v_cvt_pk_bf16_f32 v116, v24, v25
v_cvt_pk_bf16_f32 v117, v26, v27
v_cvt_pk_bf16_f32 v118, v28, v29
v_cvt_pk_bf16_f32 v119, v30, v31
s_waitcnt vmcnt(1)
v_mfma_f32_32x32x16_bf16 v[96:111], v[124:127], v[112:115], v[96:111]
s_waitcnt vmcnt(0)
v_mfma_f32_32x32x16_bf16 v[96:111], v[128:131], v[116:119], v[96:111]
buffer_load_dwordx4 v[120:123], v166, s[52:55], 0 offen offset:3072 sc1
buffer_load_dwordx4 v[124:127], v167, s[52:55], s98 offen offset:1216 sc1
buffer_load_dwordx4 v[128:131], v167, s[52:55], s98 offen offset:1824 sc1
v_mfma_f32_32x32x8_bf16 v[16:31], v[132:133], v[158:159], v[64:79]
s_waitcnt vmcnt(2)
v_mfma_f32_32x32x16_bf16 v[16:31], v[120:123], v[140:143], v[16:31]
s_nop 11
v_exp_f32 v16, v16
v_exp_f32 v17, v17
v_exp_f32 v18, v18
v_exp_f32 v19, v19
v_exp_f32 v20, v20
v_exp_f32 v21, v21
v_exp_f32 v22, v22
v_exp_f32 v23, v23
v_exp_f32 v24, v24
v_exp_f32 v25, v25
v_exp_f32 v26, v26
v_exp_f32 v27, v27
v_exp_f32 v28, v28
v_exp_f32 v29, v29
v_exp_f32 v30, v30
v_exp_f32 v31, v31
v_cvt_pk_bf16_f32 v112, v16, v17
v_cvt_pk_bf16_f32 v113, v18, v19
v_cvt_pk_bf16_f32 v114, v20, v21
v_cvt_pk_bf16_f32 v115, v22, v23
v_cvt_pk_bf16_f32 v116, v24, v25
v_cvt_pk_bf16_f32 v117, v26, v27
v_cvt_pk_bf16_f32 v118, v28, v29
v_cvt_pk_bf16_f32 v119, v30, v31
s_waitcnt vmcnt(1)
v_mfma_f32_32x32x16_bf16 v[96:111], v[124:127], v[112:115], v[96:111]
s_waitcnt vmcnt(0)
v_mfma_f32_32x32x16_bf16 v[96:111], v[128:131], v[116:119], v[96:111]
s_and_b32 s97, s96, 1
s_cmp_eq_u32 s97, 0
s_mov_b32 s97, 14848
s_cselect_b32 s97, 4096, s97
v_add_u32 v166, s97, v166
s_mov_b32 s97, 14080
s_cselect_b32 s97, 4864, s97
v_add_u32 v167, s97, v167
s_mov_b32 s97, 18432
s_cselect_b32 s97, 512, s97
v_add_u32 v168, s97, v168
s_nop 15
s_nop 3
s_sub_u32 s96, s96, 1
s_cmp_lg_u32 s96, 0
s_cbranch_scc1 Lfbl_29_%=
s_branch Lr_27_%=
Lx_34_%=:
s_mov_b32 s97, 32768
Lcp_33_%=:
s_mov_b32 s96, 24576
buffer_load_dwordx4 v[136:139], v4, s[52:55], s96 offen sc1
s_mov_b32 s96, 28672
buffer_load_dwordx4 v[140:143], v4, s[52:55], s96 offen sc1
s_mov_b32 s96, 32768
buffer_load_dwordx4 v[144:147], v4, s[52:55], s96 offen sc1
s_mov_b32 s96, 36864
buffer_load_dwordx4 v[148:151], v4, s[52:55], s96 offen sc1
s_mov_b32 s96, 40960
buffer_load_dwordx4 v[152:155], v4, s[52:55], s96 offen sc1
s_mov_b32 s96, 45056
buffer_load_dwordx4 v[156:159], v4, s[52:55], s96 offen sc1
s_waitcnt vmcnt(5)
v_max_u32 v12, v136, v137
v_max3_u32 v12, v12, v138, v139
s_waitcnt vmcnt(4)
v_max3_u32 v12, v12, v140, v141
v_max3_u32 v12, v12, v142, v143
s_waitcnt vmcnt(3)
v_max3_u32 v12, v12, v144, v145
v_max3_u32 v12, v12, v146, v147
s_waitcnt vmcnt(2)
v_max3_u32 v12, v12, v148, v149
v_max3_u32 v12, v12, v150, v151
s_waitcnt vmcnt(1)
v_max3_u32 v12, v12, v152, v153
v_max3_u32 v12, v12, v154, v155
s_waitcnt vmcnt(0)
v_max3_u32 v12, v12, v156, v157
v_max3_u32 v12, v12, v158, v159
v_cmp_eq_u32 vcc, -1, v12
s_nop 0
s_cmp_eq_u64 vcc, 0
s_cbranch_scc1 Lr_35_%=
s_sub_u32 s97, s97, 1
s_cmp_lg_u32 s97, 0
s_cbranch_scc1 Lcp_33_%=
s_branch Lr_35_%=
Lx_38_%=:
s_mov_b32 s97, 32768
Lcp_37_%=:
s_mov_b32 s96, 49152
buffer_load_dwordx4 v[160:163], v4, s[52:55], s96 offen sc1
s_mov_b32 s96, 53248
buffer_load_dwordx4 v[164:167], v4, s[52:55], s96 offen sc1
s_mov_b32 s96, 57344
buffer_load_dwordx4 v[168:171], v4, s[52:55], s96 offen sc1
s_mov_b32 s96, 61440
buffer_load_dwordx4 v[172:175], v4, s[52:55], s96 offen sc1
s_mov_b32 s96, 65536
buffer_load_dwordx4 v[176:179], v4, s[52:55], s96 offen sc1
s_mov_b32 s96, 69632
buffer_load_dwordx4 v[180:183], v4, s[52:55], s96 offen sc1
s_waitcnt vmcnt(5)
v_max_u32 v12, v160, v161
v_max3_u32 v12, v12, v162, v163
s_waitcnt vmcnt(4)
v_max3_u32 v12, v12, v164, v165
v_max3_u32 v12, v12, v166, v167
s_waitcnt vmcnt(3)
v_max3_u32 v12, v12, v168, v169
v_max3_u32 v12, v12, v170, v171
s_waitcnt vmcnt(2)
v_max3_u32 v12, v12, v172, v173
v_max3_u32 v12, v12, v174, v175
s_waitcnt vmcnt(1)
v_max3_u32 v12, v12, v176, v177
v_max3_u32 v12, v12, v178, v179
s_waitcnt vmcnt(0)
v_max3_u32 v12, v12, v180, v181
v_max3_u32 v12, v12, v182, v183
v_cmp_eq_u32 vcc, -1, v12
s_nop 0
s_cmp_eq_u64 vcc, 0
s_cbranch_scc1 Lr_39_%=
s_sub_u32 s97, s97, 1
s_cmp_lg_u32 s97, 0
s_cbranch_scc1 Lcp_37_%=
s_branch Lr_39_%=
Lx_46_%=:
s_mov_b32 s97, 32768
Lcp_45_%=:
s_waitcnt lgkmcnt(0)
s_add_u32 m0, s62, 16384
s_nop 0
buffer_load_dwordx4 v4, s[52:55], m0 offen sc1 lds
s_add_u32 m0, s62, 20480
s_nop 0
buffer_load_dwordx4 v4, s[52:55], m0 offen sc1 lds
s_add_u32 m0, s62, 24576
s_nop 0
buffer_load_dwordx4 v4, s[52:55], m0 offen sc1 lds
s_waitcnt vmcnt(2)
ds_read_b128 v[164:167], v10 offset:16384
s_waitcnt vmcnt(1)
ds_read_b128 v[168:171], v10 offset:20480
s_waitcnt vmcnt(0)
ds_read_b128 v[172:175], v10 offset:24576
s_waitcnt lgkmcnt(2)
v_max_u32 v12, v164, v165
v_max3_u32 v12, v12, v166, v167
s_waitcnt lgkmcnt(1)
v_max3_u32 v12, v12, v168, v169
v_max3_u32 v12, v12, v170, v171
s_waitcnt lgkmcnt(0)
v_max3_u32 v12, v12, v172, v173
v_max3_u32 v12, v12, v174, v175
v_lshlrev_b32 v144, 16, v164
v_and_b32 v145, 0xffff0000, v164
v_lshlrev_b32 v146, 16, v165
v_and_b32 v147, 0xffff0000, v165
v_lshlrev_b32 v148, 16, v166
v_and_b32 v149, 0xffff0000, v166
v_lshlrev_b32 v150, 16, v167
v_and_b32 v151, 0xffff0000, v167
v_fma_f32 v144, v144, v8, v9
v_fma_f32 v145, v145, v8, v9
v_fma_f32 v146, v146, v8, v9
v_fma_f32 v147, v147, v8, v9
v_fma_f32 v148, v148, v8, v9
v_fma_f32 v149, v149, v8, v9
v_fma_f32 v150, v150, v8, v9
v_fma_f32 v151, v151, v8, v9
v_cvt_pk_bf16_f32 v164, v144, v145
v_cvt_pk_bf16_f32 v165, v146, v147
v_cvt_pk_bf16_f32 v166, v148, v149
v_cvt_pk_bf16_f32 v167, v150, v151
ds_write_b128 v10, v[164:167] offset:16384
v_lshlrev_b32 v144, 16, v168
v_and_b32 v145, 0xffff0000, v168
v_lshlrev_b32 v146, 16, v169
v_and_b32 v147, 0xffff0000, v169
v_lshlrev_b32 v148, 16, v170
v_and_b32 v149, 0xffff0000, v170
v_lshlrev_b32 v150, 16, v171
v_and_b32 v151, 0xffff0000, v171
v_fma_f32 v144, v144, v8, v9
v_fma_f32 v145, v145, v8, v9
v_fma_f32 v146, v146, v8, v9
v_fma_f32 v147, v147, v8, v9
v_fma_f32 v148, v148, v8, v9
v_fma_f32 v149, v149, v8, v9
v_fma_f32 v150, v150, v8, v9
v_fma_f32 v151, v151, v8, v9
v_cvt_pk_bf16_f32 v168, v144, v145
v_cvt_pk_bf16_f32 v169, v146, v147
v_cvt_pk_bf16_f32 v170, v148, v149
v_cvt_pk_bf16_f32 v171, v150, v151
ds_write_b128 v10, v[168:171] offset:20480
v_lshlrev_b32 v144, 16, v172
v_and_b32 v145, 0xffff0000, v172
v_lshlrev_b32 v146, 16, v173
v_and_b32 v147, 0xffff0000, v173
v_lshlrev_b32 v148, 16, v174
v_and_b32 v149, 0xffff0000, v174
v_lshlrev_b32 v150, 16, v175
v_and_b32 v151, 0xffff0000, v175
v_fma_f32 v144, v144, v8, v9
v_fma_f32 v145, v145, v8, v9
v_fma_f32 v146, v146, v8, v9
v_fma_f32 v147, v147, v8, v9
v_fma_f32 v148, v148, v8, v9
v_fma_f32 v149, v149, v8, v9
v_fma_f32 v150, v150, v8, v9
v_fma_f32 v151, v151, v8, v9
v_cvt_pk_bf16_f32 v172, v144, v145
v_cvt_pk_bf16_f32 v173, v146, v147
v_cvt_pk_bf16_f32 v174, v148, v149
)"
R"(v_cvt_pk_bf16_f32 v175, v150, v151
ds_write_b128 v10, v[172:175] offset:24576
v_cmp_eq_u32 vcc, -1, v12
s_nop 0
s_cmp_eq_u64 vcc, 0
s_cbranch_scc1 Lr_47_%=
s_sub_u32 s97, s97, 1
s_cmp_lg_u32 s97, 0
s_cbranch_scc1 Lcp_45_%=
s_branch Lr_47_%=
Lx_50_%=:
s_mov_b32 s97, 32768
Lcp_49_%=:
s_waitcnt lgkmcnt(0)
s_add_u32 m0, s62, 28672
s_nop 0
buffer_load_dwordx4 v4, s[52:55], m0 offen sc1 lds
s_add_u32 m0, s62, 32768
s_nop 0
buffer_load_dwordx4 v4, s[52:55], m0 offen sc1 lds
s_add_u32 m0, s62, 36864
s_nop 0
buffer_load_dwordx4 v4, s[52:55], m0 offen sc1 lds
s_waitcnt vmcnt(2)
ds_read_b128 v[164:167], v10 offset:28672
s_waitcnt vmcnt(1)
ds_read_b128 v[168:171], v10 offset:32768
s_waitcnt vmcnt(0)
ds_read_b128 v[172:175], v10 offset:36864
s_waitcnt lgkmcnt(2)
v_max_u32 v12, v164, v165
v_max3_u32 v12, v12, v166, v167
s_waitcnt lgkmcnt(1)
v_max3_u32 v12, v12, v168, v169
v_max3_u32 v12, v12, v170, v171
s_waitcnt lgkmcnt(0)
v_max3_u32 v12, v12, v172, v173
v_max3_u32 v12, v12, v174, v175
v_lshlrev_b32 v144, 16, v164
v_and_b32 v145, 0xffff0000, v164
v_lshlrev_b32 v146, 16, v165
v_and_b32 v147, 0xffff0000, v165
v_lshlrev_b32 v148, 16, v166
v_and_b32 v149, 0xffff0000, v166
v_lshlrev_b32 v150, 16, v167
v_and_b32 v151, 0xffff0000, v167
v_fma_f32 v144, v144, v8, v9
v_fma_f32 v145, v145, v8, v9
v_fma_f32 v146, v146, v8, v9
v_fma_f32 v147, v147, v8, v9
v_fma_f32 v148, v148, v8, v9
v_fma_f32 v149, v149, v8, v9
v_fma_f32 v150, v150, v8, v9
v_fma_f32 v151, v151, v8, v9
v_cvt_pk_bf16_f32 v164, v144, v145
v_cvt_pk_bf16_f32 v165, v146, v147
v_cvt_pk_bf16_f32 v166, v148, v149
v_cvt_pk_bf16_f32 v167, v150, v151
ds_write_b128 v10, v[164:167] offset:28672
v_lshlrev_b32 v144, 16, v168
v_and_b32 v145, 0xffff0000, v168
v_lshlrev_b32 v146, 16, v169
v_and_b32 v147, 0xffff0000, v169
v_lshlrev_b32 v148, 16, v170
v_and_b32 v149, 0xffff0000, v170
v_lshlrev_b32 v150, 16, v171
v_and_b32 v151, 0xffff0000, v171
v_fma_f32 v144, v144, v8, v9
v_fma_f32 v145, v145, v8, v9
v_fma_f32 v146, v146, v8, v9
v_fma_f32 v147, v147, v8, v9
v_fma_f32 v148, v148, v8, v9
v_fma_f32 v149, v149, v8, v9
v_fma_f32 v150, v150, v8, v9
v_fma_f32 v151, v151, v8, v9
v_cvt_pk_bf16_f32 v168, v144, v145
v_cvt_pk_bf16_f32 v169, v146, v147
v_cvt_pk_bf16_f32 v170, v148, v149
v_cvt_pk_bf16_f32 v171, v150, v151
ds_write_b128 v10, v[168:171] offset:32768
v_lshlrev_b32 v144, 16, v172
v_and_b32 v145, 0xffff0000, v172
v_lshlrev_b32 v146, 16, v173
v_and_b32 v147, 0xffff0000, v173
v_lshlrev_b32 v148, 16, v174
v_and_b32 v149, 0xffff0000, v174
v_lshlrev_b32 v150, 16, v175
v_and_b32 v151, 0xffff0000, v175
v_fma_f32 v144, v144, v8, v9
v_fma_f32 v145, v145, v8, v9
v_fma_f32 v146, v146, v8, v9
v_fma_f32 v147, v147, v8, v9
v_fma_f32 v148, v148, v8, v9
v_fma_f32 v149, v149, v8, v9
v_fma_f32 v150, v150, v8, v9
v_fma_f32 v151, v151, v8, v9
v_cvt_pk_bf16_f32 v172, v144, v145
v_cvt_pk_bf16_f32 v173, v146, v147
v_cvt_pk_bf16_f32 v174, v148, v149
v_cvt_pk_bf16_f32 v175, v150, v151
ds_write_b128 v10, v[172:175] offset:36864
v_cmp_eq_u32 vcc, -1, v12
s_nop 0
s_cmp_eq_u64 vcc, 0
s_cbranch_scc1 Lr_51_%=
s_sub_u32 s97, s97, 1
s_cmp_lg_u32 s97, 0
s_cbranch_scc1 Lcp_49_%=
s_branch Lr_51_%=
Lx_54_%=:
s_mov_b32 s97, 32768
Lcp_53_%=:
s_waitcnt lgkmcnt(0)
s_add_u32 m0, s62, 40960
s_nop 0
buffer_load_dwordx4 v4, s[52:55], m0 offen sc1 lds
s_add_u32 m0, s62, 45056
s_nop 0
buffer_load_dwordx4 v4, s[52:55], m0 offen sc1 lds
s_add_u32 m0, s62, 49152
s_nop 0
buffer_load_dwordx4 v4, s[52:55], m0 offen sc1 lds
s_waitcnt vmcnt(2)
ds_read_b128 v[164:167], v10 offset:40960
s_waitcnt vmcnt(1)
ds_read_b128 v[168:171], v10 offset:45056
s_waitcnt vmcnt(0)
ds_read_b128 v[172:175], v10 offset:49152
s_waitcnt lgkmcnt(2)
v_max_u32 v12, v164, v165
v_max3_u32 v12, v12, v166, v167
s_waitcnt lgkmcnt(1)
v_max3_u32 v12, v12, v168, v169
v_max3_u32 v12, v12, v170, v171
s_waitcnt lgkmcnt(0)
v_max3_u32 v12, v12, v172, v173
v_max3_u32 v12, v12, v174, v175
v_lshlrev_b32 v144, 16, v164
v_and_b32 v145, 0xffff0000, v164
v_lshlrev_b32 v146, 16, v165
v_and_b32 v147, 0xffff0000, v165
v_lshlrev_b32 v148, 16, v166
v_and_b32 v149, 0xffff0000, v166
v_lshlrev_b32 v150, 16, v167
v_and_b32 v151, 0xffff0000, v167
v_fma_f32 v144, v144, v8, v9
v_fma_f32 v145, v145, v8, v9
v_fma_f32 v146, v146, v8, v9
v_fma_f32 v147, v147, v8, v9
v_fma_f32 v148, v148, v8, v9
v_fma_f32 v149, v149, v8, v9
v_fma_f32 v150, v150, v8, v9
v_fma_f32 v151, v151, v8, v9
v_cvt_pk_bf16_f32 v164, v144, v145
v_cvt_pk_bf16_f32 v165, v146, v147
v_cvt_pk_bf16_f32 v166, v148, v149
v_cvt_pk_bf16_f32 v167, v150, v151
ds_write_b128 v10, v[164:167] offset:40960
v_lshlrev_b32 v144, 16, v168
v_and_b32 v145, 0xffff0000, v168
v_lshlrev_b32 v146, 16, v169
v_and_b32 v147, 0xffff0000, v169
v_lshlrev_b32 v148, 16, v170
v_and_b32 v149, 0xffff0000, v170
v_lshlrev_b32 v150, 16, v171
v_and_b32 v151, 0xffff0000, v171
v_fma_f32 v144, v144, v8, v9
v_fma_f32 v145, v145, v8, v9
v_fma_f32 v146, v146, v8, v9
v_fma_f32 v147, v147, v8, v9
v_fma_f32 v148, v148, v8, v9
v_fma_f32 v149, v149, v8, v9
v_fma_f32 v150, v150, v8, v9
v_fma_f32 v151, v151, v8, v9
v_cvt_pk_bf16_f32 v168, v144, v145
v_cvt_pk_bf16_f32 v169, v146, v147
v_cvt_pk_bf16_f32 v170, v148, v149
v_cvt_pk_bf16_f32 v171, v150, v151
ds_write_b128 v10, v[168:171] offset:45056
v_lshlrev_b32 v144, 16, v172
v_and_b32 v145, 0xffff0000, v172
v_lshlrev_b32 v146, 16, v173
v_and_b32 v147, 0xffff0000, v173
v_lshlrev_b32 v148, 16, v174
v_and_b32 v149, 0xffff0000, v174
v_lshlrev_b32 v150, 16, v175
v_and_b32 v151, 0xffff0000, v175
v_fma_f32 v144, v144, v8, v9
v_fma_f32 v145, v145, v8, v9
v_fma_f32 v146, v146, v8, v9
v_fma_f32 v147, v147, v8, v9
v_fma_f32 v148, v148, v8, v9
v_fma_f32 v149, v149, v8, v9
v_fma_f32 v150, v150, v8, v9
v_fma_f32 v151, v151, v8, v9
v_cvt_pk_bf16_f32 v172, v144, v145
v_cvt_pk_bf16_f32 v173, v146, v147
v_cvt_pk_bf16_f32 v174, v148, v149
v_cvt_pk_bf16_f32 v175, v150, v151
ds_write_b128 v10, v[172:175] offset:49152
v_cmp_eq_u32 vcc, -1, v12
s_nop 0
s_cmp_eq_u64 vcc, 0
s_cbranch_scc1 Lr_55_%=
s_sub_u32 s97, s97, 1
s_cmp_lg_u32 s97, 0
s_cbranch_scc1 Lcp_53_%=
s_branch Lr_55_%=
Lx_58_%=:
s_mov_b32 s97, 32768
Lcp_57_%=:
s_waitcnt lgkmcnt(0)
s_add_u32 m0, s62, 53248
s_nop 0
buffer_load_dwordx4 v4, s[52:55], m0 offen sc1 lds
s_add_u32 m0, s62, 57344
s_nop 0
buffer_load_dwordx4 v4, s[52:55], m0 offen sc1 lds
s_add_u32 m0, s62, 61440
s_nop 0
buffer_load_dwordx4 v4, s[52:55], m0 offen sc1 lds
s_waitcnt vmcnt(2)
ds_read_b128 v[164:167], v10 offset:53248
s_waitcnt vmcnt(1)
ds_read_b128 v[168:171], v10 offset:57344
s_waitcnt vmcnt(0)
ds_read_b128 v[172:175], v10 offset:61440
s_waitcnt lgkmcnt(2)
v_max_u32 v12, v164, v165
v_max3_u32 v12, v12, v166, v167
s_waitcnt lgkmcnt(1)
v_max3_u32 v12, v12, v168, v169
v_max3_u32 v12, v12, v170, v171
s_waitcnt lgkmcnt(0)
v_max3_u32 v12, v12, v172, v173
v_max3_u32 v12, v12, v174, v175
v_lshlrev_b32 v144, 16, v164
v_and_b32 v145, 0xffff0000, v164
v_lshlrev_b32 v146, 16, v165
v_and_b32 v147, 0xffff0000, v165
v_lshlrev_b32 v148, 16, v166
v_and_b32 v149, 0xffff0000, v166
v_lshlrev_b32 v150, 16, v167
v_and_b32 v151, 0xffff0000, v167
v_fma_f32 v144, v144, v8, v9
v_fma_f32 v145, v145, v8, v9
v_fma_f32 v146, v146, v8, v9
v_fma_f32 v147, v147, v8, v9
v_fma_f32 v148, v148, v8, v9
v_fma_f32 v149, v149, v8, v9
v_fma_f32 v150, v150, v8, v9
v_fma_f32 v151, v151, v8, v9
v_cvt_pk_bf16_f32 v164, v144, v145
v_cvt_pk_bf16_f32 v165, v146, v147
v_cvt_pk_bf16_f32 v166, v148, v149
v_cvt_pk_bf16_f32 v167, v150, v151
ds_write_b128 v10, v[164:167] offset:53248
v_lshlrev_b32 v144, 16, v168
v_and_b32 v145, 0xffff0000, v168
v_lshlrev_b32 v146, 16, v169
v_and_b32 v147, 0xffff0000, v169
v_lshlrev_b32 v148, 16, v170
v_and_b32 v149, 0xffff0000, v170
v_lshlrev_b32 v150, 16, v171
v_and_b32 v151, 0xffff0000, v171
v_fma_f32 v144, v144, v8, v9
v_fma_f32 v145, v145, v8, v9
v_fma_f32 v146, v146, v8, v9
v_fma_f32 v147, v147, v8, v9
v_fma_f32 v148, v148, v8, v9
v_fma_f32 v149, v149, v8, v9
v_fma_f32 v150, v150, v8, v9
v_fma_f32 v151, v151, v8, v9
v_cvt_pk_bf16_f32 v168, v144, v145
v_cvt_pk_bf16_f32 v169, v146, v147
v_cvt_pk_bf16_f32 v170, v148, v149
v_cvt_pk_bf16_f32 v171, v150, v151
ds_write_b128 v10, v[168:171] offset:57344
v_lshlrev_b32 v144, 16, v172
v_and_b32 v145, 0xffff0000, v172
v_lshlrev_b32 v146, 16, v173
v_and_b32 v147, 0xffff0000, v173
v_lshlrev_b32 v148, 16, v174
v_and_b32 v149, 0xffff0000, v174
v_lshlrev_b32 v150, 16, v175
v_and_b32 v151, 0xffff0000, v175
v_fma_f32 v144, v144, v8, v9
v_fma_f32 v145, v145, v8, v9
v_fma_f32 v146, v146, v8, v9
v_fma_f32 v147, v147, v8, v9
v_fma_f32 v148, v148, v8, v9
v_fma_f32 v149, v149, v8, v9
v_fma_f32 v150, v150, v8, v9
v_fma_f32 v151, v151, v8, v9
v_cvt_pk_bf16_f32 v172, v144, v145
v_cvt_pk_bf16_f32 v173, v146, v147
v_cvt_pk_bf16_f32 v174, v148, v149
v_cvt_pk_bf16_f32 v175, v150, v151
ds_write_b128 v10, v[172:175] offset:61440
v_cmp_eq_u32 vcc, -1, v12
s_nop 0
s_cmp_eq_u64 vcc, 0
s_cbranch_scc1 Lr_59_%=
s_sub_u32 s97, s97, 1
s_cmp_lg_u32 s97, 0
s_cbranch_scc1 Lcp_57_%=
s_branch Lr_59_%=
Lx_62_%=:
s_mov_b32 s97, 32768
Lcp_61_%=:
s_waitcnt lgkmcnt(0)
s_add_u32 m0, s62, 65536
s_nop 0
buffer_load_dwordx4 v4, s[52:55], m0 offen sc1 lds
s_add_u32 m0, s62, 69632
s_nop 0
buffer_load_dwordx4 v4, s[52:55], m0 offen sc1 lds
s_waitcnt vmcnt(1)
ds_read_b128 v[164:167], v11
s_waitcnt vmcnt(0)
ds_read_b128 v[168:171], v11 offset:4096
s_waitcnt lgkmcnt(1)
v_max_u32 v12, v164, v165
v_max3_u32 v12, v12, v166, v167
s_waitcnt lgkmcnt(0)
v_max3_u32 v12, v12, v168, v169
v_max3_u32 v12, v12, v170, v171
v_lshlrev_b32 v144, 16, v164
v_and_b32 v145, 0xffff0000, v164
v_lshlrev_b32 v146, 16, v165
v_and_b32 v147, 0xffff0000, v165
v_lshlrev_b32 v148, 16, v166
v_and_b32 v149, 0xffff0000, v166
v_lshlrev_b32 v150, 16, v167
v_and_b32 v151, 0xffff0000, v167
v_fma_f32 v144, v144, v8, v9
v_fma_f32 v145, v145, v8, v9
v_fma_f32 v146, v146, v8, v9
v_fma_f32 v147, v147, v8, v9
v_fma_f32 v148, v148, v8, v9
v_fma_f32 v149, v149, v8, v9
v_fma_f32 v150, v150, v8, v9
v_fma_f32 v151, v151, v8, v9
v_cvt_pk_bf16_f32 v164, v144, v145
v_cvt_pk_bf16_f32 v165, v146, v147
v_cvt_pk_bf16_f32 v166, v148, v149
v_cvt_pk_bf16_f32 v167, v150, v151
ds_write_b128 v11, v[164:167]
v_lshlrev_b32 v144, 16, v168
v_and_b32 v145, 0xffff0000, v168
v_lshlrev_b32 v146, 16, v169
v_and_b32 v147, 0xffff0000, v169
v_lshlrev_b32 v148, 16, v170
v_and_b32 v149, 0xffff0000, v170
v_lshlrev_b32 v150, 16, v171
v_and_b32 v151, 0xffff0000, v171
v_fma_f32 v144, v144, v8, v9
v_fma_f32 v145, v145, v8, v9
v_fma_f32 v146, v146, v8, v9
v_fma_f32 v147, v147, v8, v9
v_fma_f32 v148, v148, v8, v9
v_fma_f32 v149, v149, v8, v9
v_fma_f32 v150, v150, v8, v9
v_fma_f32 v151, v151, v8, v9
v_cvt_pk_bf16_f32 v168, v144, v145
v_cvt_pk_bf16_f32 v169, v146, v147
v_cvt_pk_bf16_f32 v170, v148, v149
v_cvt_pk_bf16_f32 v171, v150, v151
ds_write_b128 v11, v[168:171] offset:4096
v_cmp_eq_u32 vcc, -1, v12
s_nop 0
s_cmp_eq_u64 vcc, 0
s_cbranch_scc1 Lr_63_%=
s_sub_u32 s97, s97, 1
s_cmp_lg_u32 s97, 0
s_cbranch_scc1 Lcp_61_%=
s_branch Lr_63_%=
Lx_66_%=:
s_mov_b32 s97, 32768
Lcp_65_%=:
s_mov_b32 s96, 0
buffer_load_dwordx2 v[128:129], v194, s[52:55], s96 offen sc0 sc1
s_mov_b32 s96, 16384
buffer_load_dwordx2 v[130:131], v194, s[52:55], s96 offen sc0 sc1
s_mov_b32 s96, 32768
buffer_load_dwordx2 v[132:133], v194, s[52:55], s96 offen sc0 sc1
s_mov_b32 s96, 49152
buffer_load_dwordx2 v[134:135], v194, s[52:55], s96 offen sc0 sc1
s_waitcnt vmcnt(3)
v_max_u32 v12, v128, v129
s_waitcnt vmcnt(2)
v_max3_u32 v12, v12, v130, v131
s_waitcnt vmcnt(1)
v_max3_u32 v12, v12, v132, v133
s_waitcnt vmcnt(0)
v_max3_u32 v12, v12, v134, v135
v_cmp_eq_u32 vcc, -1, v12
s_nop 0
s_cmp_eq_u64 vcc, 0
s_cbranch_scc1 Lr_67_%=
)"
R"(s_sub_u32 s97, s97, 1
s_cmp_lg_u32 s97, 0
s_cbranch_scc1 Lcp_65_%=
s_branch Lr_67_%=
Lx_70_%=:
s_mov_b32 s97, 32768
Lcp_69_%=:
s_sub_u32 s96, s93, 1
s_and_b32 s96, s96, 7
s_lshl_b32 s96, s96, 13
s_add_u32 s96, s96, 2097152
buffer_load_dwordx4 v[136:139], v4, s[52:55], s96 offen sc0 sc1
buffer_load_dwordx4 v[140:143], v4, s[52:55], s96 offen offset:1024 sc0 sc1
s_waitcnt vmcnt(1)
v_max_u32 v12, v136, v137
v_max3_u32 v12, v12, v138, v139
s_waitcnt vmcnt(0)
v_max3_u32 v12, v12, v140, v141
v_max3_u32 v12, v12, v142, v143
v_cmp_eq_u32 vcc, -1, v12
s_nop 0
s_cmp_eq_u64 vcc, 0
s_cbranch_scc1 Lr_71_%=
s_sub_u32 s97, s97, 1
s_cmp_lg_u32 s97, 0
s_cbranch_scc1 Lcp_69_%=
s_branch Lr_71_%=
Lx_74_%=:
s_mov_b32 s97, 32768
Lcp_73_%=:
s_sub_u32 s96, s93, 2
s_and_b32 s96, s96, 7
s_lshl_b32 s96, s96, 13
s_add_u32 s96, s96, 4194304
buffer_load_dwordx4 v[144:147], v4, s[52:55], s96 offen sc0 sc1
buffer_load_dwordx4 v[148:151], v4, s[52:55], s96 offen offset:1024 sc0 sc1
s_waitcnt vmcnt(1)
v_max_u32 v12, v144, v145
v_max3_u32 v12, v12, v146, v147
s_waitcnt vmcnt(0)
v_max3_u32 v12, v12, v148, v149
v_max3_u32 v12, v12, v150, v151
v_cmp_eq_u32 vcc, -1, v12
s_nop 0
s_cmp_eq_u64 vcc, 0
s_cbranch_scc1 Lr_75_%=
s_sub_u32 s97, s97, 1
s_cmp_lg_u32 s97, 0
s_cbranch_scc1 Lcp_73_%=
s_branch Lr_75_%=
LEND_%=:
)"
  : "+{s[34:35]}"(tab)
  : "s"(blockIdx.x), "s"(__builtin_amdgcn_kernarg_segment_ptr()), "{v0}"(threadIdx.x), "s"((unsigned)(size_t)lds)
  : "v1", "v2", "v3", "v4", "v5", "v6", "v7", "v8", "v9", "v10", "v11", "v12", "v13", "v14", "v15", "v16", "v17", "v18", "v19", "v20", "v21", "v22", "v23", "v24", "v25", "v26", "v27", "v28", "v29", "v30", "v31", "v32", "v33", "v34", "v35", "v36", "v37", "v38", "v39", "v40", "v41", "v42", "v43", "v44", "v45", "v46", "v47", "v48", "v49", "v50", "v51", "v52", "v53", "v54", "v55", "v56", "v57", "v58", "v59", "v60", "v61", "v62", "v63", "v64", "v65", "v66", "v67", "v68", "v69", "v70", "v71", "v72", "v73", "v74", "v75", "v76", "v77", "v78", "v79", "v80", "v81", "v82", "v83", "v84", "v85", "v86", "v87", "v88", "v89", "v90", "v91", "v92", "v93", "v94", "v95", "v96", "v97", "v98", "v99", "v100", "v101", "v102", "v103", "v104", "v105", "v106", "v107", "v108", "v109", "v110", "v111", "v112", "v113", "v114", "v115", "v116", "v117", "v118", "v119", "v120", "v121", "v122", "v123", "v124", "v125", "v126", "v127", "v128", "v129", "v130", "v131", "v132", "v133", "v134", "v135", "v136", "v137", "v138", "v139", "v140", "v141", "v142", "v143", "v144", "v145", "v146", "v147", "v148", "v149", "v150", "v151", "v152", "v153", "v154", "v155", "v156", "v157", "v158", "v159", "v160", "v161", "v162", "v163", "v164", "v165", "v166", "v167", "v168", "v169", "v170", "v171", "v172", "v173", "v174", "v175", "v176", "v177", "v178", "v179", "v180", "v181", "v182", "v183", "v184", "v185", "v186", "v187", "v188", "v189", "v190", "v191", "v192", "v193", "v194", "v195", "v196", "v197", "v198", "v199", "v200", "v201", "v202", "v203", "v204", "v205", "v206", "v207", "v208", "v209", "v210", "v211", "v212", "v213", "v214", "v215", "v216", "v217", "v218", "v219", "v220", "v221", "v222", "v223", "v224", "v225", "v226", "v227", "v228", "v229", "v230", "v231", "v232", "v233", "v234", "v235", "v236", "v237", "v238", "v239", "v240", "v241", "v242", "v243", "v244", "v245", "v246", "v247", "v248", "v249", "v250", "v251", "v252", "v253", "v254", "v255", "a0", "a1", "a2", "a3", "a4", "a5", "a6", "a7", "a8", "a9", "a10", "a11", "a12", "a13", "a14", "a15", "a16", "a17", "a18", "a19", "a20", "a21", "a22", "a23", "a24", "a25", "a26", "a27", "a28", "a29", "a30", "a31", "a32", "a33", "a34", "a35", "a36", "a37", "a38", "a39", "a40", "a41", "a42", "a43", "a44", "a45", "a46", "a47", "a48", "a49", "a50", "a51", "a52", "a53", "a54", "a55", "a56", "a57", "a58", "a59", "a60", "a61", "a62", "a63", "a64", "a65", "a66", "a67", "a68", "a69", "a70", "a71", "a72", "a73", "a74", "a75", "a76", "a77", "a78", "a79", "a80", "a81", "a82", "a83", "a84", "a85", "a86", "a87", "a88", "a89", "a90", "a91", "a92", "a93", "a94", "a95", "a96", "a97", "a98", "a99", "a100", "a101", "a102", "a103", "a104", "a105", "a106", "a107", "a108", "a109", "a110", "a111", "a112", "a113", "a114", "a115", "a116", "a117", "a118", "a119", "a120", "a121", "a122", "a123", "a124", "a125", "a126", "a127", "a128", "a129", "a130", "a131", "a132", "a133", "a134", "a135", "a136", "a137", "a138", "a139", "a140", "a141", "a142", "a143", "a144", "a145", "a146", "a147", "a148", "a149", "a150", "a151", "a152", "a153", "a154", "a155", "a156", "a157", "a158", "a159", "a160", "a161", "a162", "a163", "a164", "a165", "a166", "a167", "a168", "a169", "a170", "a171", "a172", "a173", "a174", "a175", "a176", "a177", "a178", "a179", "a180", "a181", "a182", "a183", "a184", "a185", "a186", "a187", "a188", "a189", "a190", "a191", "a192", "a193", "a194", "a195", "a196", "a197", "a198", "a199", "a200", "a201", "a202", "a203", "a204", "a205", "a206", "a207", "a208", "a209", "a210", "a211", "a212", "a213", "a214", "a215", "a216", "a217", "a218", "a219", "a220", "a221", "a222", "a223", "a224", "a225", "a226", "a227", "a228", "a229", "a230", "a231", "a232", "a233", "a234", "a235", "a236", "a237", "a238", "a239", "a240", "a241", "a242", "a243", "a244", "a245", "a246", "a247", "a248", "a249", "a250", "a251", "a252", "a253", "a254", "a255", "s4", "s5", "s6", "s7", "s8", "s9", "s10", "s11", "s12", "s13", "s14", "s15", "s16", "s17", "s18", "s19", "s20", "s21", "s22", "s23", "s24", "s25", "s26", "s27", "s28", "s29", "s30", "s31", "s33", "s36", "s37", "s38", "s39", "s40", "s41", "s42", "s43", "s44", "s45", "s46", "s47", "s48", "s49", "s50", "s51", "s52", "s53", "s54", "s55", "s56", "s57", "s58", "s59", "s60", "s61", "s62", "s63", "s64", "s65", "s66", "s67", "s68", "s69", "s70", "s71", "s72", "s73", "s74", "s75", "s76", "s77", "s78", "s79", "s80", "s81", "s82", "s83", "s84", "s85", "s86", "s87", "s88", "s89", "s90", "s91", "s92", "s93", "s94", "s95", "s96", "s97", "s98", "s99", "vcc", "scc", "memory");
}
extern "C" void kernel_launch(void* const* d_in, const int* in_sizes, int n_in, void* d_out, int out_size, void* d_ws, size_t ws_size, hipStream_t stream) {
  fused_block<<<256, 256, 0, stream>>>(d_in[0], d_in[1], d_in[2], d_in[3], d_in[4], d_in[6], d_in[7], d_in[8], d_in[9], d_out, d_ws);
}
```
